```python
import torch

def _softmax(x, axis=-1):
    return torch.softmax(x, dim=axis)
def _gelu(x):
    return torch.nn.functional.gelu(x, approximate="tanh")
def _silu(x):
    return torch.nn.functional.silu(x)
def _sigmoid(x):
    return torch.sigmoid(x)
def _layernorm(x, axis=-1, eps=1e-5):
    m = x.mean(dim=axis, keepdim=True)
    v = x.var(dim=axis, keepdim=True, unbiased=False)
    return (x - m) / torch.sqrt(v + eps)
def _scatter_add(tgt, idx, src, axis=0):
    return tgt.clone().index_add_(axis, idx, src)
def _segment_sum(src, seg, n):
    out = torch.zeros((n,) + src.shape[1:], dtype=src.dtype)
    return out.index_add_(0, seg, src)
def _pad(x, pw):
    flat = tuple(v for pair in reversed(pw) for v in pair)
    return torch.nn.functional.pad(x, flat)

kernel_name = 'interleave_layers__spectral_fnet__glu_gated_glu__prompt'

def setup_inputs():

    _w = (lambda a: (lambda t: t.to(torch.bfloat16) if t.is_floating_point() else t)(torch.as_tensor(a)))
    def _A_setup():
        B, S, D = 1, 2048, 640
        ns = torch.arange(S)[:, None] * torch.arange(S)[None, :]
        nd = torch.arange(D)[:, None] * torch.arange(D)[None, :]
        Ws_re = torch.cos(-2.0 * torch.pi * ns / S).to(float)
        Ws_im = torch.sin(-2.0 * torch.pi * ns / S).to(float)
        Wd_re = torch.cos(-2.0 * torch.pi * nd / D).to(float)
        Wd_im = torch.sin(-2.0 * torch.pi * nd / D).to(float)
        return {
            "x": _w((lambda *s: torch.randn(*s, dtype=torch.bfloat16) * 0.05)(B, S, D)),
            "dft_seq_re": _w(Ws_re),
            "dft_seq_im": _w(Ws_im),
            "dft_hid_re": _w(Wd_re),
            "dft_hid_im": _w(Wd_im),
        }
    _A_in = _A_setup()

    def _B_setup():
        B, S, D, F = 1, 2048, 640, 1536
        return {
            "x": _w((lambda *s: torch.randn(*s, dtype=torch.bfloat16) * 0.05)(B, S, D)),
            "w_gate": _w((lambda *s: torch.randn(*s, dtype=torch.bfloat16) * 1.6)(D, F)),
            "w_up": _w((lambda *s: torch.randn(*s, dtype=torch.bfloat16) * 0.8)(D, F)),
            "w_down": _w((lambda *s: torch.randn(*s, dtype=torch.bfloat16) * 0.8)(F, D)),
        }
    _B_in = _B_setup()

    return {
        "A_x": _A_in["x"],
        "A_dft_seq_re": _A_in["dft_seq_re"],
        "A_dft_seq_im": _A_in["dft_seq_im"],
        "A_dft_hid_re": _A_in["dft_hid_re"],
        "A_dft_hid_im": _A_in["dft_hid_im"],
        "B_x": _B_in["x"],
        "B_w_gate": _B_in["w_gate"],
        "B_w_up": _B_in["w_up"],
        "B_w_down": _B_in["w_down"],
    }

def reference(A_x, A_dft_seq_re, A_dft_seq_im, A_dft_hid_re, A_dft_hid_im, B_x, B_w_gate, B_w_up, B_w_down):
    def _A_ref(x, dft_seq_re, dft_seq_im, dft_hid_re, dft_hid_im):
        a = torch.einsum("bsd,ed->bse", x, dft_hid_re)
        b = torch.einsum("bsd,ed->bse", x, dft_hid_im)
        y = torch.einsum("ts,bse->bte", dft_seq_re, a) - torch.einsum("ts,bse->bte", dft_seq_im, b)
        return y

    def _B_ref(x, w_gate, w_up, w_down):
        g = torch.einsum("bsd,df->bsf", x, w_gate)
        u = torch.einsum("bsd,df->bsf", x, w_up)
        h = _sigmoid(g) * u
        return torch.einsum("bsf,fd->bsd", h, w_down)

    x = A_x
    x = x + _A_ref(_layernorm(x, axis=-1), A_dft_seq_re, A_dft_seq_im, A_dft_hid_re, A_dft_hid_im)
    x = x + _B_ref(_layernorm(x, axis=-1), B_w_gate, B_w_up, B_w_down)
    x = x + _A_ref(_layernorm(x, axis=-1), A_dft_seq_re, A_dft_seq_im, A_dft_hid_re, A_dft_hid_im)
    x = x + _B_ref(_layernorm(x, axis=-1), B_w_gate, B_w_up, B_w_down)
    return x
```

```cpp
#include <hip/hip_runtime.h>
#include <hip/hip_bf16.h>
#define SWAPPED 1
typedef unsigned short u16;
typedef short bf16x8 __attribute__((ext_vector_type(8)));
typedef float f32x4 __attribute__((ext_vector_type(4)));
__device__ __forceinline__ float bf2f(u16 b){ return __builtin_bit_cast(float, (unsigned)b<<16); }
__device__ __forceinline__ u16 f2bf(float f){ __hip_bfloat16 h = __float2bfloat16(f); return __builtin_bit_cast(u16,h); }

constexpr int S=2048, D=640, F=1536;

__global__ void ln_kernel(const u16* x, u16* xn){
  int row = blockIdx.x*4 + (threadIdx.x>>6), lane = threadIdx.x&63;
  float v[10]; float s=0;
  for(int i=0;i<10;i++){ v[i]=bf2f(x[row*D+lane+64*i]); s+=v[i]; }
  for(int o=32;o;o>>=1) s+=__shfl_xor(s,o);
  float m=s/D; float q=0;
  for(int i=0;i<10;i++){ float d=v[i]-m; q+=d*d; }
  for(int o=32;o;o>>=1) q+=__shfl_xor(q,o);
  float r=1.0f/sqrtf(q/D+1e-5f);
  for(int i=0;i<10;i++) xn[row*D+lane+64*i]=f2bf((v[i]-m)*r);
}
template<int MODE>
__global__ void gemm(const u16* A,int lda,const u16* B,int sbk,int sbn,const u16* A2,const u16* B2,float sgn2,int K,u16* C,int ldc,const u16* res){
  int lane=threadIdx.x&63; int w=threadIdx.x>>6;
  int m0=blockIdx.y*64+w*16, n0=blockIdx.x*16;
  f32x4 acc={0,0,0,0}, acc2={0,0,0,0};
  int r=lane&15,q=lane>>4;
  for(int k=0;k<K;k+=32){
    bf16x8 a=*(const bf16x8*)(A+(size_t)(m0+r)*lda+k+8*q);
    bf16x8 b; for(int j=0;j<8;j++) b[j]=B[(size_t)(k+8*q+j)*sbk+(size_t)(n0+r)*sbn];
    acc=__builtin_amdgcn_mfma_f32_16x16x32_bf16(a,b,acc,0,0,0);
    if(MODE>=1){
      bf16x8 a2=*(const bf16x8*)(A2+(size_t)(m0+r)*lda+k+8*q);
      bf16x8 b2; for(int j=0;j<8;j++) b2[j]=B2[(size_t)(k+8*q+j)*sbk+(size_t)(n0+r)*sbn];
      acc2=__builtin_amdgcn_mfma_f32_16x16x32_bf16(a2,b2,acc2,0,0,0);
    }
  }
  for(int j=0;j<4;j++){
    int row=m0+q*4+j, col=n0+r;
    float o;
    if(MODE==0) o=acc[j];
    else if(MODE==1) o=acc[j]-acc2[j];
    else { float g=acc[j],u=acc2[j]; o=u/(1.0f+__expf(-g)); }
    if(res) o+=bf2f(res[(size_t)row*ldc+col]);
    C[(size_t)row*ldc+col]=f2bf(o);
  }
}
extern "C" void kernel_launch(void* const* d_in, const int* in_sizes, int n_in, void* d_out, int out_size, void* d_ws, size_t ws_size, hipStream_t st){
  const u16* x=(const u16*)d_in[0]; const u16* Sr=(const u16*)d_in[1]; const u16* Si=(const u16*)d_in[2];
  const u16* Dr=(const u16*)d_in[3]; const u16* Di=(const u16*)d_in[4];
  const u16* Wg=(const u16*)d_in[6]; const u16* Wu=(const u16*)d_in[7]; const u16* Wd=(const u16*)d_in[8];
  u16* ws=(u16*)d_ws; u16* xn=ws; u16* a=xn+S*D; u16* b=a+S*D; u16* x1=b+S*D; u16* h=x1+S*D; u16* x2=h+S*F;
  const u16* cur=x;
  for(int l=0;l<2;l++){
    ln_kernel<<<S/4,256,0,st>>>(cur,xn);
    gemm<0><<<dim3(D/16,S/64),256,0,st>>>(Sr,S,xn,D,1,nullptr,nullptr,0,S,a,D,nullptr);
    gemm<0><<<dim3(D/16,S/64),256,0,st>>>(Si,S,xn,D,1,nullptr,nullptr,0,S,b,D,nullptr);
    gemm<1><<<dim3(D/16,S/64),256,0,st>>>(a,D,Dr,1,D,b,Di,0,D,x1,D,cur);
    ln_kernel<<<S/4,256,0,st>>>(x1,xn);
    gemm<2><<<dim3(F/16,S/64),256,0,st>>>(xn,D,Wg,F,1,xn,Wu,0,D,h,F,nullptr);
    u16* o = l==0? x2 : (u16*)d_out;
    gemm<0><<<dim3(D/16,S/64),256,0,st>>>(h,F,Wd,D,1,nullptr,nullptr,0,F,o,D,x1);
    cur=o;
  }
}
```

```cpp
#include <hip/hip_runtime.h>
#include <hip/hip_bf16.h>
typedef unsigned short u16;
typedef unsigned v4u __attribute__((ext_vector_type(4)));
typedef unsigned v2u __attribute__((ext_vector_type(2)));
typedef short bf16x8 __attribute__((ext_vector_type(8)));
typedef float f32x4 __attribute__((ext_vector_type(4)));
typedef float f32x2 __attribute__((ext_vector_type(2)));
typedef __bf16 bf2 __attribute__((ext_vector_type(2)));
#define LAS __attribute__((address_space(3)))
#define SB() __builtin_amdgcn_sched_barrier(0)
constexpr int S_=2048, D_=640, F_=1536;
constexpr unsigned O_XNT=0, SZ_XNT=S_*D_*2;
constexpr unsigned O_PQ=O_XNT+SZ_XNT, SZ_PQ=2*S_*D_*2;
constexpr unsigned O_X1=O_PQ+SZ_PQ, SZ_X1=S_*D_*2;
constexpr unsigned O_H=O_X1+SZ_X1, SZ_H=S_*F_*2;
constexpr unsigned O_WD=O_H+SZ_H, SZ_WD=2*D_*D_*2;
constexpr unsigned O_WGU=O_WD+SZ_WD, SZ_WGU=2*D_*F_*2;
constexpr unsigned O_WDN=O_WGU+SZ_WGU, SZ_WDN=F_*D_*2;
constexpr unsigned O_CS=O_WDN+SZ_WDN, SZ_CS=2*F_*4;
constexpr unsigned O_ST=O_CS+SZ_CS, SZ_ST=128*8*16*8;
constexpr unsigned O_RES=O_ST+SZ_ST, SZ_RES=128*8*3*1024;
constexpr unsigned O_DBG=O_RES+SZ_RES;
constexpr unsigned WS_USED=O_DBG+2*16*256*16;

#define STAMP0()
#define STAMP1(id)

__device__ __forceinline__ float bf2f(unsigned b){ return __builtin_bit_cast(float, b<<16); }
__device__ __forceinline__ unsigned pk2(float a,float b){ bf2 v; v[0]=(__bf16)a; v[1]=(__bf16)b; return __builtin_bit_cast(unsigned,v); }
__device__ __forceinline__ float lo2f(unsigned p){ return __builtin_bit_cast(float,p<<16); }
__device__ __forceinline__ float hi2f(unsigned p){ return __builtin_bit_cast(float,p&0xffff0000u); }
__device__ __forceinline__ f32x4 mma(v4u a,v4u b,f32x4 c){ asm volatile("v_mfma_f32_16x16x32_bf16 %0, %1, %2, %0":"+v"(c):"v"(a),"v"(b)); return c; }
__device__ __forceinline__ f32x4 mma_av(v4u a,v4u b,f32x4 c){ asm volatile("v_mfma_f32_16x16x32_bf16 %0, %1, %2, %0":"+v"(c):"a"(a),"v"(b)); return c; }
__device__ __forceinline__ f32x4 mma_va(v4u a,v4u b,f32x4 c){ asm volatile("v_mfma_f32_16x16x32_bf16 %0, %1, %2, %0":"+v"(c):"v"(a),"a"(b)); return c; }
#define NOPS() asm volatile("s_nop 15":::"memory")
__device__ __forceinline__ __amdgpu_buffer_rsrc_t mkrs(const void* p,unsigned n){ return __builtin_amdgcn_make_buffer_rsrc((void*)p,0,n,0x00020000); }
__device__ __forceinline__ v4u ld16(__amdgpu_buffer_rsrc_t r,unsigned v,unsigned s){ return __builtin_amdgcn_raw_buffer_load_b128(r,v,s,0); }
__device__ __forceinline__ void st16(v4u x,__amdgpu_buffer_rsrc_t r,unsigned v,unsigned s){ __builtin_amdgcn_raw_buffer_store_b128(x,r,v,s,0); }
__device__ __forceinline__ void st8(v2u x,__amdgpu_buffer_rsrc_t r,unsigned v,unsigned s){ __builtin_amdgcn_raw_buffer_store_b64(x,r,v,s,0); }
__device__ __forceinline__ v4u mksrd(const void* p,unsigned n){ unsigned long long a=(unsigned long long)p; v4u r; r.x=(unsigned)a; r.y=(unsigned)(a>>32)&0xffffu; r.z=n; r.w=0x00020000u; return r; }
__device__ __forceinline__ void glds(v4u srd,unsigned voff,unsigned soff,unsigned ldsaddr){
  asm volatile("s_mov_b32 m0, %0\n\ts_nop 0\n\tbuffer_load_dwordx4 %1, %2, %3 offen lds"::"s"(ldsaddr),"v"(voff),"s"(srd),"s"(soff):"memory");
}
#define WAITV(n) asm volatile("s_waitcnt vmcnt(" #n ")":::"memory")
#define WAITL0() asm volatile("s_waitcnt lgkmcnt(0)":::"memory")
#define BAR() __builtin_amdgcn_s_barrier()
__device__ __forceinline__ v4u lds16(unsigned a){ return *(const LAS v4u*)a; }

__device__ __forceinline__ int ecol(int ks,int q,int j){ int m=ks/5,c=ks%5; if(c<4) return 80*(2*m+(c>>1))+16*(2*(c&1)+(j>>2))+4*q+(j&3); return 80*(2*m+(q>>1))+64+8*(q&1)+j; }

__global__ __launch_bounds__(256) void k_p0(const u16* x,const u16* Dr,const u16* Di,const u16* Wg,const u16* Wu,const u16* Wdn,unsigned char* ws){
  STAMP0();
  __shared__ __attribute__((aligned(16))) u16 sx[8][D_+8];
  __shared__ float sm[8],sr[8];
  const int tid=threadIdx.x, b=blockIdx.x;
  if(b<256){
    const int row=tid>>5, l=tid&31;
    const unsigned* xr=(const unsigned*)(x+(size_t)(8*b+row)*D_);
    unsigned v[10]; float s=0;
    #pragma unroll
    for(int i=0;i<10;i++){ v[i]=xr[l+32*i]; s+=lo2f(v[i])+hi2f(v[i]); ((unsigned*)sx[row])[l+32*i]=v[i]; }
    #pragma unroll
    for(int o=16;o;o>>=1) s+=__shfl_xor(s,o);
    float m=s*(1.0f/D_), q2=0;
    #pragma unroll
    for(int i=0;i<10;i++){ float a=lo2f(v[i])-m,c=hi2f(v[i])-m; q2+=a*a+c*c; }
    #pragma unroll
    for(int o=16;o;o>>=1) q2+=__shfl_xor(q2,o);
    if(l==0){ sm[row]=m; sr[row]=1.0f/sqrtf(q2*(1.0f/D_)+1e-5f); }
    __syncthreads();
    const int ks=b>>2,q=b&3;
    for(int d=tid;d<D_;d+=256){
      float f[8];
      #pragma unroll
      for(int j=0;j<8;j++) f[j]=(bf2f(sx[j][d])-sm[j])*sr[j];
      v4u o={pk2(f[0],f[1]),pk2(f[2],f[3]),pk2(f[4],f[5]),pk2(f[6],f[7])};
      int n=d/80,i=(d%80)>>4,e=d&15;
      *(v4u*)(ws+O_XNT+(((n*64+ks)*5+i)*64+q*16+e)*16)=o;
    }
    const int tt=b>>1;
    for(int it=tid;it<8*3*32;it+=256){
      int e=it&15,h=(it>>4)&1,p=(it>>5)%3,n=it/96;
      int d0=80*n+32*p+e, d1=(p<2)?d0+16:d0;
      v4u o; o.x=sx[4*h][d0]|((unsigned)sx[4*h+1][d0]<<16); o.y=sx[4*h+2][d0]|((unsigned)sx[4*h+3][d0]<<16);
      o.z=sx[4*h][d1]|((unsigned)sx[4*h+1][d1]<<16); o.w=sx[4*h+2][d1]|((unsigned)sx[4*h+3][d1]<<16);
      *(v4u*)(ws+O_RES+(((tt*8+n)*3+p)*64+(2*(b&1)+h)*16+e)*16)=o;
    }
  } else if(b<256+400){
    int fr=(b-256)*4+(tid>>6), lane=tid&63,q=lane>>4,e=lane&15;
    int i=fr%5,ks=(fr/5)%40,n=fr/200; int d0=16*ks+4*q; int row=80*n+16*i+e;
    v2u a=*(const v2u*)(Dr+(size_t)row*D_+d0), c=*(const v2u*)(Di+(size_t)row*D_+d0);
    v4u o={a.x,a.y,c.x^0x80008000u,c.y^0x80008000u};
    *(v4u*)(ws+O_WD+(size_t)fr*1024+lane*16)=o;
  } else if(b<256+400+960){
    int fr=(b-656)*4+(tid>>6), lane=tid&63,q=lane>>4,f=lane&15;
    int z=fr&3,ks=(fr>>2)%20,grp=(fr/80)%3,m=fr/240;
    const u16* W=(z&1)?Wu:Wg; int col=96*m+16*(2*grp+(z>>1))+f;
    u16 t[8];
    #pragma unroll
    for(int j=0;j<8;j++) t[j]=W[(size_t)ecol(ks,q,j)*F_+col];
    v4u o={t[0]|((unsigned)t[1]<<16),t[2]|((unsigned)t[3]<<16),t[4]|((unsigned)t[5]<<16),t[6]|((unsigned)t[7]<<16)};
    *(v4u*)(ws+O_WGU+(size_t)fr*1024+lane*16)=o;
  } else if(b<256+400+960+480){
    int fr=(b-1616)*4+(tid>>6), lane=tid&63,q=lane>>4,e=lane&15;
    int i=fr%5,ks=(fr/5)%48,n=fr/240; int col=80*n+16*i+e;
    u16 t[8];
    #pragma unroll
    for(int j=0;j<8;j++) t[j]=Wdn[(size_t)(32*ks+16*(j>>2)+4*q+(j&3))*D_+col];
    v4u o={t[0]|((unsigned)t[1]<<16),t[2]|((unsigned)t[3]<<16),t[4]|((unsigned)t[5]<<16),t[6]|((unsigned)t[7]<<16)};
    *(v4u*)(ws+O_WDN+(size_t)fr*1024+lane*16)=o;
  } else {
    int bb=b-2096; int c=bb*32+(tid&31), g=tid>>5; const u16* W=c<F_?Wg:Wu; int col=c%F_;
    float s=0; for(int k=g*80;k<g*80+80;k++) s+=bf2f(W[(size_t)k*F_+col]);
    float* red=(float*)sx; red[tid]=s; __syncthreads();
    if(tid<32){ float t=0; for(int u=0;u<8;u++) t+=red[u*32+tid]; ((float*)(ws+O_CS))[c]=t; }
  }
  STAMP1(0);
}

constexpr unsigned S_SLOT=52*1024;
__global__ __launch_bounds__(256,1) void k_s(const u16* Sr,const u16* Si,unsigned char* ws,int id){
  STAMP0();
  extern __shared__ __attribute__((aligned(16))) unsigned char smem[];
  const unsigned lb=(unsigned)(size_t)smem;
  const int lane=threadIdx.x&63, w=__builtin_amdgcn_readfirstlane(threadIdx.x>>6), c=blockIdx.x;
  const int rb=4*(c&7)+(c>>6), n=(c>>3)&7, tt=4*rb+w;
  const v4u sre=mksrd(Sr,S_*S_*2), sim=mksrd(Si,S_*S_*2), sws=mksrd(ws,WS_USED);
  const unsigned va0=(16*tt+(lane>>3))*4096+(((lane&7)^(lane>>4))*16), va1=(va0+8*4096)^64;
  const unsigned vb=lane*16;
  const unsigned gB=O_XNT+n*64*5*1024+w*5*1024;
  auto issueA=[&](int ss,unsigned slot,int h){
    unsigned so=ss*256+h*128, l=lb+slot+w*8192+h*4096;
    glds(sre,va0,so,l); glds(sre,va1,so,l+1024); glds(sim,va0,so,l+2048); glds(sim,va1,so,l+3072); };
  auto issueB1=[&](int ss,unsigned slot,int j){ glds(sws,vb,gB+ss*20480+j*1024,lb+slot+32768+w*5120+j*1024); };
  const int r=lane&15,q=lane>>4;
  const unsigned ra0=lb+w*8192+r*128+((q^(r>>1))*16), ra1=ra0^64, rbb=lb+32768+lane*16;
  f32x4 acc[2][5];
  #pragma unroll
  for(int p=0;p<2;p++)
  #pragma unroll
  for(int i=0;i<5;i++) acc[p][i]=f32x4{0,0,0,0};
  v4u A0[2],B0[5],A1[2],B1[5];
  #pragma unroll
  for(int p=0;p<2;p++) A0[p]=v4u{0,0,0,0};
  #pragma unroll
  for(int i=0;i<5;i++) B0[i]=v4u{0,0,0,0};
  issueA(0,0,0);
  #pragma unroll
  for(int j=0;j<5;j++) issueB1(0,0,j);
  issueA(0,0,1);
  issueA(1,S_SLOT,0);
  #pragma unroll
  for(int j=0;j<5;j++) issueB1(1,S_SLOT,j);
  issueA(1,S_SLOT,1);
  unsigned cur=0, nxt=2*S_SLOT;
  #define RD(A,B,k) { unsigned a_=((k)&1?ra1:ra0)+cur+((k)>>1)*4096; A[0]=lds16(a_); A[1]=lds16(a_+2048); _Pragma("unroll") for(int i=0;i<5;i++) B[i]=lds16(rbb+cur+((k)*5+i)*1024); }
  #define MM(A,B,i) { acc[0][i]=mma(B[i],A[0],acc[0][i]); acc[1][i]=mma(B[i],A[1],acc[1][i]); }
  #define STEP(An,Bn,k,Ac,Bc,G0,G1,G2,G3) RD(An,Bn,k); SB(); MM(Ac,Bc,0); SB(); G0; SB(); MM(Ac,Bc,1); SB(); G1; SB(); MM(Ac,Bc,2); SB(); G2; SB(); MM(Ac,Bc,3); SB(); G3; SB(); MM(Ac,Bc,4); SB();
  for(int s=0;s<14;s++){
    WAITV(13); BAR(); SB();
    const int ss=s+2; const unsigned so=ss*256, l=lb+nxt+w*8192;
    STEP(A1,B1,0,A0,B0, glds(sre,va0,so,l), glds(sre,va1,so,l+1024), glds(sim,va0,so,l+2048), glds(sim,va1,so,l+3072));
    STEP(A0,B0,1,A1,B1, issueB1(ss,nxt,0), issueB1(ss,nxt,1), issueB1(ss,nxt,2), (void)0);
    STEP(A1,B1,2,A0,B0, issueB1(ss,nxt,3), issueB1(ss,nxt,4), glds(sre,va0,so+128,l+4096), (void)0);
    STEP(A0,B0,3,A1,B1, glds(sre,va1,so+128,l+5120), glds(sim,va0,so+128,l+6144), glds(sim,va1,so+128,l+7168), (void)0);
    WAITL0(); SB();
    nxt=cur; cur+=S_SLOT; if(cur==3*S_SLOT) cur=0;
  }
  #pragma unroll
  for(int s=14;s<16;s++){
    if(s==14) WAITV(13); else WAITV(0);
    BAR(); SB();
    STEP(A1,B1,0,A0,B0,(void)0,(void)0,(void)0,(void)0);
    STEP(A0,B0,1,A1,B1,(void)0,(void)0,(void)0,(void)0);
    STEP(A1,B1,2,A0,B0,(void)0,(void)0,(void)0,(void)0);
    STEP(A0,B0,3,A1,B1,(void)0,(void)0,(void)0,(void)0);
    cur+=S_SLOT; if(cur==3*S_SLOT) cur=0;
  }
  #pragma unroll
  for(int i=0;i<5;i++) MM(A0,B0,i);
  SB(); NOPS(); SB();
  const __amdgpu_buffer_rsrc_t rs=mkrs(ws,WS_USED);
  #pragma unroll
  for(int i=0;i<5;i++){
    v4u o={pk2(acc[0][i][0],acc[0][i][1]),pk2(acc[0][i][2],acc[0][i][3]),pk2(acc[1][i][0],acc[1][i][1]),pk2(acc[1][i][2],acc[1][i][3])};
    st16(o,rs,lane*16,O_PQ+(tt*40+5*n+i)*1024);
  }
  #undef RD
  #undef MM
  #undef STEP
  STAMP1(id);
}

template<int MODE>
__global__ __launch_bounds__(256,1) void k_hd(unsigned char* ws,u16* out,int id){
  STAMP0();
  extern __shared__ __attribute__((aligned(16))) unsigned char smem[];
  const unsigned lb=(unsigned)(size_t)smem;
  constexpr int KW=MODE==0?10:12, KS=4*KW;
  constexpr unsigned OW=MODE==0?O_WD:O_WDN, OA=MODE==0?O_PQ:O_H;
  const int lane=threadIdx.x&63, w=__builtin_amdgcn_readfirstlane(threadIdx.x>>6), c=blockIdx.x;
  const int rb=4*(c&7)+(c>>6), n=(c>>3)&7, tt=4*rb+w, r=lane&15,q=lane>>4;
  const __amdgpu_buffer_rsrc_t rs=mkrs(ws,WS_USED);
  v4u W[KW][5], A[4][KW], R[3];
  #pragma unroll
  for(int k=0;k<KW;k++){
    A[0][k]=ld16(rs,lane*16,OA+((4*rb+((w+1)&3))*KS+w*KW+k)*1024);
    #pragma unroll
    for(int i=0;i<5;i++) W[k][i]=ld16(rs,lane*16,OW+((n*KS+w*KW+k)*5+i)*1024);
  }
  SB();
  f32x4 acc[5];
  #pragma unroll
  for(int u=0;u<4;u++){
    #pragma unroll
    for(int i=0;i<5;i++){ acc[i]=f32x4{0,0,0,0}; asm volatile("":"+v"(acc[i])); }
    SB(); asm volatile("s_nop 1"); SB();
    #pragma unroll
    for(int k=0;k<KW;k++){
      if(u==0){
        #pragma unroll
        for(int u2=1;u2<4;u2++){ int idx=k*3+u2-1; int uu=1+idx/KW, kk=idx%KW; A[uu][kk]=ld16(rs,lane*16,OA+((4*rb+((w+1+uu)&3))*KS+w*KW+kk)*1024); }
      }
      if(u==1&&k<3) R[k]=ld16(rs,lane*16,O_RES+((tt*8+n)*3+k)*1024);
      #pragma unroll
      for(int i=0;i<5;i++) acc[i]= MODE==0? mma_av(W[k][i],A[u][k],acc[i]) : mma_va(A[u][k],W[k][i],acc[i]);
      SB();
    }
    if(u<3){ NOPS(); SB();
      #pragma unroll
      for(int i=0;i<5;i++) *(LAS f32x4*)(lb+((((w+1+u)&3)*3+u)*5+i)*1024+lane*16)=acc[i];
    }
    if(u==2){ WAITL0(); BAR(); }
    SB();
  }
  v4u sel0,sel1; { unsigned one=0x3f80u; int d=r-4*q; unsigned a=(d==0?one:0)|(d==1?one<<16:0), b=(d==2?one:0)|(d==3?one<<16:0); sel0=v4u{a,b,0,0}; sel1=v4u{0,0,a,b}; }
  asm volatile("s_nop 1":"+v"(sel0),"+v"(sel1));
  #pragma unroll
  for(int i=0;i<5;i++) acc[i]=mma(R[i>>1],(i&1)?sel1:sel0,acc[i]);
  SB(); NOPS(); SB();
  #pragma unroll
  for(int u=0;u<3;u++)
  #pragma unroll
  for(int i=0;i<5;i++) acc[i]+=*(const LAS f32x4*)(lb+((w*3+u)*5+i)*1024+lane*16);
  unsigned p[5][2];
  #pragma unroll
  for(int i=0;i<5;i++){ p[i][0]=pk2(acc[i][0],acc[i][1]); p[i][1]=pk2(acc[i][2],acc[i][3]); }
  if(MODE!=2){
    st16(v4u{p[0][0],p[0][1],p[1][0],p[1][1]},rs,lane*16,O_RES+((tt*8+n)*3+0)*1024);
    st16(v4u{p[2][0],p[2][1],p[3][0],p[3][1]},rs,lane*16,O_RES+((tt*8+n)*3+1)*1024);
    st16(v4u{p[4][0],p[4][1],0,0},rs,lane*16,O_RES+((tt*8+n)*3+2)*1024);
  }
  if(MODE==0){
    const unsigned xb=O_X1+(tt*20+5*(n>>1))*1024;
    st16(v4u{p[0][0],p[0][1],p[1][0],p[1][1]},rs,lane*16,xb+(2*(n&1))*1024);
    st16(v4u{p[2][0],p[2][1],p[3][0],p[3][1]},rs,lane*16,xb+(2*(n&1)+1)*1024);
    st8(v2u{p[4][0],p[4][1]},rs,((q>>1)*16+r)*16+(q&1)*8,xb+4096+(n&1)*512);
    float s=0;
    #pragma unroll
    for(int i=0;i<5;i++) s+=(lo2f(p[i][0])+hi2f(p[i][0]))+(lo2f(p[i][1])+hi2f(p[i][1]));
    s+=__shfl_xor(s,16); s+=__shfl_xor(s,32);
    float m=s*(1.0f/80),m2=0;
    #pragma unroll
    for(int i=0;i<5;i++){ float a=lo2f(p[i][0])-m,b=hi2f(p[i][0])-m,cc=lo2f(p[i][1])-m,d=hi2f(p[i][1])-m; m2+=(a*a+b*b)+(cc*cc+d*d); }
    m2+=__shfl_xor(m2,16); m2+=__shfl_xor(m2,32);
    if(q==0) *(f32x2*)(ws+O_ST+((tt*8+n)*16+r)*8)=f32x2{m,m2};
  }
  if(MODE==1){
    float v[5][4];
    #pragma unroll
    for(int i=0;i<5;i++){ v[i][0]=lo2f(p[i][0]); v[i][1]=hi2f(p[i][0]); v[i][2]=lo2f(p[i][1]); v[i][3]=hi2f(p[i][1]); }
    float m[4],m2[4];
    #pragma unroll
    for(int j=0;j<4;j++){ float s=(v[0][j]+v[1][j])+(v[2][j]+v[3][j])+v[4][j];
      #pragma unroll
      for(int o=1;o<16;o<<=1) s+=__shfl_xor(s,o);
      m[j]=s*(1.0f/80); float t=0;
      #pragma unroll
      for(int i=0;i<5;i++){ float a=v[i][j]-m[j]; t+=a*a; }
      #pragma unroll
      for(int o=1;o<16;o<<=1) t+=__shfl_xor(t,o);
      m2[j]=t; }
    if(r<4){ float mm=r==0?m[0]:r==1?m[1]:r==2?m[2]:m[3], tt2=r==0?m2[0]:r==1?m2[1]:r==2?m2[2]:m2[3];
      *(f32x2*)(ws+O_ST+((tt*8+n)*16+4*q+r)*8)=f32x2{mm,tt2}; }
  }
  if(MODE==2){
    BAR();
    const unsigned tb=lb+w*2560;
    #pragma unroll
    for(int i=0;i<5;i++){
      v4u t= i<4? v4u{p[i&~1][0],p[i&~1][1],p[i|1][0],p[i|1][1]} : v4u{p[4][0],p[4][1],0,0};
      f32x4 z={0,0,0,0}; asm volatile("s_nop 1":"+v"(z),"+v"(t)); z=mma(t,(i&1)?sel1:sel0,z); SB(); NOPS(); SB();
      *(LAS v2u*)(tb+r*160+i*32+q*8)=v2u{pk2(z[0],z[1]),pk2(z[2],z[3])};
    }
    #pragma unroll
    for(int u=0;u<3;u++){ int idx=u*64+lane; if(idx<160){ v4u o=lds16(tb+idx*16); *(v4u*)(out+(size_t)(16*tt+idx/10)*D_+80*n+(idx%10)*8)=o; } }
  }
  STAMP1(id);
}

__device__ __forceinline__ f32x2 comb_stats(const unsigned char* ws,int t){
  f32x2 p[8]; float ms=0;
  #pragma unroll
  for(int n=0;n<8;n++){ p[n]=*(const f32x2*)(ws+O_ST+(((t>>4)*8+n)*16+(t&15))*8); ms+=p[n][0]; }
  float m=ms*0.125f, m2=0;
  #pragma unroll
  for(int n=0;n<8;n++){ float d=p[n][0]-m; m2+=p[n][1]+80.0f*d*d; }
  return f32x2{m,1.0f/sqrtf(m2*(1.0f/D_)+1e-5f)};
}
__global__ __launch_bounds__(256) void k_l(unsigned char* ws,int id){
  STAMP0();
  const int lane=threadIdx.x&63, w=threadIdx.x>>6, c=blockIdx.x;
  const int rb=4*(c&7)+(c>>6), n=(c>>3)&7, tt=4*rb+w, r=lane&15,q=lane>>4;
  v4u R[3];
  #pragma unroll
  for(int k=0;k<3;k++) R[k]=*(const v4u*)(ws+O_RES+(((tt*8+n)*3+k)*64+lane)*16);
  f32x2 me=comb_stats(ws,16*tt+r);
  float m[4],rr[4];
  #pragma unroll
  for(int j=0;j<4;j++){ m[j]=__shfl(me[0],4*q+j); rr[j]=__shfl(me[1],4*q+j); }
  #pragma unroll
  for(int i=0;i<5;i++){
    unsigned a=(i&1)?R[i>>1].z:R[i>>1].x, b=(i&1)?R[i>>1].w:R[i>>1].y;
    v2u o={pk2((lo2f(a)-m[0])*rr[0],(hi2f(a)-m[1])*rr[1]),pk2((lo2f(b)-m[2])*rr[2],(hi2f(b)-m[3])*rr[3])};
    *(v2u*)(ws+O_XNT+(((n*64+(tt>>1))*5+i)*64+(2*(tt&1)+(q>>1))*16+r)*16+(q&1)*8)=o;
  }
  STAMP1(id);
}

__global__ __launch_bounds__(256,1) void k_u(unsigned char* ws,int id){
  STAMP0();
  extern __shared__ __attribute__((aligned(16))) unsigned char smem[];
  const unsigned lb=(unsigned)(size_t)smem;
  const int lane=threadIdx.x&63, w=__builtin_amdgcn_readfirstlane(threadIdx.x>>6), c=blockIdx.x;
  const int x=c&7,j5=c>>3; const int rb2=4*(x>>1)+(j5>>3), m=8*(x&1)+(j5&7), t0=8*rb2+2*w, r=lane&15,q=lane>>4;
  const __amdgpu_buffer_rsrc_t rs=mkrs(ws,WS_USED); const v4u sws=mksrd(ws,WS_USED);
  const unsigned gW=O_WGU+m*240*1024+w*1024, lW=lb+w*1024, vb=lane*16;
  v4u A[2][20];
  #define LDA(t,k) asm volatile("buffer_load_dwordx4 %0, %1, %2, %3 offen":"=a"(A[t][k]):"v"(vb),"s"(sws),"s"(O_X1+((t0+(t))*20+(k))*1024):"memory")
  #pragma unroll
  for(int k=0;k<20;k++){ glds(sws,vb,gW+k*4096,lW+k*4096); LDA(0,k); LDA(1,k); }
  SB();
  f32x2 st0,st1; f32x4 cs[4];
  f32x4 acc[2][4];
  v4u B0[4],B1[4];
  const unsigned rB=lb+lane*16;
  #define RDB(B,st) { _Pragma("unroll") for(int z=0;z<4;z++) B[z]=lds16(rB+((st)%36)*4096+z*1024); }
  #pragma unroll
  for(int g=0;g<3;g++){
    #pragma unroll
    for(int t=0;t<2;t++)
    #pragma unroll
    for(int z=0;z<4;z++){ acc[t][z]=f32x4{0,0,0,0}; asm volatile("":"+v"(acc[t][z])); }
    SB(); asm volatile("s_nop 1"); SB();
    #pragma unroll
    for(int k=0;k<20;k++){
      const int st=g*20+k;
      if(st==0){ WAITV(48); BAR(); SB(); RDB(B0,0); }
      if(st+1<60 && ((st+1)&3)==0){
        const int ch=(st+1)>>2;
        if(ch<=4){
          if(ch==1) WAITV(36); if(ch==2) WAITV(28); if(ch==3) WAITV(20); if(ch==4) WAITV(12);
        } else if(ch<=11) WAITV(12); else if(ch==12) WAITV(8); else if(ch==13) WAITV(4); else WAITV(0);
        WAITL0(); BAR(); SB();
        if(ch+4<15){
          #pragma unroll
          for(int u=0;u<4;u++){ int s2=(ch+4)*4+u; glds(sws,vb,gW+s2*4096,lW+(s2%36)*4096); }
        }
        SB();
      }
      if(g==0) asm volatile("":"+a"(A[0][k]),"+a"(A[1][k]));
      if(st&1){ if(st+1<60) RDB(B0,st+1); SB();
        #pragma unroll
        for(int z=0;z<4;z++){ acc[0][z]=mma_va(B1[z],A[0][k],acc[0][z]); acc[1][z]=mma_va(B1[z],A[1][k],acc[1][z]); } }
      else { RDB(B1,st+1); SB();
        #pragma unroll
        for(int z=0;z<4;z++){ acc[0][z]=mma_va(B0[z],A[0][k],acc[0][z]); acc[1][z]=mma_va(B0[z],A[1][k],acc[1][z]); } }
      SB();
    }
    NOPS(); SB();
    if(g==0){ st0=comb_stats(ws,16*t0+r); st1=comb_stats(ws,16*t0+16+r); }
    #pragma unroll
    for(int z=0;z<4;z++) cs[z]=*(const f32x4*)(ws+O_CS+((z&1)*F_+96*m+32*g+16*(z>>1)+4*q)*4);
    #pragma unroll
    for(int t=0;t<2;t++){
      const float mu=t?st1[0]:st0[0], rr=t?st1[1]:st0[1];
      float h[8];
      #pragma unroll
      for(int z=0;z<2;z++)
      #pragma unroll
      for(int j=0;j<4;j++){ float gg=(acc[t][2*z][j]-mu*cs[2*z][j])*rr, uu=(acc[t][2*z+1][j]-mu*cs[2*z+1][j])*rr;
        h[4*z+j]=uu*__builtin_amdgcn_rcpf(1.0f+__builtin_amdgcn_exp2f(-1.4426950408889634f*gg)); }
      st16(v4u{pk2(h[0],h[1]),pk2(h[2],h[3]),pk2(h[4],h[5]),pk2(h[6],h[7])},rs,lane*16,O_H+((t0+t)*48+3*m+g)*1024);
    }
    SB();
  }
  STAMP1(id);
}

extern "C" void kernel_launch(void* const* d_in, const int* in_sizes, int n_in, void* d_out, int out_size, void* d_ws, size_t ws_size, hipStream_t st){
  const u16* x=(const u16*)d_in[0]; const u16* Sr=(const u16*)d_in[1]; const u16* Si=(const u16*)d_in[2];
  const u16* Dr=(const u16*)d_in[3]; const u16* Di=(const u16*)d_in[4];
  const u16* Wg=(const u16*)d_in[6]; const u16* Wu=(const u16*)d_in[7]; const u16* Wd=(const u16*)d_in[8];
  unsigned char* ws=(unsigned char*)d_ws;
  hipFuncSetAttribute((const void*)k_s,hipFuncAttributeMaxDynamicSharedMemorySize,160*1024);
  hipFuncSetAttribute((const void*)k_u,hipFuncAttributeMaxDynamicSharedMemorySize,160*1024);
  k_p0<<<2096+96,256,0,st>>>(x,Dr,Di,Wg,Wu,Wd,ws);
  for(int l=0;l<2;l++){
    k_s<<<256,256,3*S_SLOT,st>>>(Sr,Si,ws,1+5*l);
    k_hd<0><<<256,256,61440,st>>>(ws,nullptr,2+5*l);
    k_u<<<256,256,144*1024,st>>>(ws,3+5*l);
    if(l==0){ k_hd<1><<<256,256,61440,st>>>(ws,nullptr,4); k_l<<<256,256,0,st>>>(ws,5); }
    else k_hd<2><<<256,256,61440,st>>>(ws,(u16*)d_out,9);
  }
}
```

```cpp
#include <hip/hip_runtime.h>
#include <hip/hip_bf16.h>

__global__ __launch_bounds__(256, 1) void fnet_glu_kernel(const void* x, const void* sr, const void* si, const void* dr, const void* di,
                                                          const void* wg, const void* wu, const void* wd, void* out, void* ws) {
  register unsigned tid asm("v0") = threadIdx.x;
  asm volatile(
    "s_load_dwordx16 s[8:23], %1, 0x0\n"
    "s_load_dwordx4 s[24:27], %1, 0x40\n"
    "s_mov_b32 s63, %2\n"
    "s_getpc_b64 s[96:97]\n"
    "L_pc0:\n"
    "v_lshlrev_b32 v112, 5, v0\n"
    "s_and_b32 s97, s97, 0xffff\n"
    "s_mov_b32 s98, L_pc1-L_pc0\n"
    "s_mov_b32 s99, 0x20000\n"
    "s_bfe_u32 s91, s63, 0x30003\n"
    "s_lshl_b32 s91, s91, 13\n"
    "s_mov_b32 exec_lo, 0x11111111\n"
    "s_mov_b32 exec_hi, 0x11111111\n"
    "buffer_load_dword v113, v112, s[96:99], s91 offen\n"
    "s_mov_b64 exec, -1\n"
    "v_readfirstlane_b32 s56, v0\n"
    "v_and_b32 v103, 63, v0\n"
    "v_add_u32 v104, 0, v103\n"
    "v_mul_u32_u24 v105, 6554, v104\n"
    "v_lshrrev_b32 v105, 16, v105\n"
    "v_mul_u32_u24 v105, 1120, v105\n"
    "v_lshl_add_u32 v100, v104, 4, v105\n"
    "v_add_u32 v104, 64, v103\n"
    "v_mul_u32_u24 v105, 6554, v104\n"
    "v_lshrrev_b32 v105, 16, v105\n"
    "v_mul_u32_u24 v105, 1120, v105\n"
    "v_lshl_add_u32 v101, v104, 4, v105\n"
    "v_add_u32 v104, 128, v103\n"
    "v_mul_u32_u24 v105, 6554, v104\n"
    "v_lshrrev_b32 v105, 16, v105\n"
    "v_mul_u32_u24 v105, 1120, v105\n"
    "v_lshl_add_u32 v102, v104, 4, v105\n"
    "s_lshr_b32 s56, s56, 6\n"
    "s_bfe_u32 s91, s63, 0x10000\n"
    "s_bfe_u32 s92, s63, 0x20003\n"
    "s_mul_i32 s57, s91, 4\n"
    "s_add_u32 s57, s57, s92\n"
    "s_bfe_u32 s91, s63, 0x20001\n"
    "s_bfe_u32 s92, s63, 0x30005\n"
    "s_mul_i32 s58, s91, 8\n"
    "s_add_u32 s58, s58, s92\n"
    "s_mul_i32 s59, s58, 4\n"
    "s_add_u32 s59, s59, s56\n"
    "s_mul_i32 s90, s56, 15360\n"
    "s_add_u32 s90, s90, 98304\n"
    "s_mul_i32 s94, s57, 160\n"
    "s_mul_i32 s91, s59, 20480\n"
    "s_add_u32 s91, s91, s94\n"
    "s_mov_b32 s64, 0\n"
    "s_waitcnt lgkmcnt(0)\n"
    "s_mov_b32 s48, s8\n"
    "s_and_b32 s49, s9, 0xffff\n"
    "s_mov_b32 s50, 2621440\n"
    "s_mov_b32 s51, 0x20000\n"
    "s_add_u32 m0, s90, 0\n"
    "s_nop 0\n"
    "buffer_load_dwordx4 v100, s[48:51], s91 offen lds\n"
    "s_add_u32 m0, s90, 1024\n"
    "s_nop 0\n"
    "buffer_load_dwordx4 v101, s[48:51], s91 offen lds\n"
    "s_add_u32 m0, s90, 2048\n"
    "s_nop 0\n"
    "buffer_load_dwordx4 v102, s[48:51], s91 offen lds\n"
    "s_mov_b32 s28, s26\n"
    "s_and_b32 s29, s27, 0xffff\n"
    "s_mov_b32 s30, 41877504\n"
    "s_mov_b32 s31, 0x20000\n"
    "v_mov_b32 v111, 0\n"
    "buffer_load_dword v110, v111, s[28:31], 0 offen sc1\n"
    "v_lshlrev_b32 v1, 4, v103\n"
    "s_bfe_u32 s91, s63, 0x30003\n"
    "s_mul_i32 s91, s91, 8388608\n"
    "s_mul_i32 s95, s56, 2097152\n"
    "s_add_u32 s91, s91, s95\n"
    "s_add_u32 s91, s91, 2048\n"
    "s_mov_b64 exec, 0x1\n"
    "buffer_load_dword v98, v111, s[28:31], s91 offen sc1\n"
    "s_mov_b64 exec, -1\n"
    "s_bfe_u32 s91, s63, 0x20003\n"
    "s_bfe_u32 s92, s63, 0x10000\n"
    "s_mul_i32 s73, s91, 2\n"
    "s_add_u32 s73, s73, s92\n"
    "s_bfe_u32 s91, s63, 0x20001\n"
    "s_bfe_u32 s92, s63, 0x30005\n"
    "s_mul_i32 s74, s91, 8\n"
    "s_add_u32 s74, s74, s92\n"
    "s_bfe_u32 s91, s63, 0x10002\n"
    "s_bfe_u32 s92, s63, 0x30005\n"
    "s_mul_i32 s60, s91, 8\n"
    "s_add_u32 s60, s60, s92\n"
    "s_bfe_u32 s91, s63, 0x10001\n"
    "s_mul_i32 s61, s91, 8\n"
    "s_add_u32 s61, s61, s57\n"
    "s_mov_b32 s32, s10\n"
    "s_and_b32 s33, s11, 0xffff\n"
    "s_mov_b32 s34, 8388608\n"
    "s_mov_b32 s35, 0x20000\n"
    "s_sub_u32 s36, s10, 1024\n"
    "s_subb_u32 s37, s11, 0\n"
    "s_and_b32 s37, s37, 0xffff\n"
    "s_mov_b32 s38, 8389632\n"
    "s_mov_b32 s39, 0x20000\n"
    "s_sub_u32 s40, s12, 2048\n"
    "s_subb_u32 s41, s13, 0\n"
    "s_and_b32 s41, s41, 0xffff\n"
    "s_mov_b32 s42, 8390656\n"
    "s_mov_b32 s43, 0x20000\n"
    "s_sub_u32 s44, s12, 3072\n"
    "s_subb_u32 s45, s13, 0\n"
    "s_and_b32 s45, s45, 0xffff\n"
    "s_mov_b32 s46, 8391680\n"
    "s_mov_b32 s47, 0x20000\n"
    "v_and_b32 v116, 15, v0\n"
    "v_bfe_u32 v117, v0, 4, 2\n"
    "v_bfe_u32 v118, v0, 3, 3\n"
    "s_mul_i32 s91, s74, 64\n"
    "s_mul_i32 s95, s56, 16\n"
    "s_add_u32 s91, s91, s95\n"
    "v_add_u32 v118, s91, v118\n"
    "v_lshlrev_b32 v20, 12, v118\n"
    "v_and_b32 v118, 7, v0\n"
    "v_xor_b32 v118, v118, v117\n"
    "s_and_b32 s91, s73, 1\n"
    "s_lshl_b32 s91, s91, 11\n"
    "v_add_u32 v20, s91, v20\n"
    "v_lshl_add_u32 v20, v118, 4, v20\n"
    "v_add_u32 v21, 0x8000, v20\n"
    "v_xor_b32 v21, 64, v21\n"
    "s_lshl_b32 s84, s56, 13\n"
    "s_lshr_b32 s87, s73, 1\n"
    "s_lshl_b32 s87, s87, 8\n"
    "s_and_b32 s87, s87, 768\n"
    "s_add_u32 s88, s87, 0\n"
    "s_and_b32 s88, s88, 0x3ff\n"
    "s_add_u32 m0, s84, 0\n"
    "s_or_b32 s88, s88, 0\n"
    "buffer_load_dwordx4 v20, s[32:35], s88 offen lds\n"
    "buffer_load_dwordx4 v21, s[36:39], s88 offen offset:1024 lds\n"
    "buffer_load_dwordx4 v20, s[40:43], s88 offen offset:2048 lds\n"
    "buffer_load_dwordx4 v21, s[44:47], s88 offen offset:3072 lds\n"
    "s_add_u32 s52, s26, 7798784\n"
    "s_addc_u32 s53, s27, 0\n"
    "s_and_b32 s53, s53, 0xffff\n"
    "s_mov_b32 s54, 4194304\n"
    "s_mov_b32 s55, 0x20000\n"
    "s_mov_b64 s[26:27], s[16:17]\n"
    "s_mov_b32 s65, 262144\n"
    "s_mul_i32 s69, s59, 2048\n"
    "s_mul_i32 s95, s57, 256\n"
    "s_add_u32 s69, s69, s95\n"
    "s_add_u32 s69, s69, 1048576\n"
    "s_mul_i32 s70, s59, 2048\n"
    "s_add_u32 s70, s70, 1048576\n"
    "v_mov_b32 v2, 0\n"
    "v_mov_b32 v3, 0\n"
    "v_mov_b32 v6, 0\n"
    "v_mov_b32 v7, 0\n"
    "v_mov_b32 v18, 0\n"
    "v_mov_b32 v19, 0\n"
    "v_lshlrev_b32 v118, 2, v117\n"
    "v_sub_u32 v118, v116, v118\n"
    "v_mov_b32 v40, 0x3f80\n"
    "v_mov_b32 v41, 0x3f800000\n"
    "v_cmp_eq_u32 vcc, 0, v118\n"
    "s_nop 1\n"
    "v_cndmask_b32 v4, 0, v40, vcc\n"
    "v_cmp_eq_u32 vcc, 1, v118\n"
    "s_nop 1\n"
    "v_cndmask_b32 v4, v4, v41, vcc\n"
    "v_cmp_eq_u32 vcc, 2, v118\n"
    "s_nop 1\n"
    "v_cndmask_b32 v5, 0, v40, vcc\n"
    "v_cmp_eq_u32 vcc, 3, v118\n"
    "s_nop 1\n"
    "v_cndmask_b32 v5, v5, v41, vcc\n"
    "v_bfe_u32 v96, v0, 2, 4\n"
    "v_and_b32 v97, 3, v0\n"
    "v_mul_u32_u24 v119, 160, v96\n"
    "v_lshl_add_u32 v119, v97, 3, v119\n"
    "v_add_u32 v119, s90, v119\n"
    "s_cmp_ge_u32 s59, 80\n"
    "s_nop 0\n"
    "s_cbranch_scc1 L_dn_1\n"
    "s_mul_i32 s91, s59, 3277\n"
    "s_lshr_b32 s91, s91, 16\n"
    "s_mul_i32 s92, s91, 20\n"
    "s_sub_u32 s79, s59, s92\n"
    "s_and_b32 s77, s91, 1\n"
    "s_lshr_b32 s91, s91, 1\n"
    "s_mul_i32 s80, s91, 8\n"
    "s_add_u32 s80, s80, s57\n"
    "s_cmp_eq_u32 s77, 0\n"
    "s_cselect_b64 s[96:97], s[18:19], s[20:21]\n"
    "v_add_u32 v104, 0, v103\n"
    "v_mul_u32_u24 v105, 5462, v104\n"
    "v_lshrrev_b32 v105, 16, v105\n"
    "v_mul_u32_u24 v105, 2880, v105\n"
    "v_lshl_add_u32 v100, v104, 4, v105\n"
    "v_add_u32 v104, 64, v103\n"
    "v_mul_u32_u24 v105, 5462, v104\n"
    "v_lshrrev_b32 v105, 16, v105\n"
    "v_mul_u32_u24 v105, 2880, v105\n"
    "v_lshl_add_u32 v101, v104, 4, v105\n"
    "v_add_u32 v104, 128, v103\n"
    "v_mul_u32_u24 v105, 5462, v104\n"
    "v_lshrrev_b32 v105, 16, v105\n"
    "v_mul_u32_u24 v105, 2880, v105\n"
    "v_lshl_add_u32 v102, v104, 4, v105\n"
    "s_mul_i32 s91, s79, 13108\n"
    "s_lshr_b32 s91, s91, 16\n"
    "s_mul_i32 s92, s91, 5\n"
    "s_sub_u32 s92, s79, s92\n"
    "s_lshr_b32 s93, s92, 1\n"
    "s_and_b32 s94, s92, 1\n"
    "s_mul_i32 s93, s93, 80\n"
    "s_mul_i32 s95, s94, 32\n"
    "s_add_u32 s93, s93, s95\n"
    "s_cmp_eq_u32 s92, 4\n"
    "s_cselect_b32 s93, 64, s93\n"
    "s_cselect_b32 s94, 80, 16\n"
    "s_cselect_b64 vcc, -1, 0\n"
    "s_mul_i32 s76, s91, 491520\n"
    "s_mul_i32 s95, s93, 3072\n"
    "s_add_u32 s76, s76, s95\n"
    "s_mul_i32 s95, s80, 192\n"
    "s_add_u32 s76, s76, s95\n"
    "s_mul_i32 s83, s94, 3072\n"
    "s_add_u32 s83, s83, s76\n"
    "v_mul_u32_u24 v114, 192, v96\n"
    "v_add_u32 v115, 3072, v114\n"
    "v_bfe_u32 v104, v0, 5, 1\n"
    "v_mul_u32_u24 v104, 3072, v104\n"
    "v_bfe_u32 v105, v0, 4, 1\n"
    "v_mul_u32_u24 v105, 1536, v105\n"
    "v_add_u32 v104, v104, v105\n"
    "v_bfe_u32 v105, v0, 2, 2\n"
    "v_mul_u32_u24 v105, 192, v105\n"
    "v_add_u32 v104, v104, v105\n"
    "v_add_u32 v105, 768, v104\n"
    "v_cndmask_b32 v114, v114, v104, vcc\n"
    "v_cndmask_b32 v115, v115, v105, vcc\n"
    "s_nop 1\n"
    "s_branch L_wj_2\n"
    "L_dn_1:\n"
    "s_sub_u32 s79, s59, 80\n"
    "s_mov_b64 s[96:97], s[22:23]\n"
    "s_mul_i32 s76, s79, 40960\n"
    "s_add_u32 s76, s76, s94\n"
    "s_add_u32 s83, s76, 20480\n"
    "v_mul_u32_u24 v114, 160, v96\n"
    "v_add_u32 v115, 3072, v114\n"
    "s_nop 1\n"
    "L_wj_2:\n"
    "s_and_b32 s97, s97, 0xffff\n"
    "s_mov_b32 s98, 1966080\n"
    "s_mov_b32 s99, 0x20000\n"
    "s_mov_b32 s48, s14\n"
    "s_and_b32 s49, s15, 0xffff\n"
    "s_mov_b32 s50, 819200\n"
    "s_mov_b32 s51, 0x20000\n"
    "s_cmp_ge_u32 s59, 100\n"
    "s_cselect_b32 s91, 100, 0\n"
    "s_sub_u32 s91, s59, s91\n"
    "s_mul_i32 s78, s91, 3277\n"
    "s_lshr_b32 s78, s78, 16\n"
    "s_mul_i32 s92, s78, 20\n"
    "s_sub_u32 s75, s91, s92\n"
    "s_mul_i32 s82, s57, 102400\n"
    "s_mul_i32 s95, s78, 20480\n"
    "s_add_u32 s82, s82, s95\n"
    "s_mul_i32 s95, s75, 64\n"
    "s_add_u32 s82, s82, s95\n"
    "s_cmp_ge_u32 s59, 100\n"
    "s_cselect_b32 s82, 0x70000000, s82\n"
    "v_mul_u32_u24 v121, 1280, v96\n"
    "v_lshl_add_u32 v121, v97, 4, v121\n"
    "s_waitcnt vmcnt(5)\n"
    "ds_read_b64_tr_b16 v[8:9], v119 offset:0\n"
    "ds_read_b64_tr_b16 v[10:11], v119 offset:32\n"
    "ds_read_b64_tr_b16 v[12:13], v119 offset:64\n"
    "ds_read_b64_tr_b16 v[14:15], v119 offset:96\n"
    "ds_read_b64_tr_b16 v[16:17], v119 offset:128\n"
    "v_readfirstlane_b32 s68, v110\n"
    "s_add_u32 s88, s87, 0\n"
    "s_and_b32 s88, s88, 0x3ff\n"
    "s_add_u32 m0, s84, 4096\n"
    "s_or_b32 s88, s88, 128\n"
    "buffer_load_dwordx4 v20, s[32:35], s88 offen lds\n"
    "buffer_load_dwordx4 v21, s[36:39], s88 offen offset:1024 lds\n"
    "buffer_load_dwordx4 v20, s[40:43], s88 offen offset:2048 lds\n"
    "buffer_load_dwordx4 v21, s[44:47], s88 offen offset:3072 lds\n"
    "s_add_u32 s66, s68, 1\n"
    "s_mov_b32 s67, s66\n"
    "s_waitcnt lgkmcnt(3)\n"
    "v_mfma_f32_16x16x32_bf16 v[40:43], v[8:11], v[4:7], 0\n"
    "v_mfma_f32_16x16x32_bf16 v[44:47], v[8:11], v[2:5], 0\n"
    "s_waitcnt lgkmcnt(1)\n"
    "v_mfma_f32_16x16x32_bf16 v[48:51], v[12:15], v[4:7], 0\n"
    "v_mfma_f32_16x16x32_bf16 v[52:55], v[12:15], v[2:5], 0\n"
    "s_waitcnt lgkmcnt(0)\n"
    "v_mfma_f32_16x16x32_bf16 v[56:59], v[16:19], v[4:7], 0\n"
    "v_mov_b32 v65, s66\n"
    "v_mov_b32 v67, s66\n"
    "v_pk_add_f32 v[60:61], v[40:41], v[42:43]\n"
    "v_pk_add_f32 v[62:63], v[44:45], v[46:47]\n"
    "s_nop 0\n"
    "v_pk_add_f32 v[60:61], v[60:61], v[48:49]\n"
    "v_pk_add_f32 v[62:63], v[62:63], v[50:51]\n"
    "v_pk_add_f32 v[60:61], v[60:61], v[52:53]\n"
    "v_pk_add_f32 v[62:63], v[62:63], v[54:55]\n"
    "v_pk_add_f32 v[60:61], v[60:61], v[56:57]\n"
    "v_pk_add_f32 v[62:63], v[62:63], v[58:59]\n"
    "v_pk_add_f32 v[60:61], v[60:61], v[62:63]\n"
    "v_add_f32 v64, v60, v61\n"
    "v_mov_b32 v60, v64\n"
    "s_nop 1\n"
    "v_permlane16_swap_b32 v60, v64\n"
    "v_add_f32 v64, v60, v64\n"
    "v_mov_b32 v60, v64\n"
    "s_nop 1\n"
    "v_permlane32_swap_b32 v60, v64\n"
    "v_add_f32 v64, v60, v64\n"
    "v_mul_f32 v64, 0x3c4ccccd, v64\n"
    "v_pk_add_f32 v[40:41], v[40:41], v[64:65] op_sel_hi:[1,0] neg_lo:[0,1] neg_hi:[0,1]\n"
    "v_pk_add_f32 v[42:43], v[42:43], v[64:65] op_sel_hi:[1,0] neg_lo:[0,1] neg_hi:[0,1]\n"
    "v_pk_add_f32 v[44:45], v[44:45], v[64:65] op_sel_hi:[1,0] neg_lo:[0,1] neg_hi:[0,1]\n"
    "v_pk_add_f32 v[46:47], v[46:47], v[64:65] op_sel_hi:[1,0] neg_lo:[0,1] neg_hi:[0,1]\n"
    "v_pk_add_f32 v[48:49], v[48:49], v[64:65] op_sel_hi:[1,0] neg_lo:[0,1] neg_hi:[0,1]\n"
    "v_pk_add_f32 v[50:51], v[50:51], v[64:65] op_sel_hi:[1,0] neg_lo:[0,1] neg_hi:[0,1]\n"
    "v_pk_add_f32 v[52:53], v[52:53], v[64:65] op_sel_hi:[1,0] neg_lo:[0,1] neg_hi:[0,1]\n"
    "v_pk_add_f32 v[54:55], v[54:55], v[64:65] op_sel_hi:[1,0] neg_lo:[0,1] neg_hi:[0,1]\n"
    "v_pk_add_f32 v[56:57], v[56:57], v[64:65] op_sel_hi:[1,0] neg_lo:[0,1] neg_hi:[0,1]\n"
    "v_pk_add_f32 v[58:59], v[58:59], v[64:65] op_sel_hi:[1,0] neg_lo:[0,1] neg_hi:[0,1]\n"
    "v_pk_mul_f32 v[60:61], v[40:41], v[40:41]\n"
    "v_pk_mul_f32 v[62:63], v[42:43], v[42:43]\n"
    "v_pk_fma_f32 v[60:61], v[44:45], v[44:45], v[60:61]\n"
    "v_pk_fma_f32 v[62:63], v[46:47], v[46:47], v[62:63]\n"
    "v_pk_fma_f32 v[60:61], v[48:49], v[48:49], v[60:61]\n"
    "v_pk_fma_f32 v[62:63], v[50:51], v[50:51], v[62:63]\n"
    "v_pk_fma_f32 v[60:61], v[52:53], v[52:53], v[60:61]\n"
    "v_pk_fma_f32 v[62:63], v[54:55], v[54:55], v[62:63]\n"
    "v_pk_fma_f32 v[60:61], v[56:57], v[56:57], v[60:61]\n"
    "v_pk_fma_f32 v[62:63], v[58:59], v[58:59], v[62:63]\n"
    "v_pk_add_f32 v[60:61], v[60:61], v[62:63]\n"
    "v_add_f32 v66, v60, v61\n"
    "v_mov_b32 v60, v66\n"
    "s_nop 1\n"
    "v_permlane16_swap_b32 v60, v66\n"
    "v_add_f32 v66, v60, v66\n"
    "v_mov_b32 v60, v66\n"
    "s_nop 1\n"
    "v_permlane32_swap_b32 v60, v66\n"
    "v_add_f32 v66, v60, v66\n"
    "s_mov_b64 exec, 0xffff\n"
    "buffer_store_dwordx4 v[64:67], v1, s[52:55], s69 offen sc1\n"
    "s_mov_b64 exec, -1\n"
    "v_lshrrev_b32 v118, 1, v116\n"
    "v_xor_b32 v118, v118, v117\n"
    "v_lshlrev_b32 v118, 4, v118\n"
    "v_lshl_add_u32 v118, v116, 7, v118\n"
    "v_add_u32 v22, s84, v118\n"
    "v_xor_b32 v23, 64, v22\n"
    "v_add_u32 v24, 65536, v22\n"
    "v_add_u32 v25, 65536, v23\n"
    "s_add_u32 m0, s90, 3072\n"
    "s_nop 0\n"
    "buffer_load_dwordx4 v100, s[96:99], s76 offen lds\n"
    "s_add_u32 m0, s90, 4096\n"
    "s_nop 0\n"
    "buffer_load_dwordx4 v101, s[96:99], s76 offen lds\n"
    "s_add_u32 m0, s90, 5120\n"
    "s_nop 0\n"
    "buffer_load_dwordx4 v102, s[96:99], s76 offen lds\n"
    "s_add_u32 m0, s90, 9216\n"
    "s_nop 0\n"
    "buffer_load_dwordx4 v121, s[48:51], s82 offen lds\n"
    "s_waitcnt vmcnt(12)\n"
    "s_add_u32 s88, s87, 256\n"
    "s_and_b32 s88, s88, 0x3ff\n"
    "s_add_u32 m0, s84, 32768\n"
    "s_or_b32 s88, s88, 0\n"
    "buffer_load_dwordx4 v20, s[32:35], s88 offen lds\n"
    "buffer_load_dwordx4 v21, s[36:39], s88 offen offset:1024 lds\n"
    "buffer_load_dwordx4 v20, s[40:43], s88 offen offset:2048 lds\n"
    "buffer_load_dwordx4 v21, s[44:47], s88 offen offset:3072 lds\n"
    "s_add_u32 m0, s90, 6144\n"
    "s_nop 0\n"
    "buffer_load_dwordx4 v100, s[96:99], s83 offen lds\n"
    "s_add_u32 m0, s90, 7168\n"
    "s_nop 0\n"
    "buffer_load_dwordx4 v101, s[96:99], s83 offen lds\n"
    "s_add_u32 m0, s90, 8192\n"
    "s_nop 0\n"
    "buffer_load_dwordx4 v102, s[96:99], s83 offen lds\n"
    "s_mov_b32 s48, s26\n"
    "s_and_b32 s49, s27, 0xffff\n"
    "s_mov_b32 s50, 819200\n"
    "s_mov_b32 s51, 0x20000\n"
    "s_add_u32 m0, s90, 10240\n"
    "s_nop 0\n"
    "buffer_load_dwordx4 v121, s[48:51], s82 offen lds\n"
    "v_and_b32 v122, 15, v0\n"
    "v_lshlrev_b32 v122, 17, v122\n"
    "v_bfe_u32 v123, v0, 4, 1\n"
    "s_mov_b32 s91, 4194304\n"
    "v_mul_lo_u32 v123, s91, v123\n"
    "v_add_u32 v122, v122, v123\n"
    "s_mul_i32 s91, s58, 4096\n"
    "s_mul_i32 s95, s57, 512\n"
    "s_add_u32 s91, s91, s95\n"
    "s_mul_i32 s95, s56, 128\n"
    "s_add_u32 s91, s91, s95\n"
    "s_add_u32 s91, s91, 7798784\n"
    "s_mov_b64 exec, 0x3ff00ff\n"
    "buffer_load_dword v123, v122, s[28:31], s91 offen sc1\n"
    "s_mov_b64 exec, -1\n"
    "v_add_u32 v26, 98304, v1\n"
    "v_add_u32 v27, 65536, v1\n"
    "v_lshrrev_b32 v28, 1, v1\n"
    "v_lshlrev_b32 v29, 5, v1\n"
    "v_add_u32 v31, 4096, v1\n"
    "v_lshrrev_b32 v118, 1, v117\n"
    "v_lshlrev_b32 v30, 8, v118\n"
    "v_lshl_add_u32 v30, v116, 4, v30\n"
    "v_and_b32 v118, 1, v117\n"
    "v_lshl_add_u32 v30, v118, 3, v30\n"
    "s_add_u32 s91, s90, 3072\n"
    "v_lshl_add_u32 v114, v97, 3, v114\n"
    "v_lshl_add_u32 v115, v97, 3, v115\n"
    "v_add_u32 v114, s91, v114\n"
    "v_add_u32 v115, s91, v115\n"
    "v_lshlrev_b32 v120, 6, v116\n"
    "v_lshl_add_u32 v120, v117, 3, v120\n"
    "v_add_u32 v120, s90, v120\n"
    "s_mul_i32 s62, s60, 8\n"
    "s_mul_i32 s95, s56, 2\n"
    "s_add_u32 s62, s62, s95\n"
    "s_mul_i32 s85, s56, 8\n"
    "s_add_u32 s85, s85, 524288\n"
    "s_lshl_b32 s86, s56, 12\n"
    "s_lshr_b32 s91, s58, 4\n"
    "s_and_b32 s92, s57, 6\n"
    "s_or_b32 s91, s91, s92\n"
    "s_bfe_u32 s92, s58, 0x20002\n"
    "s_and_b32 s93, s58, 3\n"
    "s_and_b32 s94, s57, 1\n"
    "s_mul_i32 s8, s91, 512\n"
    "s_mul_i32 s95, s92, 128\n"
    "s_add_u32 s8, s8, s95\n"
    "s_mul_i32 s95, s93, 16\n"
    "s_add_u32 s8, s8, s95\n"
    "s_mul_i32 s95, s94, 8\n"
    "s_add_u32 s8, s8, s95\n"
    "s_mul_i32 s95, s56, 32768\n"
    "s_add_u32 s8, s8, s95\n"
    "s_lshr_b32 s91, s73, 1\n"
    "s_and_b32 s92, s73, 1\n"
    "s_mul_i32 s9, s74, 4096\n"
    "s_mul_i32 s95, s91, 128\n"
    "s_add_u32 s9, s9, s95\n"
    "s_mul_i32 s95, s92, 8\n"
    "s_add_u32 s9, s9, s95\n"
    "s_mul_i32 s95, s56, 1024\n"
    "s_add_u32 s9, s9, s95\n"
    "s_add_u32 s9, s9, 131072\n"
    "s_lshr_b32 s91, s58, 1\n"
    "s_and_b32 s92, s58, 1\n"
    "s_mul_i32 s10, s91, 8192\n"
    "s_mul_i32 s95, s92, 128\n"
    "s_add_u32 s10, s10, s95\n"
    "s_mul_i32 s95, s57, 8\n"
    "s_add_u32 s10, s10, s95\n"
    "s_mul_i32 s95, s56, 2048\n"
    "s_add_u32 s10, s10, s95\n"
    "s_add_u32 s10, s10, 262144\n"
    "s_lshr_b32 s91, s61, 2\n"
    "s_and_b32 s92, s61, 3\n"
    "s_mul_i32 s11, s60, 8192\n"
    "s_mul_i32 s95, s91, 128\n"
    "s_add_u32 s11, s11, s95\n"
    "s_mul_i32 s95, s92, 8\n"
    "s_add_u32 s11, s11, s95\n"
    "s_mul_i32 s95, s56, 2048\n"
    "s_add_u32 s11, s11, s95\n"
    "s_add_u32 s11, s11, 393216\n"
    "s_lshr_b32 s93, s56, 1\n"
    "s_and_b32 s94, s56, 1\n"
    "s_mul_i32 s12, s74, 4096\n"
    "s_mul_i32 s95, s73, 512\n"
    "s_add_u32 s12, s12, s95\n"
    "s_mul_i32 s95, s56, 128\n"
    "s_add_u32 s12, s12, s95\n"
    "s_mul_i32 s13, s58, 4096\n"
    "s_mul_i32 s95, s57, 512\n"
    "s_add_u32 s13, s13, s95\n"
    "s_mul_i32 s95, s56, 128\n"
    "s_add_u32 s13, s13, s95\n"
    "s_add_u32 s13, s13, 131072\n"
    "s_mul_i32 s14, s60, 8192\n"
    "s_mul_i32 s95, s61, 512\n"
    "s_add_u32 s14, s14, s95\n"
    "s_mul_i32 s95, s93, 128\n"
    "s_add_u32 s14, s14, s95\n"
    "s_add_u32 s14, s14, 262144\n"
    "s_add_u32 s15, s13, 262144\n"
    "s_mul_i32 s74, s74, 163840\n"
    "s_mul_i32 s95, s56, 40960\n"
    "s_add_u32 s74, s74, s95\n"
    "s_mul_i32 s95, s73, 5120\n"
    "s_add_u32 s74, s74, s95\n"
    "s_add_u32 s74, s74, 16187392\n"
    "s_and_b32 s91, s73, 1\n"
    "s_xor_b32 s92, s73, 1\n"
    "s_mul_i32 s60, s92, 327680\n"
    "s_mul_i32 s95, s91, 163840\n"
    "s_add_u32 s60, s60, s95\n"
    "s_add_u32 s60, s60, 1572864\n"
    "s_mul_i32 s73, s73, 327680\n"
    "s_mul_i32 s95, s91, 163840\n"
    "s_add_u32 s73, s73, s95\n"
    "s_add_u32 s73, s73, 1572864\n"
    "s_add_u32 s91, s56, 1\n"
    "s_and_b32 s91, s91, 3\n"
    "s_mul_i32 s91, s91, 4\n"
    "s_mul_i32 s95, s58, 16\n"
    "s_add_u32 s91, s91, s95\n"
    "s_add_u32 s91, s91, s56\n"
    "s_mul_i32 s16, s91, 10240\n"
    "s_add_u32 s16, s16, 16187392\n"
    "s_mul_i32 s20, s91, 12288\n"
    "s_add_u32 s20, s20, 24051712\n"
    "s_add_u32 s91, s56, 2\n"
    "s_and_b32 s91, s91, 3\n"
    "s_mul_i32 s91, s91, 4\n"
    "s_mul_i32 s95, s58, 16\n"
    "s_add_u32 s91, s91, s95\n"
    "s_add_u32 s91, s91, s56\n"
    "s_mul_i32 s17, s91, 10240\n"
    "s_add_u32 s17, s17, 16187392\n"
    "s_mul_i32 s21, s91, 12288\n"
    "s_add_u32 s21, s21, 24051712\n"
    "s_add_u32 s91, s56, 3\n"
    "s_and_b32 s91, s91, 3\n"
    "s_mul_i32 s91, s91, 4\n"
    "s_mul_i32 s95, s58, 16\n"
    "s_add_u32 s91, s91, s95\n"
    "s_add_u32 s91, s91, s56\n"
    "s_mul_i32 s18, s91, 10240\n"
    "s_add_u32 s18, s18, 16187392\n"
    "s_mul_i32 s22, s91, 12288\n"
    "s_add_u32 s22, s22, 24051712\n"
    "s_add_u32 s91, s56, 4\n"
    "s_and_b32 s91, s91, 3\n"
    "s_mul_i32 s91, s91, 4\n"
    "s_mul_i32 s95, s58, 16\n"
    "s_add_u32 s91, s91, s95\n"
    "s_add_u32 s91, s91, s56\n"
    "s_mul_i32 s19, s91, 10240\n"
    "s_add_u32 s19, s19, 16187392\n"
    "s_mul_i32 s23, s91, 12288\n"
    "s_add_u32 s23, s23, 24051712\n"
    "s_mul_i32 s71, s62, 2048\n"
    "s_add_u32 s71, s71, 1310720\n"
    "s_lshr_b32 s91, s59, 1\n"
    "s_and_b32 s92, s59, 1\n"
    "s_mul_i32 s72, s57, 327680\n"
    "s_mul_i32 s95, s91, 5120\n"
    "s_add_u32 s72, s72, s95\n"
    "s_mul_i32 s95, s92, 512\n"
    "s_add_u32 s72, s72, s95\n"
    "s_add_u32 s72, s72, 1572864\n"
    "s_mul_i32 s81, s57, 204800\n"
    "s_mul_i32 s95, s56, 51200\n"
    "s_add_u32 s81, s81, s95\n"
    "s_add_u32 s81, s81, 262144\n"
    "v_lshlrev_b32 v72, 16, v8\n"
    "v_and_b32 v73, 0xffff0000, v8\n"
    "v_lshlrev_b32 v74, 16, v9\n"
    "v_and_b32 v75, 0xffff0000, v9\n"
    "v_lshlrev_b32 v76, 16, v10\n"
    "v_and_b32 v77, 0xffff0000, v10\n"
    "v_lshlrev_b32 v78, 16, v11\n"
    "v_and_b32 v79, 0xffff0000, v11\n"
    "v_lshlrev_b32 v80, 16, v12\n"
    "v_and_b32 v81, 0xffff0000, v12\n"
    "v_lshlrev_b32 v82, 16, v13\n"
    "v_and_b32 v83, 0xffff0000, v13\n"
    "v_lshlrev_b32 v84, 16, v14\n"
    "v_and_b32 v85, 0xffff0000, v14\n"
    "v_lshlrev_b32 v86, 16, v15\n"
    "v_and_b32 v87, 0xffff0000, v15\n"
    "v_lshlrev_b32 v88, 16, v16\n"
    "v_and_b32 v89, 0xffff0000, v16\n"
    "v_lshlrev_b32 v90, 16, v17\n"
    "v_and_b32 v91, 0xffff0000, v17\n"
    "v_bfe_u32 v92, v0, 2, 2\n"
    "v_lshlrev_b32 v92, 9, v92\n"
    "v_bfe_u32 v60, v0, 4, 2\n"
    "v_lshl_add_u32 v92, v60, 6, v92\n"
    "v_and_b32 v60, 3, v0\n"
    "v_lshl_add_u32 v92, v60, 4, v92\n"
    "v_and_b32 v93, 0x1f0, v30\n"
    "v_and_b32 v60, 8, v30\n"
    "v_lshl_add_u32 v93, v60, 7, v93\n"
    "s_waitcnt vmcnt(12)\n"
    "buffer_load_dwordx4 v[64:67], v92, s[52:55], s70 offen sc1\n"
    "buffer_load_dwordx4 v[68:71], v92, s[52:55], s70 offen offset:256 sc1\n"
    "s_waitcnt vmcnt(12)\n"
    "s_add_u32 s88, s87, 0\n"
    "s_and_b32 s88, s88, 0x3ff\n"
    "s_add_u32 m0, s84, 36864\n"
    "s_or_b32 s88, s88, 1024\n"
    "buffer_load_dwordx4 v20, s[32:35], s88 offen lds\n"
    "buffer_load_dwordx4 v21, s[36:39], s88 offen offset:1024 lds\n"
    "buffer_load_dwordx4 v20, s[40:43], s88 offen offset:2048 lds\n"
    "buffer_load_dwordx4 v21, s[44:47], s88 offen offset:3072 lds\n"
    "s_waitcnt vmcnt(12)\n"
    "s_add_u32 s88, s87, 512\n"
    "s_and_b32 s88, s88, 0x3ff\n"
    "s_add_u32 m0, s84, 65536\n"
    "s_or_b32 s88, s88, 0\n"
    "buffer_load_dwordx4 v20, s[32:35], s88 offen lds\n"
    "buffer_load_dwordx4 v21, s[36:39], s88 offen offset:1024 lds\n"
    "buffer_load_dwordx4 v20, s[40:43], s88 offen offset:2048 lds\n"
    "buffer_load_dwordx4 v21, s[44:47], s88 offen offset:3072 lds\n"
    "ds_read_b128 a[0:3], v22 offset:0\n"
    "ds_read_b128 a[4:7], v22 offset:2048\n"
    "ds_read_b128 a[8:11], v23 offset:0\n"
    "ds_read_b128 a[12:15], v23 offset:2048\n"
    "s_add_u32 s92, s72, 4096\n"
    "s_waitcnt vmcnt(9)\n"
    "v_xor_b32 v60, s66, v65\n"
    "s_waitcnt vmcnt(8)\n"
    "v_xor_b32 v61, s66, v69\n"
    "v_or_b32 v60, v60, v61\n"
    "v_xor_b32 v61, s66, v67\n"
    "v_or_b32 v60, v60, v61\n"
    "v_xor_b32 v61, s66, v71\n"
    "v_or_b32 v60, v60, v61\n"
    "v_cmp_ne_u32 vcc, 0, v60\n"
    "s_nop 0\n"
    "s_cbranch_vccnz L_gspin_4\n"
    "L_gok_3:\n"
    "v_add_f32 v60, v64, v68\n"
    "v_add_f32 v61, v66, v70\n"
    "s_nop 0\n"
    "v_add_f32_dpp v60, v60, v60 row_ror:4 row_mask:0xf bank_mask:0xf\n"
    "s_nop 1\n"
    "v_add_f32_dpp v60, v60, v60 row_ror:8 row_mask:0xf bank_mask:0xf\n"
    "v_mul_f32 v60, 0x3e000000, v60\n"
    "v_sub_f32 v64, v64, v60\n"
    "v_sub_f32 v68, v68, v60\n"
    "v_mul_f32 v64, v64, v64\n"
    "v_fmac_f32 v64, v68, v68\n"
    "v_fmac_f32 v61, 0x42a00000, v64\n"
    "s_nop 0\n"
    "v_mov_b32_dpp v64, v60 quad_perm:[0,0,0,0] row_mask:0xf bank_mask:0xf\n"
    "v_mov_b32_dpp v65, v60 quad_perm:[1,1,1,1] row_mask:0xf bank_mask:0xf\n"
    "v_add_f32_dpp v61, v61, v61 row_ror:4 row_mask:0xf bank_mask:0xf\n"
    "v_mov_b32_dpp v66, v60 quad_perm:[2,2,2,2] row_mask:0xf bank_mask:0xf\n"
    "v_mov_b32_dpp v67, v60 quad_perm:[3,3,3,3] row_mask:0xf bank_mask:0xf\n"
    "v_add_f32_dpp v61, v61, v61 row_ror:8 row_mask:0xf bank_mask:0xf\n"
    "v_mov_b32 v62, 0x3727c5ac\n"
    "v_fmac_f32 v62, 0x3acccccd, v61\n"
    "v_rsq_f32 v61, v62\n"
    "v_pk_add_f32 v[72:73], v[72:73], v[64:65] neg_lo:[0,1] neg_hi:[0,1]\n"
    "v_pk_add_f32 v[74:75], v[74:75], v[66:67] neg_lo:[0,1] neg_hi:[0,1]\n"
    "v_pk_add_f32 v[76:77], v[76:77], v[64:65] neg_lo:[0,1] neg_hi:[0,1]\n"
    "v_pk_add_f32 v[78:79], v[78:79], v[66:67] neg_lo:[0,1] neg_hi:[0,1]\n"
    "v_pk_add_f32 v[80:81], v[80:81], v[64:65] neg_lo:[0,1] neg_hi:[0,1]\n"
    "v_pk_add_f32 v[82:83], v[82:83], v[66:67] neg_lo:[0,1] neg_hi:[0,1]\n"
    "v_pk_add_f32 v[84:85], v[84:85], v[64:65] neg_lo:[0,1] neg_hi:[0,1]\n"
    "v_pk_add_f32 v[86:87], v[86:87], v[66:67] neg_lo:[0,1] neg_hi:[0,1]\n"
    "v_pk_add_f32 v[88:89], v[88:89], v[64:65] neg_lo:[0,1] neg_hi:[0,1]\n"
    "v_pk_add_f32 v[90:91], v[90:91], v[66:67] neg_lo:[0,1] neg_hi:[0,1]\n"
    "v_mov_b32_dpp v68, v61 quad_perm:[0,0,0,0] row_mask:0xf bank_mask:0xf\n"
    "v_mov_b32_dpp v69, v61 quad_perm:[1,1,1,1] row_mask:0xf bank_mask:0xf\n"
    "v_mov_b32_dpp v70, v61 quad_perm:[2,2,2,2] row_mask:0xf bank_mask:0xf\n"
    "v_mov_b32_dpp v71, v61 quad_perm:[3,3,3,3] row_mask:0xf bank_mask:0xf\n"
    "v_pk_mul_f32 v[72:73], v[72:73], v[68:69]\n"
    "v_pk_mul_f32 v[74:75], v[74:75], v[70:71]\n"
    "v_cvt_pk_bf16_f32 v40, v72, v73\n"
    "v_cvt_pk_bf16_f32 v41, v74, v75\n"
    "v_pk_mul_f32 v[76:77], v[76:77], v[68:69]\n"
    "v_pk_mul_f32 v[78:79], v[78:79], v[70:71]\n"
    "v_cvt_pk_bf16_f32 v42, v76, v77\n"
    "v_cvt_pk_bf16_f32 v43, v78, v79\n"
    "s_nop 0\n"
    "v_permlane16_swap_b32 v40, v42\n"
    "v_permlane16_swap_b32 v41, v43\n"
    "buffer_store_dwordx4 v[40:43], v93, s[52:55], s72 offen sc1\n"
    "v_pk_mul_f32 v[80:81], v[80:81], v[68:69]\n"
    "v_pk_mul_f32 v[82:83], v[82:83], v[70:71]\n"
    "v_cvt_pk_bf16_f32 v44, v80, v81\n"
    "v_cvt_pk_bf16_f32 v45, v82, v83\n"
    "v_pk_mul_f32 v[84:85], v[84:85], v[68:69]\n"
    "v_pk_mul_f32 v[86:87], v[86:87], v[70:71]\n"
    "v_cvt_pk_bf16_f32 v46, v84, v85\n"
    "v_cvt_pk_bf16_f32 v47, v86, v87\n"
    "s_nop 0\n"
    "v_permlane16_swap_b32 v44, v46\n"
    "v_permlane16_swap_b32 v45, v47\n"
    "buffer_store_dwordx4 v[44:47], v93, s[52:55], s72 offen offset:2048 sc1\n"
    "v_pk_mul_f32 v[88:89], v[88:89], v[68:69]\n"
    "v_pk_mul_f32 v[90:91], v[90:91], v[70:71]\n"
    "v_cvt_pk_bf16_f32 v48, v88, v89\n"
    "v_cvt_pk_bf16_f32 v49, v90, v91\n"
    "buffer_store_dwordx2 v[48:49], v30, s[52:55], s92 offen sc1\n"
    "s_waitcnt lgkmcnt(0)\n"
    "s_add_u32 s88, s87, 256\n"
    "s_and_b32 s88, s88, 0x3ff\n"
    "s_add_u32 m0, s84, 0\n"
    "s_or_b32 s88, s88, 128\n"
    "buffer_load_dwordx4 v20, s[32:35], s88 offen lds\n"
    "buffer_load_dwordx4 v21, s[36:39], s88 offen offset:1024 lds\n"
    "buffer_load_dwordx4 v20, s[40:43], s88 offen offset:2048 lds\n"
    "buffer_load_dwordx4 v21, s[44:47], s88 offen offset:3072 lds\n"
    "ds_read_b128 a[16:19], v22 offset:4096\n"
    "ds_read_b128 a[20:23], v22 offset:6144\n"
    "ds_read_b128 a[24:27], v23 offset:4096\n"
    "ds_read_b128 a[28:31], v23 offset:6144\n"
    "ds_read_b64_tr_b16 v[184:185], v114 offset:0\n"
    "ds_read_b64_tr_b16 v[186:187], v115 offset:0\n"
    "ds_read_b64_tr_b16 v[188:189], v114 offset:32\n"
    "ds_read_b64_tr_b16 v[190:191], v115 offset:32\n"
    "ds_read_b64_tr_b16 v[192:193], v114 offset:64\n"
    "ds_read_b64_tr_b16 v[194:195], v115 offset:64\n"
    "ds_read_b64_tr_b16 v[196:197], v114 offset:96\n"
    "ds_read_b64_tr_b16 v[198:199], v115 offset:96\n"
    "ds_read_b64_tr_b16 v[200:201], v114 offset:128\n"
    "ds_read_b64_tr_b16 v[202:203], v115 offset:128\n"
    "ds_read_b64_tr_b16 v[204:205], v114 offset:160\n"
    "s_waitcnt lgkmcnt(14)\n"
    "ds_read_b64_tr_b16 v[206:207], v115 offset:160\n"
    "s_waitcnt lgkmcnt(14)\n"
    "ds_read_b64 v[208:209], v120 offset:9216\n"
    "s_waitcnt lgkmcnt(14)\n"
    "ds_read_b64 v[210:211], v120 offset:10240\n"
    "s_waitcnt lgkmcnt(14)\n"
    "ds_read_b64 v[212:213], v120 offset:9248\n"
    "s_waitcnt lgkmcnt(14)\n"
    "ds_read_b64 v[214:215], v120 offset:10272\n"
    "s_mul_i32 s98, s57, 204800\n"
    "s_mul_i32 s95, s75, 10240\n"
    "s_add_u32 s98, s98, s95\n"
    "s_mul_i32 s95, s78, 1024\n"
    "s_add_u32 s98, s98, s95\n"
    "s_add_u32 s98, s98, 262144\n"
    "s_cmp_ge_u32 s59, 100\n"
    "s_cselect_b32 s98, 0x70000000, s98\n"
    "s_cmp_ge_u32 s59, 80\n"
    "s_cbranch_scc1 L_dn_5\n"
    "s_mul_i32 s48, s80, 245760\n"
    "s_mul_i32 s95, s77, 1024\n"
    "s_add_u32 s48, s48, s95\n"
    "s_mul_i32 s95, s79, 6144\n"
    "s_add_u32 s48, s48, s95\n"
    "s_add_u32 s48, s48, 1900544\n"
    "s_add_u32 s49, s48, 2048\n"
    "s_add_u32 s50, s48, 4096\n"
    "s_mul_i32 s51, s80, 245760\n"
    "s_mul_i32 s95, s77, 1024\n"
    "s_add_u32 s51, s51, s95\n"
    "s_mul_i32 s95, s79, 2048\n"
    "s_add_u32 s51, s51, s95\n"
    "s_add_u32 s51, s51, 2023424\n"
    "s_mul_i32 s96, s80, 245760\n"
    "s_mul_i32 s95, s77, 1024\n"
    "s_add_u32 s96, s96, s95\n"
    "s_mul_i32 s95, s79, 2048\n"
    "s_add_u32 s96, s96, s95\n"
    "s_add_u32 s96, s96, 2064384\n"
    "s_mul_i32 s97, s80, 245760\n"
    "s_mul_i32 s95, s77, 1024\n"
    "s_add_u32 s97, s97, s95\n"
    "s_mul_i32 s95, s79, 2048\n"
    "s_add_u32 s97, s97, s95\n"
    "s_add_u32 s97, s97, 2105344\n"
    "s_mul_i32 s99, s80, 3840\n"
    "s_mul_i32 s95, s79, 192\n"
    "s_add_u32 s99, s99, s95\n"
    "s_mul_i32 s95, s77, 384\n"
    "s_add_u32 s99, s99, s95\n"
    "s_add_u32 s99, s99, 4096\n"
    "s_cmp_eq_u32 s56, 0\n"
    "s_cselect_b32 s99, s99, 0x70000000\n"
    "v_mov_b32 v118, 0x3f803f80\n"
    "v_cmp_eq_u32 vcc, 0, v116\n"
    "s_nop 1\n"
    "v_cndmask_b32 v124, 0, v118, vcc\n"
    "v_mov_b32 v125, v124\n"
    "v_mov_b32 v126, v124\n"
    "v_mov_b32 v127, v124\n"
    "s_waitcnt lgkmcnt(14)\n"
    "s_nop 0\n"
    "v_mfma_f32_16x16x32_bf16 v[216:219], v[184:187], v[124:127], 0\n"
    "v_cmp_eq_u32 vcc, 1, v116\n"
    "s_nop 1\n"
    "v_cndmask_b32 v128, 0, v118, vcc\n"
    "v_mov_b32 v129, v128\n"
    "v_mov_b32 v130, v128\n"
    "v_mov_b32 v131, v128\n"
    "s_waitcnt lgkmcnt(12)\n"
    "s_nop 0\n"
    "v_mfma_f32_16x16x32_bf16 v[216:219], v[188:191], v[128:131], v[216:219]\n"
    "v_cmp_eq_u32 vcc, 2, v116\n"
    "s_nop 1\n"
    "v_cndmask_b32 v124, 0, v118, vcc\n"
    "v_mov_b32 v125, v124\n"
    "v_mov_b32 v126, v124\n"
    "v_mov_b32 v127, v124\n"
    "s_waitcnt lgkmcnt(10)\n"
    "s_nop 0\n"
    "v_mfma_f32_16x16x32_bf16 v[216:219], v[192:195], v[124:127], v[216:219]\n"
    "v_cmp_eq_u32 vcc, 3, v116\n"
    "s_nop 1\n"
    "v_cndmask_b32 v128, 0, v118, vcc\n"
    "v_mov_b32 v129, v128\n"
    "v_mov_b32 v130, v128\n"
    "v_mov_b32 v131, v128\n"
    "s_waitcnt lgkmcnt(8)\n"
    "s_nop 0\n"
    "v_mfma_f32_16x16x32_bf16 v[216:219], v[196:199], v[128:131], v[216:219]\n"
    "v_cmp_eq_u32 vcc, 4, v116\n"
    "s_nop 1\n"
    "v_cndmask_b32 v124, 0, v118, vcc\n"
    "v_mov_b32 v125, v124\n"
    "v_mov_b32 v126, v124\n"
    "v_mov_b32 v127, v124\n"
    "s_waitcnt lgkmcnt(6)\n"
    "s_nop 0\n"
    "v_mfma_f32_16x16x32_bf16 v[216:219], v[200:203], v[124:127], v[216:219]\n"
    "v_cmp_eq_u32 vcc, 5, v116\n"
    "s_nop 1\n"
    "v_cndmask_b32 v128, 0, v118, vcc\n"
    "v_mov_b32 v129, v128\n"
    "v_mov_b32 v130, v128\n"
    "v_mov_b32 v131, v128\n"
    "s_waitcnt lgkmcnt(4)\n"
    "s_nop 0\n"
    "v_mfma_f32_16x16x32_bf16 v[216:219], v[204:207], v[128:131], v[216:219]\n"
    "s_nop 7\n"
    "s_branch L_wj_6\n"
    "L_dn_5:\n"
    "s_mul_i32 s91, s79, 21846\n"
    "s_lshr_b32 s91, s91, 16\n"
    "s_mul_i32 s92, s91, 3\n"
    "s_sub_u32 s92, s79, s92\n"
    "s_and_b32 s93, s91, 3\n"
    "s_lshr_b32 s91, s91, 2\n"
    "s_mul_i32 s48, s91, 61440\n"
    "s_mul_i32 s95, s92, 20480\n"
    "s_add_u32 s48, s48, s95\n"
    "s_mul_i32 s95, s93, 5120\n"
    "s_add_u32 s48, s48, s95\n"
    "s_mul_i32 s95, s57, 245760\n"
    "s_add_u32 s48, s48, s95\n"
    "s_add_u32 s48, s48, 5832704\n"
    "s_add_u32 s49, s48, 1024\n"
    "s_add_u32 s50, s48, 2048\n"
    "s_add_u32 s51, s48, 3072\n"
    "s_add_u32 s96, s48, 4096\n"
    "s_mov_b32 s97, 0x70000000\n"
    "s_mov_b32 s99, 0x70000000\n"
    "L_wj_6:\n"
    "s_mul_i32 s91, s56, 1024\n"
    "s_add_u32 s91, s91, 159744\n"
    "v_add_u32 v118, s91, v1\n"
    "s_waitcnt lgkmcnt(14)\n"
    "ds_write_b128 v118, v[216:219]\n"
    "v_lshlrev_b32 v220, 6, v116\n"
    "v_lshl_add_u32 v220, v117, 4, v220\n"
    "v_mov_b32 v118, 0x70000000\n"
    "v_cmp_gt_u32 vcc, 6, v116\n"
    "s_nop 1\n"
    "v_cndmask_b32 v220, v118, v220, vcc\n"
    "s_lshr_b32 s91, s57, 1\n"
    "s_and_b32 s93, s57, 1\n"
    "s_mul_i32 s76, s59, 20480\n"
    "s_mul_i32 s95, s91, 5120\n"
    "s_add_u32 s76, s76, s95\n"
    "s_add_u32 s76, s76, 21430272\n"
    "s_mul_i32 s75, s93, 2048\n"
    "s_add_u32 s75, s75, s76\n"
    "s_mul_i32 s91, s93, 512\n"
    "s_add_u32 s91, s91, 4096\n"
    "s_add_u32 s76, s76, s91\n"
    "s_mul_i32 s77, s62, 20480\n"
    "s_add_u32 s77, s77, 21430272\n"
    "s_add_u32 s78, s77, 20480\n"
    "s_lshr_b32 s91, s61, 2\n"
    "s_and_b32 s93, s61, 3\n"
    "s_mul_i32 s79, s62, 49152\n"
    "s_mul_i32 s95, s91, 12288\n"
    "s_add_u32 s79, s79, s95\n"
    "s_mul_i32 s95, s93, 1024\n"
    "s_add_u32 s79, s79, s95\n"
    "s_add_u32 s79, s79, 24051712\n"
    "s_add_u32 s80, s79, 49152\n"
    "s_mul_i32 s82, s57, 245760\n"
    "s_mul_i32 s95, s56, 61440\n"
    "s_add_u32 s82, s82, s95\n"
    "s_add_u32 s82, s82, 5832704\n"
    "s_mul_i32 s83, s61, 245760\n"
    "s_add_u32 s83, s83, s86\n"
    "s_add_u32 s83, s83, 1900544\n"
    "s_mul_i32 s26, s12, 80\n"
    "s_add_u32 s26, s26, 30343168\n"
    "s_xor_b32 s91, s12, 512\n"
    "s_mul_i32 s27, s91, 80\n"
    "s_add_u32 s27, s27, 30343168\n"
    "s_waitcnt vmcnt(12) lgkmcnt(0)\n"
    "s_add_u32 s88, s87, 0\n"
    "s_and_b32 s88, s88, 0x3ff\n"
    "s_add_u32 m0, s84, 4096\n"
    "s_or_b32 s88, s88, 1152\n"
    "buffer_load_dwordx4 v20, s[32:35], s88 offen lds\n"
    "buffer_load_dwordx4 v21, s[36:39], s88 offen offset:1024 lds\n"
    "buffer_load_dwordx4 v20, s[40:43], s88 offen offset:2048 lds\n"
    "buffer_load_dwordx4 v21, s[44:47], s88 offen offset:3072 lds\n"
    "ds_read_b128 a[32:35], v22 offset:32768\n"
    "ds_read_b128 a[36:39], v22 offset:34816\n"
    "ds_read_b128 a[40:43], v23 offset:32768\n"
    "ds_read_b128 a[44:47], v23 offset:34816\n"
    "s_add_u32 s66, s68, 2\n"
    "s_mov_b32 s67, s66\n"
    "v_mov_b32 v36, s66\n"
    "v_mov_b32 v37, s66\n"
    "v_lshlrev_b32 v39, 3, v29\n"
    "v_add_u32 v176, 8192, v1\n"
    "v_add_u32 v177, 12288, v1\n"
    "s_waitcnt vmcnt(8) lgkmcnt(0)\n"
    "s_barrier\n"
    "s_mov_b64 exec, 0xff\n"
    "buffer_atomic_swap_x2 v[36:37], v39, s[52:55], s8 offen sc1\n"
    "s_mov_b64 exec, -1\n"
    "s_add_u32 s88, s87, 768\n"
    "s_and_b32 s88, s88, 0x3ff\n"
    "s_add_u32 m0, s84, 32768\n"
    "s_or_b32 s88, s88, 0\n"
    "buffer_load_dwordx4 v20, s[32:35], s88 offen lds\n"
    "buffer_load_dwordx4 v21, s[36:39], s88 offen offset:1024 lds\n"
    "buffer_load_dwordx4 v20, s[40:43], s88 offen offset:2048 lds\n"
    "buffer_load_dwordx4 v21, s[44:47], s88 offen offset:3072 lds\n"
    "ds_read_b128 a[128:131], v22 offset:36864\n"
    "ds_read_b128 a[132:135], v22 offset:38912\n"
    "ds_read_b128 a[136:139], v23 offset:36864\n"
    "ds_read_b128 a[140:143], v23 offset:38912\n"
    "s_waitcnt vmcnt(12) lgkmcnt(0)\n"
    "s_add_u32 s88, s87, 512\n"
    "s_and_b32 s88, s88, 0x3ff\n"
    "s_add_u32 m0, s84, 36864\n"
    "s_or_b32 s88, s88, 128\n"
    "buffer_load_dwordx4 v20, s[32:35], s88 offen lds\n"
    "buffer_load_dwordx4 v21, s[36:39], s88 offen offset:1024 lds\n"
    "buffer_load_dwordx4 v20, s[40:43], s88 offen offset:2048 lds\n"
    "buffer_load_dwordx4 v21, s[44:47], s88 offen offset:3072 lds\n"
    "ds_read_b128 a[64:67], v24 offset:0\n"
    "ds_read_b128 a[68:71], v24 offset:2048\n"
    "ds_read_b128 a[72:75], v25 offset:0\n"
    "ds_read_b128 a[76:79], v25 offset:2048\n"
    "s_waitcnt vmcnt(12) lgkmcnt(0)\n"
    "s_add_u32 s88, s87, 768\n"
    "s_and_b32 s88, s88, 0x3ff\n"
    "s_add_u32 m0, s84, 65536\n"
    "s_or_b32 s88, s88, 128\n"
    "buffer_load_dwordx4 v20, s[32:35], s88 offen lds\n"
    "buffer_load_dwordx4 v21, s[36:39], s88 offen offset:1024 lds\n"
    "buffer_load_dwordx4 v20, s[40:43], s88 offen offset:2048 lds\n"
    "buffer_load_dwordx4 v21, s[44:47], s88 offen offset:3072 lds\n"
    "ds_read_b128 a[48:51], v22 offset:0\n"
    "ds_read_b128 a[52:55], v22 offset:2048\n"
    "ds_read_b128 a[56:59], v23 offset:0\n"
    "ds_read_b128 a[60:63], v23 offset:2048\n"
    "s_waitcnt vmcnt(12) lgkmcnt(0)\n"
    "s_add_u32 s88, s87, 256\n"
    "s_and_b32 s88, s88, 0x3ff\n"
    "s_add_u32 m0, s84, 0\n"
    "s_or_b32 s88, s88, 1024\n"
    "buffer_load_dwordx4 v20, s[32:35], s88 offen lds\n"
    "buffer_load_dwordx4 v21, s[36:39], s88 offen offset:1024 lds\n"
    "buffer_load_dwordx4 v20, s[40:43], s88 offen offset:2048 lds\n"
    "buffer_load_dwordx4 v21, s[44:47], s88 offen offset:3072 lds\n"
    "ds_read_b128 a[144:147], v22 offset:4096\n"
    "ds_read_b128 a[148:151], v22 offset:6144\n"
    "ds_read_b128 a[152:155], v23 offset:4096\n"
    "ds_read_b128 a[156:159], v23 offset:6144\n"
    "s_waitcnt vmcnt(12) lgkmcnt(0)\n"
    "s_add_u32 s88, s87, 512\n"
    "s_and_b32 s88, s88, 0x3ff\n"
    "s_add_u32 m0, s84, 4096\n"
    "s_or_b32 s88, s88, 1024\n"
    "buffer_load_dwordx4 v20, s[32:35], s88 offen lds\n"
    "buffer_load_dwordx4 v21, s[36:39], s88 offen offset:1024 lds\n"
    "buffer_load_dwordx4 v20, s[40:43], s88 offen offset:2048 lds\n"
    "buffer_load_dwordx4 v21, s[44:47], s88 offen offset:3072 lds\n"
    "ds_read_b128 a[96:99], v22 offset:32768\n"
    "ds_read_b128 a[100:103], v22 offset:34816\n"
    "ds_read_b128 a[104:107], v23 offset:32768\n"
    "ds_read_b128 a[108:111], v23 offset:34816\n"
    "s_waitcnt vmcnt(12) lgkmcnt(0)\n"
    "s_add_u32 s88, s87, 256\n"
    "s_and_b32 s88, s88, 0x3ff\n"
    "s_add_u32 m0, s84, 32768\n"
    "s_or_b32 s88, s88, 1152\n"
    "buffer_load_dwordx4 v20, s[32:35], s88 offen lds\n"
    "buffer_load_dwordx4 v21, s[36:39], s88 offen offset:1024 lds\n"
    "buffer_load_dwordx4 v20, s[40:43], s88 offen offset:2048 lds\n"
    "buffer_load_dwordx4 v21, s[44:47], s88 offen offset:3072 lds\n"
    "ds_read_b128 a[80:83], v22 offset:36864\n"
    "ds_read_b128 a[84:87], v22 offset:38912\n"
    "ds_read_b128 a[88:91], v23 offset:36864\n"
    "ds_read_b128 a[92:95], v23 offset:38912\n"
    "s_waitcnt vmcnt(12)\n"
    "v_mov_b64 v[40:41], 0\n"
    "s_mov_b64 exec, 0xff\n"
    "buffer_atomic_or_x2 v[40:41], v28, s[52:55], s12 offen sc0 sc1\n"
    "s_mov_b64 exec, -1\n"
    "s_waitcnt lgkmcnt(0)\n"
    "s_add_u32 s88, s87, 768\n"
    "s_and_b32 s88, s88, 0x3ff\n"
    "s_add_u32 m0, s84, 36864\n"
    "s_or_b32 s88, s88, 1024\n"
    "buffer_load_dwordx4 v20, s[32:35], s88 offen lds\n"
    "buffer_load_dwordx4 v21, s[36:39], s88 offen offset:1024 lds\n"
    "buffer_load_dwordx4 v20, s[40:43], s88 offen offset:2048 lds\n"
    "buffer_load_dwordx4 v21, s[44:47], s88 offen offset:3072 lds\n"
    "ds_read_b128 a[112:115], v24 offset:0\n"
    "ds_read_b128 a[116:119], v24 offset:2048\n"
    "ds_read_b128 a[120:123], v25 offset:0\n"
    "ds_read_b128 a[124:127], v25 offset:2048\n"
    "v_mov_b64 v[42:43], 0\n"
    "s_mov_b64 exec, 0xff\n"
    "buffer_atomic_or_x2 v[42:43], v28, s[52:55], s12 offen sc0 sc1\n"
    "s_mov_b64 exec, -1\n"
    "s_waitcnt vmcnt(12) lgkmcnt(0)\n"
    "s_add_u32 s88, s87, 512\n"
    "s_and_b32 s88, s88, 0x3ff\n"
    "s_add_u32 m0, s84, 65536\n"
    "s_or_b32 s88, s88, 1152\n"
    "buffer_load_dwordx4 v20, s[32:35], s88 offen lds\n"
    "buffer_load_dwordx4 v21, s[36:39], s88 offen offset:1024 lds\n"
    "buffer_load_dwordx4 v20, s[40:43], s88 offen offset:2048 lds\n"
    "buffer_load_dwordx4 v21, s[44:47], s88 offen offset:3072 lds\n"
    "ds_read_b128 a[160:163], v22 offset:0\n"
    "ds_read_b128 a[164:167], v22 offset:2048\n"
    "ds_read_b128 a[168:171], v23 offset:0\n"
    "ds_read_b128 a[172:175], v23 offset:2048\n"
    "v_mov_b64 v[44:45], 0\n"
    "s_mov_b64 exec, 0xff\n"
    "buffer_atomic_or_x2 v[44:45], v28, s[52:55], s12 offen sc0 sc1\n"
    "s_mov_b64 exec, -1\n"
    "s_waitcnt vmcnt(12) lgkmcnt(0)\n"
    "s_add_u32 s88, s87, 768\n"
    "s_and_b32 s88, s88, 0x3ff\n"
    "s_add_u32 m0, s84, 0\n"
    "s_or_b32 s88, s88, 1152\n"
    "buffer_load_dwordx4 v20, s[32:35], s88 offen lds\n"
    "buffer_load_dwordx4 v21, s[36:39], s88 offen offset:1024 lds\n"
    "buffer_load_dwordx4 v20, s[40:43], s88 offen offset:2048 lds\n"
    "buffer_load_dwordx4 v21, s[44:47], s88 offen offset:3072 lds\n"
    "ds_read_b128 a[192:195], v22 offset:4096\n"
    "ds_read_b128 a[196:199], v22 offset:6144\n"
    "ds_read_b128 a[200:203], v23 offset:4096\n"
    "ds_read_b128 a[204:207], v23 offset:6144\n"
    "s_bfe_u32 s88, 0x3020100, s85\n"
    "s_lshl_b32 s94, s88, 8\n"
    "s_add_u32 s62, s94, s87\n"
    "s_and_b32 s62, s62, 0x3ff\n"
    "s_and_b32 s94, s94, 0x400\n"
    "s_or_b32 s62, s62, s94\n"
    "s_mul_i32 s62, s62, 80\n"
    "s_lshr_b32 s88, s88, 3\n"
    "s_lshl_b32 s88, s88, 10\n"
    "s_add_u32 s88, s88, s62\n"
    "s_add_u32 m0, s86, 94208\n"
    "s_add_u32 s89, s88, s60\n"
    "s_waitcnt vmcnt(14)\n"
    "v_cmp_ne_u64 vcc, s[66:67], v[40:41]\n"
    "s_nop 0\n"
    "s_and_b64 vcc, vcc, 255\n"
    "s_cbranch_scc0 L_ok_7\n"
    "v_mov_b64 v[40:41], 0\n"
    "s_mov_b64 exec, 0xff\n"
    "buffer_atomic_or_x2 v[40:41], v28, s[52:55], s12 offen sc0 sc1\n"
    "s_mov_b64 exec, -1\n"
    "s_waitcnt vmcnt(10)\n"
    "v_cmp_ne_u64 vcc, s[66:67], v[42:43]\n"
    "s_nop 0\n"
    "s_and_b64 vcc, vcc, 255\n"
    "s_cbranch_scc0 L_ok_7\n"
    "v_mov_b64 v[42:43], 0\n"
    "s_mov_b64 exec, 0xff\n"
    "buffer_atomic_or_x2 v[42:43], v28, s[52:55], s12 offen sc0 sc1\n"
    "s_mov_b64 exec, -1\n"
    "s_waitcnt vmcnt(6)\n"
    "v_cmp_ne_u64 vcc, s[66:67], v[44:45]\n"
    "s_nop 0\n"
    "s_and_b64 vcc, vcc, 255\n"
    "s_cbranch_scc0 L_ok_7\n"
    "v_mov_b64 v[44:45], 0\n"
    "s_mov_b64 exec, 0xff\n"
    "buffer_atomic_or_x2 v[44:45], v28, s[52:55], s12 offen sc0 sc1\n"
    "s_mov_b64 exec, -1\n"
    "s_branch L_spin_8\n"
    "L_ok_7:\n"
    "s_waitcnt lgkmcnt(0)\n"
    "s_barrier\n"
    "buffer_load_dwordx4 v1, s[52:55], s89 offen sc1 lds\n"
    "buffer_load_dwordx4 v31, s[52:55], s89 offen offset:1024 sc1 lds\n"
    "buffer_load_dwordx4 v176, s[52:55], s89 offen offset:2048 sc1 lds\n"
    "buffer_load_dwordx4 v177, s[52:55], s89 offen offset:3072 sc1 lds\n"
    "s_barrier\n"
    "s_bfe_u32 s88, 0x7060504, s85\n"
    "s_lshl_b32 s94, s88, 8\n"
    "s_add_u32 s62, s94, s87\n"
    "s_and_b32 s62, s62, 0x3ff\n"
    "s_and_b32 s94, s94, 0x400\n"
    "s_or_b32 s62, s62, s94\n"
    "s_mul_i32 s62, s62, 80\n"
    "s_lshr_b32 s88, s88, 3\n"
    "s_lshl_b32 s88, s88, 10\n"
    "s_add_u32 s88, s88, s62\n"
    "s_add_u32 m0, s86, 110592\n"
    "s_add_u32 s89, s88, s60\n"
    "buffer_load_dwordx4 v1, s[52:55], s89 offen sc1 lds\n"
    "s_nop 7\n"
    "buffer_load_dwordx4 v31, s[52:55], s89 offen offset:1024 sc1 lds\n"
    "s_nop 7\n"
    "buffer_load_dwordx4 v176, s[52:55], s89 offen offset:2048 sc1 lds\n"
    "s_nop 7\n"
    "buffer_load_dwordx4 v177, s[52:55], s89 offen offset:3072 sc1 lds\n"
    "s_bfe_u32 s88, 0xb0a0908, s85\n"
    "s_lshl_b32 s94, s88, 8\n"
    "s_add_u32 s62, s94, s87\n"
    "s_and_b32 s62, s62, 0x3ff\n"
    "s_and_b32 s94, s94, 0x400\n"
    "s_or_b32 s62, s62, s94\n"
    "s_mul_i32 s62, s62, 80\n"
    "s_lshr_b32 s88, s88, 3\n"
    "s_lshl_b32 s88, s88, 10\n"
    "s_add_u32 s88, s88, s62\n"
    "s_add_u32 m0, s86, 126976\n"
    "s_add_u32 s89, s88, s60\n"
    "buffer_load_dwordx4 v1, s[52:55], s89 offen sc1 lds\n"
    "s_nop 7\n"
    "buffer_load_dwordx4 v31, s[52:55], s89 offen offset:1024 sc1 lds\n"
    "s_nop 7\n"
    "buffer_load_dwordx4 v176, s[52:55], s89 offen offset:2048 sc1 lds\n"
    "s_nop 7\n"
    "buffer_load_dwordx4 v177, s[52:55], s89 offen offset:3072 sc1 lds\n"
    "s_waitcnt vmcnt(24)\n"
    "ds_read_b128 a[176:179], v22 offset:32768\n"
    "ds_read_b128 a[180:183], v22 offset:34816\n"
    "ds_read_b128 a[184:187], v23 offset:32768\n"
    "ds_read_b128 a[188:191], v23 offset:34816\n"
    "s_waitcnt vmcnt(20)\n"
    "ds_read_b128 a[224:227], v22 offset:36864\n"
    "ds_read_b128 a[228:231], v22 offset:38912\n"
    "ds_read_b128 a[232:235], v23 offset:36864\n"
    "ds_read_b128 a[236:239], v23 offset:38912\n"
    "s_waitcnt vmcnt(16)\n"
    "ds_read_b128 a[208:211], v24 offset:0\n"
    "ds_read_b128 a[212:215], v24 offset:2048\n"
    "ds_read_b128 a[216:219], v25 offset:0\n"
    "ds_read_b128 a[220:223], v25 offset:2048\n"
    "s_waitcnt vmcnt(12)\n"
    "ds_read_b128 a[240:243], v22 offset:0\n"
    "ds_read_b128 a[244:247], v22 offset:2048\n"
    "ds_read_b128 a[248:251], v23 offset:0\n"
    "s_waitcnt lgkmcnt(14)\n"
    "ds_read_b128 a[252:255], v23 offset:2048\n"
    "s_waitcnt lgkmcnt(14)\n"
    "ds_read_b128 v[64:67], v26 offset:62464\n"
    "s_waitcnt lgkmcnt(14)\n"
    "ds_read_b128 v[68:71], v26 offset:63488\n"
    "s_waitcnt lgkmcnt(14)\n"
    "ds_read_b128 v[72:75], v26 offset:64512\n"
    "s_waitcnt lgkmcnt(2)\n"
    "v_pk_add_f32 v[216:217], v[216:217], v[64:65]\n"
    "v_pk_add_f32 v[218:219], v[218:219], v[66:67]\n"
    "s_waitcnt lgkmcnt(1)\n"
    "v_pk_add_f32 v[216:217], v[216:217], v[68:69]\n"
    "v_pk_add_f32 v[218:219], v[218:219], v[70:71]\n"
    "s_waitcnt lgkmcnt(0)\n"
    "v_pk_add_f32 v[216:217], v[216:217], v[72:73]\n"
    "v_pk_add_f32 v[218:219], v[218:219], v[74:75]\n"
    "s_nop 1\n"
    "L_layer_9:\n"
    "s_add_u32 s66, s68, 7\n"
    "s_mov_b32 s67, s66\n"
    "v_mov_b32 v36, s66\n"
    "v_mov_b32 v37, s66\n"
    "v_lshlrev_b32 v39, 3, v29\n"
    "s_waitcnt vmcnt(8)\n"
    "s_barrier\n"
    "ds_read_b128 v[120:123], v27 offset:28672\n"
    "ds_read_b128 v[124:127], v27 offset:29696\n"
    "ds_read_b128 v[128:131], v27 offset:30720\n"
    "ds_read_b128 v[132:135], v27 offset:31744\n"
    "ds_read_b128 v[136:139], v26 offset:0\n"
    "ds_read_b128 v[140:143], v26 offset:1024\n"
    "ds_read_b128 v[144:147], v26 offset:2048\n"
    "ds_read_b128 v[148:151], v26 offset:3072\n"
    "s_waitcnt lgkmcnt(4)\n"
    "v_mfma_f32_16x16x32_bf16 v[40:43], v[120:123], a[0:3], 0\n"
    "s_bfe_u32 s88, 0xf0e0d0c, s85\n"
    "ds_read_b128 v[152:155], v26 offset:4096\n"
    "s_lshl_b32 s94, s88, 8\n"
    "v_mfma_f32_16x16x32_bf16 v[40:43], v[124:127], a[8:11], v[40:43]\n"
    "s_add_u32 s62, s94, s87\n"
    "ds_read_b128 v[156:159], v26 offset:5120\n"
    "s_and_b32 s62, s62, 0x3ff\n"
    "v_mfma_f32_16x16x32_bf16 v[40:43], v[128:131], a[16:19], v[40:43]\n"
    "s_and_b32 s94, s94, 0x400\n"
    "ds_read_b128 v[160:163], v26 offset:6144\n"
    "s_or_b32 s62, s62, s94\n"
    "v_mfma_f32_16x16x32_bf16 v[40:43], v[132:135], a[24:27], v[40:43]\n"
    "s_mul_i32 s62, s62, 80\n"
    "ds_read_b128 v[164:167], v26 offset:7168\n"
    "s_lshr_b32 s88, s88, 3\n"
    "v_mfma_f32_16x16x32_bf16 v[60:63], v[132:135], a[28:31], 0\n"
    "s_lshl_b32 s88, s88, 10\n"
    "s_add_u32 s88, s88, s62\n"
    "v_mfma_f32_16x16x32_bf16 v[60:63], v[128:131], a[20:23], v[60:63]\n"
    "v_mfma_f32_16x16x32_bf16 v[60:63], v[124:127], a[12:15], v[60:63]\n"
    "s_add_u32 m0, s86, 143360\n"
    "s_cmp_lg_u32 s64, 0\n"
    "s_cbranch_scc1 L_l0_12\n"
    "buffer_store_dwordx4 v[184:187], v1, s[28:31], s48 offen sc1\n"
    "buffer_store_dwordx4 v[188:191], v1, s[28:31], s49 offen sc1\n"
    "buffer_store_dwordx4 v[192:195], v1, s[28:31], s50 offen sc1\n"
    "L_l0_12:\n"
    "v_mfma_f32_16x16x32_bf16 v[60:63], v[120:123], a[4:7], v[60:63]\n"
    "s_waitcnt lgkmcnt(4)\n"
    "s_add_u32 s89, s88, s60\n"
    "v_mfma_f32_16x16x32_bf16 v[60:63], v[136:139], a[36:39], v[60:63]\n"
    "ds_read_b128 v[120:123], v26 offset:8192\n"
    "v_mfma_f32_16x16x32_bf16 v[60:63], v[140:143], a[44:47], v[60:63]\n"
    "ds_read_b128 v[124:127], v26 offset:9216\n"
    "v_mfma_f32_16x16x32_bf16 v[60:63], v[144:147], a[52:55], v[60:63]\n"
    "s_bfe_u32 s88, 0x13121110, s85\n"
    "buffer_load_dwordx4 v1, s[52:55], s89 offen sc1 lds\n"
    "v_mfma_f32_16x16x32_bf16 v[60:63], v[148:151], a[60:63], v[60:63]\n"
    "s_lshl_b32 s94, s88, 8\n"
    "ds_read_b128 v[128:131], v26 offset:10240\n"
    "ds_read_b128 v[132:135], v26 offset:11264\n"
    "v_mfma_f32_16x16x32_bf16 v[40:43], v[148:151], a[56:59], v[40:43]\n"
    "v_mfma_f32_16x16x32_bf16 v[40:43], v[144:147], a[48:51], v[40:43]\n"
    "s_add_u32 s62, s94, s87\n"
    "buffer_load_dwordx4 v31, s[52:55], s89 offen offset:1024 sc1 lds\n"
    "v_mfma_f32_16x16x32_bf16 v[40:43], v[140:143], a[40:43], v[40:43]\n"
    "s_waitcnt vmcnt(6)\n"
    "v_mfma_f32_16x16x32_bf16 v[40:43], v[136:139], a[32:35], v[40:43]\n"
    "s_waitcnt lgkmcnt(4)\n"
    "s_barrier\n"
    "s_and_b32 s62, s62, 0x3ff\n"
    "v_mfma_f32_16x16x32_bf16 v[40:43], v[152:155], a[64:67], v[40:43]\n"
    "ds_read_b128 v[136:139], v26 offset:12288\n"
    "v_mfma_f32_16x16x32_bf16 v[40:43], v[156:159], a[72:75], v[40:43]\n"
    "s_and_b32 s94, s94, 0x400\n"
    "ds_read_b128 v[140:143], v26 offset:13312\n"
    "v_mfma_f32_16x16x32_bf16 v[40:43], v[160:163], a[80:83], v[40:43]\n"
    "ds_read_b128 v[144:147], v26 offset:14336\n"
    "v_mfma_f32_16x16x32_bf16 v[40:43], v[164:167], a[88:91], v[40:43]\n"
    "s_or_b32 s62, s62, s94\n"
    "ds_read_b128 v[148:151], v26 offset:15360\n"
    "v_mfma_f32_16x16x32_bf16 v[60:63], v[164:167], a[92:95], v[60:63]\n"
    "buffer_load_dwordx4 v176, s[52:55], s89 offen offset:2048 sc1 lds\n"
    "v_mfma_f32_16x16x32_bf16 v[60:63], v[160:163], a[84:87], v[60:63]\n"
    "s_mul_i32 s62, s62, 80\n"
    "v_mfma_f32_16x16x32_bf16 v[60:63], v[156:159], a[76:79], v[60:63]\n"
    "v_mfma_f32_16x16x32_bf16 v[60:63], v[152:155], a[68:71], v[60:63]\n"
    "s_waitcnt lgkmcnt(4)\n"
    "s_lshr_b32 s88, s88, 3\n"
    "v_mfma_f32_16x16x32_bf16 v[60:63], v[120:123], a[100:103], v[60:63]\n"
    "ds_read_b128 v[152:155], v26 offset:16384\n"
    "v_mfma_f32_16x16x32_bf16 v[60:63], v[124:127], a[108:111], v[60:63]\n"
    "s_lshl_b32 s88, s88, 10\n"
    "buffer_load_dwordx4 v177, s[52:55], s89 offen offset:3072 sc1 lds\n"
    "v_mfma_f32_16x16x32_bf16 v[60:63], v[128:131], a[116:119], v[60:63]\n"
    "ds_read_b128 v[156:159], v26 offset:17408\n"
    "ds_read_b128 v[160:163], v26 offset:18432\n"
    "v_mfma_f32_16x16x32_bf16 v[60:63], v[132:135], a[124:127], v[60:63]\n"
    "s_add_u32 s88, s88, s62\n"
    "ds_read_b128 v[164:167], v26 offset:19456\n"
    "v_mfma_f32_16x16x32_bf16 v[40:43], v[132:135], a[120:123], v[40:43]\n"
    "v_mfma_f32_16x16x32_bf16 v[40:43], v[128:131], a[112:115], v[40:43]\n"
    "v_mfma_f32_16x16x32_bf16 v[40:43], v[124:127], a[104:107], v[40:43]\n"
    "s_add_u32 m0, s86, 12288\n"
    "v_mfma_f32_16x16x32_bf16 v[40:43], v[120:123], a[96:99], v[40:43]\n"
    "s_waitcnt lgkmcnt(4)\n"
    "s_add_u32 s89, s88, s60\n"
    "v_mfma_f32_16x16x32_bf16 v[40:43], v[136:139], a[128:131], v[40:43]\n"
    "ds_read_b128 v[120:123], v26 offset:20480\n"
    "v_mfma_f32_16x16x32_bf16 v[40:43], v[140:143], a[136:139], v[40:43]\n"
    "ds_read_b128 v[124:127], v26 offset:21504\n"
    "v_mfma_f32_16x16x32_bf16 v[40:43], v[144:147], a[144:147], v[40:43]\n"
    "s_bfe_u32 s88, 0x17161514, s85\n"
    "buffer_load_dwordx4 v1, s[52:55], s89 offen sc1 lds\n"
    "v_mfma_f32_16x16x32_bf16 v[40:43], v[148:151], a[152:155], v[40:43]\n"
    "s_lshl_b32 s94, s88, 8\n"
    "ds_read_b128 v[128:131], v26 offset:22528\n"
    "ds_read_b128 v[132:135], v26 offset:23552\n"
    "v_mfma_f32_16x16x32_bf16 v[60:63], v[148:151], a[156:159], v[60:63]\n"
    "v_mfma_f32_16x16x32_bf16 v[60:63], v[144:147], a[148:151], v[60:63]\n"
    "s_add_u32 s62, s94, s87\n"
    "buffer_load_dwordx4 v31, s[52:55], s89 offen offset:1024 sc1 lds\n"
    "v_mfma_f32_16x16x32_bf16 v[60:63], v[140:143], a[140:143], v[60:63]\n"
    "v_mfma_f32_16x16x32_bf16 v[60:63], v[136:139], a[132:135], v[60:63]\n"
    "s_waitcnt lgkmcnt(4)\n"
    "s_and_b32 s62, s62, 0x3ff\n"
    "v_mfma_f32_16x16x32_bf16 v[60:63], v[152:155], a[164:167], v[60:63]\n"
    "ds_read_b128 v[136:139], v26 offset:24576\n"
    "v_mfma_f32_16x16x32_bf16 v[60:63], v[156:159], a[172:175], v[60:63]\n"
    "s_and_b32 s94, s94, 0x400\n"
    "ds_read_b128 v[140:143], v26 offset:25600\n"
    "v_mfma_f32_16x16x32_bf16 v[60:63], v[160:163], a[180:183], v[60:63]\n"
    "ds_read_b128 v[144:147], v26 offset:26624\n"
    "v_mfma_f32_16x16x32_bf16 v[60:63], v[164:167], a[188:191], v[60:63]\n"
    "s_or_b32 s62, s62, s94\n"
    "ds_read_b128 v[148:151], v26 offset:27648\n"
    "v_mfma_f32_16x16x32_bf16 v[40:43], v[164:167], a[184:187], v[40:43]\n"
    "buffer_load_dwordx4 v176, s[52:55], s89 offen offset:2048 sc1 lds\n"
    "v_mfma_f32_16x16x32_bf16 v[40:43], v[160:163], a[176:179], v[40:43]\n"
    "s_mul_i32 s62, s62, 80\n"
    "v_mfma_f32_16x16x32_bf16 v[40:43], v[156:159], a[168:171], v[40:43]\n"
    "s_waitcnt vmcnt(7)\n"
    "v_mfma_f32_16x16x32_bf16 v[40:43], v[152:155], a[160:163], v[40:43]\n"
    "s_waitcnt lgkmcnt(4)\n"
    "s_barrier\n"
    "s_lshr_b32 s88, s88, 3\n"
    "v_mfma_f32_16x16x32_bf16 v[40:43], v[120:123], a[192:195], v[40:43]\n"
    "ds_read_b128 v[152:155], v26 offset:28672\n"
    "v_mfma_f32_16x16x32_bf16 v[40:43], v[124:127], a[200:203], v[40:43]\n"
    "s_lshl_b32 s88, s88, 10\n"
    "buffer_load_dwordx4 v177, s[52:55], s89 offen offset:3072 sc1 lds\n"
    "v_mfma_f32_16x16x32_bf16 v[40:43], v[128:131], a[208:211], v[40:43]\n"
    "ds_read_b128 v[156:159], v26 offset:29696\n"
    "ds_read_b128 v[160:163], v26 offset:30720\n"
    "v_mfma_f32_16x16x32_bf16 v[40:43], v[132:135], a[216:219], v[40:43]\n"
    "s_add_u32 s88, s88, s62\n"
    "ds_read_b128 v[164:167], v26 offset:31744\n"
    "v_mfma_f32_16x16x32_bf16 v[60:63], v[132:135], a[220:223], v[60:63]\n"
    "v_mfma_f32_16x16x32_bf16 v[60:63], v[128:131], a[212:215], v[60:63]\n"
    "v_mfma_f32_16x16x32_bf16 v[60:63], v[124:127], a[204:207], v[60:63]\n"
    "s_add_u32 m0, s86, 28672\n"
    "v_mfma_f32_16x16x32_bf16 v[60:63], v[120:123], a[196:199], v[60:63]\n"
    "s_waitcnt lgkmcnt(4)\n"
    "s_add_u32 s89, s88, s60\n"
    "v_mfma_f32_16x16x32_bf16 v[60:63], v[136:139], a[228:231], v[60:63]\n"
    "ds_read_b128 v[120:123], v26 offset:32768\n"
    "v_mfma_f32_16x16x32_bf16 v[60:63], v[140:143], a[236:239], v[60:63]\n"
    "ds_read_b128 v[124:127], v26 offset:33792\n"
    "v_mfma_f32_16x16x32_bf16 v[60:63], v[144:147], a[244:247], v[60:63]\n"
    "s_bfe_u32 s88, 0x1b1a1918, s85\n"
    "buffer_load_dwordx4 v1, s[52:55], s89 offen sc1 lds\n"
    "v_mfma_f32_16x16x32_bf16 v[60:63], v[148:151], a[252:255], v[60:63]\n"
    "s_lshl_b32 s94, s88, 8\n"
    "ds_read_b128 v[128:131], v26 offset:34816\n"
    "ds_read_b128 v[132:135], v26 offset:35840\n"
    "v_mfma_f32_16x16x32_bf16 v[40:43], v[148:151], a[248:251], v[40:43]\n"
    "v_mfma_f32_16x16x32_bf16 v[40:43], v[144:147], a[240:243], v[40:43]\n"
    "s_add_u32 s62, s94, s87\n"
    "buffer_load_dwordx4 v31, s[52:55], s89 offen offset:1024 sc1 lds\n"
    "v_mfma_f32_16x16x32_bf16 v[40:43], v[140:143], a[232:235], v[40:43]\n"
    "v_mfma_f32_16x16x32_bf16 v[40:43], v[136:139], a[224:227], v[40:43]\n"
    "s_waitcnt lgkmcnt(4)\n"
    "s_and_b32 s62, s62, 0x3ff\n"
    "v_mfma_f32_16x16x32_bf16 v[44:47], v[152:155], a[0:3], 0\n"
    "ds_read_b128 v[136:139], v26 offset:36864\n"
    "v_mfma_f32_16x16x32_bf16 v[44:47], v[156:159], a[8:11], v[44:47]\n"
    "s_and_b32 s94, s94, 0x400\n"
    "ds_read_b128 v[140:143], v26 offset:37888\n"
    "v_mfma_f32_16x16x32_bf16 v[44:47], v[160:163], a[16:19], v[44:47]\n"
    "ds_read_b128 v[144:147], v26 offset:38912\n"
    "v_mfma_f32_16x16x32_bf16 v[44:47], v[164:167], a[24:27], v[44:47]\n"
    "s_or_b32 s62, s62, s94\n"
    "ds_read_b128 v[148:151], v26 offset:39936\n"
    "v_mfma_f32_16x16x32_bf16 v[64:67], v[164:167], a[28:31], 0\n"
    "s_cmp_lg_u32 s64, 0\n"
    "s_cbranch_scc1 L_l0_13\n"
    "buffer_store_dwordx4 v[196:199], v1, s[28:31], s51 offen sc1\n"
    "buffer_store_dwordx4 v[200:203], v1, s[28:31], s96 offen sc1\n"
    "buffer_store_dwordx4 v[204:207], v1, s[28:31], s97 offen sc1\n"
    "L_l0_13:\n"
    "buffer_load_dwordx4 v176, s[52:55], s89 offen offset:2048 sc1 lds\n"
    "v_mfma_f32_16x16x32_bf16 v[64:67], v[160:163], a[20:23], v[64:67]\n"
    "s_mul_i32 s62, s62, 80\n"
    "v_cvt_pk_bf16_f32 v226, v60, v61\n"
    "v_cvt_pk_bf16_f32 v227, v62, v63\n"
    "v_mfma_f32_16x16x32_bf16 v[64:67], v[156:159], a[12:15], v[64:67]\n"
    "v_cvt_pk_bf16_f32 v224, v40, v41\n"
    "v_cvt_pk_bf16_f32 v225, v42, v43\n"
    "v_mfma_f32_16x16x32_bf16 v[64:67], v[152:155], a[4:7], v[64:67]\n"
    "s_waitcnt lgkmcnt(4)\n"
    "buffer_store_dwordx4 v[224:227], v1, s[28:31], s27 offen sc1\n"
    "v_mfma_f32_16x16x32_bf16 v[64:67], v[120:123], a[36:39], v[64:67]\n"
    "s_lshr_b32 s88, s88, 3\n"
    "ds_read_b128 v[152:155], v26 offset:40960\n"
    "v_mfma_f32_16x16x32_bf16 v[64:67], v[124:127], a[44:47], v[64:67]\n"
    "s_lshl_b32 s88, s88, 10\n"
    "buffer_load_dwordx4 v177, s[52:55], s89 offen offset:3072 sc1 lds\n"
    "v_mfma_f32_16x16x32_bf16 v[64:67], v[128:131], a[52:55], v[64:67]\n"
    "ds_read_b128 v[156:159], v26 offset:41984\n"
    "ds_read_b128 v[160:163], v26 offset:43008\n"
    "v_mfma_f32_16x16x32_bf16 v[64:67], v[132:135], a[60:63], v[64:67]\n"
    "s_add_u32 s88, s88, s62\n"
    "ds_read_b128 v[164:167], v26 offset:44032\n"
    "v_mfma_f32_16x16x32_bf16 v[44:47], v[132:135], a[56:59], v[44:47]\n"
    "v_mfma_f32_16x16x32_bf16 v[44:47], v[128:131], a[48:51], v[44:47]\n"
    "v_mfma_f32_16x16x32_bf16 v[44:47], v[124:127], a[40:43], v[44:47]\n"
    "s_waitcnt vmcnt(9)\n"
    "s_add_u32 m0, s86, 45056\n"
    "v_mfma_f32_16x16x32_bf16 v[44:47], v[120:123], a[32:35], v[44:47]\n"
    "s_waitcnt lgkmcnt(4)\n"
    "s_barrier\n"
    "s_add_u32 s89, s88, s60\n"
    "v_mfma_f32_16x16x32_bf16 v[44:47], v[136:139], a[64:67], v[44:47]\n"
    "ds_read_b128 v[120:123], v26 offset:45056\n"
    "v_mfma_f32_16x16x32_bf16 v[44:47], v[140:143], a[72:75], v[44:47]\n"
    "ds_read_b128 v[124:127], v26 offset:46080\n"
    "v_mfma_f32_16x16x32_bf16 v[44:47], v[144:147], a[80:83], v[44:47]\n"
    "s_bfe_u32 s88, 0x1f1e1d1c, s85\n"
    "buffer_load_dwordx4 v1, s[52:55], s89 offen sc1 lds\n"
    "v_mfma_f32_16x16x32_bf16 v[44:47], v[148:151], a[88:91], v[44:47]\n"
    "s_lshl_b32 s94, s88, 8\n"
    "ds_read_b128 v[128:131], v26 offset:47104\n"
    "ds_read_b128 v[132:135], v26 offset:48128\n"
    "v_mfma_f32_16x16x32_bf16 v[64:67], v[148:151], a[92:95], v[64:67]\n"
    "v_mfma_f32_16x16x32_bf16 v[64:67], v[144:147], a[84:87], v[64:67]\n"
    "s_add_u32 s62, s94, s87\n"
    "buffer_load_dwordx4 v31, s[52:55], s89 offen offset:1024 sc1 lds\n"
    "v_mfma_f32_16x16x32_bf16 v[64:67], v[140:143], a[76:79], v[64:67]\n"
    "v_mfma_f32_16x16x32_bf16 v[64:67], v[136:139], a[68:71], v[64:67]\n"
    "s_waitcnt lgkmcnt(4)\n"
    "s_and_b32 s62, s62, 0x3ff\n"
    "v_mfma_f32_16x16x32_bf16 v[64:67], v[152:155], a[100:103], v[64:67]\n"
    "ds_read_b128 v[136:139], v26 offset:49152\n"
    "v_mfma_f32_16x16x32_bf16 v[64:67], v[156:159], a[108:111], v[64:67]\n"
    "s_and_b32 s94, s94, 0x400\n"
    "ds_read_b128 v[140:143], v26 offset:50176\n"
    "v_mfma_f32_16x16x32_bf16 v[64:67], v[160:163], a[116:119], v[64:67]\n"
    "ds_read_b128 v[144:147], v26 offset:51200\n"
    "v_mfma_f32_16x16x32_bf16 v[64:67], v[164:167], a[124:127], v[64:67]\n"
    "s_or_b32 s62, s62, s94\n"
    "ds_read_b128 v[148:151], v26 offset:52224\n"
    "v_mfma_f32_16x16x32_bf16 v[44:47], v[164:167], a[120:123], v[44:47]\n"
    "buffer_load_dwordx4 v176, s[52:55], s89 offen offset:2048 sc1 lds\n"
    "v_mfma_f32_16x16x32_bf16 v[44:47], v[160:163], a[112:115], v[44:47]\n"
    "s_mul_i32 s62, s62, 80\n"
    "v_mfma_f32_16x16x32_bf16 v[44:47], v[156:159], a[104:107], v[44:47]\n"
    "v_mfma_f32_16x16x32_bf16 v[44:47], v[152:155], a[96:99], v[44:47]\n"
    "s_waitcnt lgkmcnt(4)\n"
    "s_lshr_b32 s88, s88, 3\n"
    "v_mfma_f32_16x16x32_bf16 v[44:47], v[120:123], a[128:131], v[44:47]\n"
    "ds_read_b128 v[152:155], v26 offset:53248\n"
    "v_mfma_f32_16x16x32_bf16 v[44:47], v[124:127], a[136:139], v[44:47]\n"
    "s_lshl_b32 s88, s88, 10\n"
    "buffer_load_dwordx4 v177, s[52:55], s89 offen offset:3072 sc1 lds\n"
    "v_mfma_f32_16x16x32_bf16 v[44:47], v[128:131], a[144:147], v[44:47]\n"
    "ds_read_b128 v[156:159], v26 offset:54272\n"
    "ds_read_b128 v[160:163], v26 offset:55296\n"
    "v_mfma_f32_16x16x32_bf16 v[44:47], v[132:135], a[152:155], v[44:47]\n"
    "s_add_u32 s88, s88, s62\n"
    "ds_read_b128 v[164:167], v26 offset:56320\n"
    "v_mfma_f32_16x16x32_bf16 v[64:67], v[132:135], a[156:159], v[64:67]\n"
    "v_mfma_f32_16x16x32_bf16 v[64:67], v[128:131], a[148:151], v[64:67]\n"
    "v_mfma_f32_16x16x32_bf16 v[64:67], v[124:127], a[140:143], v[64:67]\n"
    "s_add_u32 m0, s86, 61440\n"
    "v_mfma_f32_16x16x32_bf16 v[64:67], v[120:123], a[132:135], v[64:67]\n"
    "s_waitcnt lgkmcnt(4)\n"
    "s_add_u32 s89, s88, s60\n"
    "v_mfma_f32_16x16x32_bf16 v[64:67], v[136:139], a[164:167], v[64:67]\n"
    "ds_read_b128 v[120:123], v26 offset:57344\n"
    "v_mfma_f32_16x16x32_bf16 v[64:67], v[140:143], a[172:175], v[64:67]\n"
    "ds_read_b128 v[124:127], v26 offset:58368\n"
    "v_mfma_f32_16x16x32_bf16 v[64:67], v[144:147], a[180:183], v[64:67]\n"
    "s_bfe_u32 s88, 0x23222120, s85\n"
    "buffer_load_dwordx4 v1, s[52:55], s89 offen sc1 lds\n"
    "v_mfma_f32_16x16x32_bf16 v[64:67], v[148:151], a[188:191], v[64:67]\n"
    "s_lshl_b32 s94, s88, 8\n"
    "ds_read_b128 v[128:131], v26 offset:59392\n"
    "ds_read_b128 v[132:135], v26 offset:60416\n"
    "v_mfma_f32_16x16x32_bf16 v[44:47], v[148:151], a[184:187], v[44:47]\n"
    "v_mfma_f32_16x16x32_bf16 v[44:47], v[144:147], a[176:179], v[44:47]\n"
    "s_add_u32 s62, s94, s87\n"
    "buffer_load_dwordx4 v31, s[52:55], s89 offen offset:1024 sc1 lds\n"
    "v_mfma_f32_16x16x32_bf16 v[44:47], v[140:143], a[168:171], v[44:47]\n"
    "s_waitcnt vmcnt(11)\n"
    "v_mfma_f32_16x16x32_bf16 v[44:47], v[136:139], a[160:163], v[44:47]\n"
    "s_waitcnt lgkmcnt(4)\n"
    "s_barrier\n"
    "s_and_b32 s62, s62, 0x3ff\n"
    "v_mfma_f32_16x16x32_bf16 v[44:47], v[152:155], a[192:195], v[44:47]\n"
    "ds_read_b128 v[136:139], v1 offset:12288\n"
    "v_mfma_f32_16x16x32_bf16 v[44:47], v[156:159], a[200:203], v[44:47]\n"
    "s_and_b32 s94, s94, 0x400\n"
    "ds_read_b128 v[140:143], v1 offset:13312\n"
    "v_mfma_f32_16x16x32_bf16 v[44:47], v[160:163], a[208:211], v[44:47]\n"
    "ds_read_b128 v[144:147], v1 offset:14336\n"
    "v_mfma_f32_16x16x32_bf16 v[44:47], v[164:167], a[216:219], v[44:47]\n"
    "s_or_b32 s62, s62, s94\n"
    "ds_read_b128 v[148:151], v1 offset:15360\n"
    "v_mfma_f32_16x16x32_bf16 v[64:67], v[164:167], a[220:223], v[64:67]\n"
    "buffer_load_dwordx4 v176, s[52:55], s89 offen offset:2048 sc1 lds\n"
    "v_mfma_f32_16x16x32_bf16 v[64:67], v[160:163], a[212:215], v[64:67]\n"
    "s_mul_i32 s62, s62, 80\n"
    "v_mfma_f32_16x16x32_bf16 v[64:67], v[156:159], a[204:207], v[64:67]\n"
    "v_mfma_f32_16x16x32_bf16 v[64:67], v[152:155], a[196:199], v[64:67]\n"
    "s_waitcnt lgkmcnt(4)\n"
    "s_lshr_b32 s88, s88, 3\n"
    "v_mfma_f32_16x16x32_bf16 v[64:67], v[120:123], a[228:231], v[64:67]\n"
    "ds_read_b128 v[152:155], v1 offset:16384\n"
    "v_mfma_f32_16x16x32_bf16 v[64:67], v[124:127], a[236:239], v[64:67]\n"
    "s_lshl_b32 s88, s88, 10\n"
    "buffer_load_dwordx4 v177, s[52:55], s89 offen offset:3072 sc1 lds\n"
    "v_mfma_f32_16x16x32_bf16 v[64:67], v[128:131], a[244:247], v[64:67]\n"
    "ds_read_b128 v[156:159], v1 offset:17408\n"
    "ds_read_b128 v[160:163], v1 offset:18432\n"
    "v_mfma_f32_16x16x32_bf16 v[64:67], v[132:135], a[252:255], v[64:67]\n"
    "s_add_u32 s88, s88, s62\n"
    "ds_read_b128 v[164:167], v1 offset:19456\n"
    "v_mfma_f32_16x16x32_bf16 v[44:47], v[132:135], a[248:251], v[44:47]\n"
    "v_mfma_f32_16x16x32_bf16 v[44:47], v[128:131], a[240:243], v[44:47]\n"
    "s_cmp_lg_u32 s64, 0\n"
    "s_cbranch_scc1 L_l0_14\n"
    "buffer_store_dwordx4 v[208:211], v1, s[28:31], s98 offen sc1\n"
    "buffer_store_dwordx4 v[212:215], v31, s[28:31], s98 offen offset:1024 sc1\n"
    "buffer_store_dwordx4 v[216:219], v220, s[28:31], s99 offen sc1\n"
    "L_l0_14:\n"
    "v_mfma_f32_16x16x32_bf16 v[44:47], v[124:127], a[232:235], v[44:47]\n"
    "s_add_u32 m0, s86, 77824\n"
    "v_mfma_f32_16x16x32_bf16 v[44:47], v[120:123], a[224:227], v[44:47]\n"
    "s_waitcnt lgkmcnt(4)\n"
    "s_add_u32 s89, s88, s60\n"
    "v_mfma_f32_16x16x32_bf16 v[48:51], v[136:139], a[0:3], 0\n"
    "ds_read_b128 v[120:123], v1 offset:20480\n"
    "v_mfma_f32_16x16x32_bf16 v[48:51], v[140:143], a[8:11], v[48:51]\n"
    "ds_read_b128 v[124:127], v1 offset:21504\n"
    "v_mfma_f32_16x16x32_bf16 v[48:51], v[144:147], a[16:19], v[48:51]\n"
    "s_bfe_u32 s88, 0x27262524, s85\n"
    "buffer_load_dwordx4 v1, s[52:55], s89 offen sc1 lds\n"
    "v_mfma_f32_16x16x32_bf16 v[48:51], v[148:151], a[24:27], v[48:51]\n"
    "s_lshl_b32 s94, s88, 8\n"
    "ds_read_b128 v[128:131], v1 offset:22528\n"
    "ds_read_b128 v[132:135], v1 offset:23552\n"
    "v_mfma_f32_16x16x32_bf16 v[68:71], v[148:151], a[28:31], 0\n"
    "v_cvt_pk_bf16_f32 v230, v64, v65\n"
    "v_cvt_pk_bf16_f32 v231, v66, v67\n"
    "v_mfma_f32_16x16x32_bf16 v[68:71], v[144:147], a[20:23], v[68:71]\n"
    "s_add_u32 s62, s94, s87\n"
    "v_cvt_pk_bf16_f32 v228, v44, v45\n"
    "v_cvt_pk_bf16_f32 v229, v46, v47\n"
    "v_mfma_f32_16x16x32_bf16 v[68:71], v[140:143], a[12:15], v[68:71]\n"
    "buffer_store_dwordx4 v[228:231], v1, s[28:31], s27 offen offset:1024 sc1\n"
    "v_mfma_f32_16x16x32_bf16 v[68:71], v[136:139], a[4:7], v[68:71]\n"
    "s_waitcnt lgkmcnt(4)\n"
    "buffer_load_dwordx4 v31, s[52:55], s89 offen offset:1024 sc1 lds\n"
    "v_mfma_f32_16x16x32_bf16 v[68:71], v[152:155], a[36:39], v[68:71]\n"
    "s_and_b32 s62, s62, 0x3ff\n"
    "ds_read_b128 v[136:139], v1 offset:24576\n"
    "v_mfma_f32_16x16x32_bf16 v[68:71], v[156:159], a[44:47], v[68:71]\n"
    "s_and_b32 s94, s94, 0x400\n"
    "ds_read_b128 v[140:143], v1 offset:25600\n"
    "v_mfma_f32_16x16x32_bf16 v[68:71], v[160:163], a[52:55], v[68:71]\n"
    "ds_read_b128 v[144:147], v1 offset:26624\n"
    "v_mfma_f32_16x16x32_bf16 v[68:71], v[164:167], a[60:63], v[68:71]\n"
    "s_or_b32 s62, s62, s94\n"
    "ds_read_b128 v[148:151], v1 offset:27648\n"
    "v_mfma_f32_16x16x32_bf16 v[48:51], v[164:167], a[56:59], v[48:51]\n"
    "buffer_load_dwordx4 v176, s[52:55], s89 offen offset:2048 sc1 lds\n"
    "v_mfma_f32_16x16x32_bf16 v[48:51], v[160:163], a[48:51], v[48:51]\n"
    "s_mul_i32 s62, s62, 80\n"
    "v_mfma_f32_16x16x32_bf16 v[48:51], v[156:159], a[40:43], v[48:51]\n"
    "s_waitcnt vmcnt(12)\n"
    "v_mfma_f32_16x16x32_bf16 v[48:51], v[152:155], a[32:35], v[48:51]\n"
    "s_waitcnt lgkmcnt(4)\n"
    "s_barrier\n"
    "s_lshr_b32 s88, s88, 3\n"
    "v_mfma_f32_16x16x32_bf16 v[48:51], v[120:123], a[64:67], v[48:51]\n"
    "ds_read_b128 v[152:155], v1 offset:28672\n"
    "v_mfma_f32_16x16x32_bf16 v[48:51], v[124:127], a[72:75], v[48:51]\n"
    "s_lshl_b32 s88, s88, 10\n"
    "buffer_load_dwordx4 v177, s[52:55], s89 offen offset:3072 sc1 lds\n"
    "v_mfma_f32_16x16x32_bf16 v[48:51], v[128:131], a[80:83], v[48:51]\n"
    "ds_read_b128 v[156:159], v1 offset:29696\n"
    "ds_read_b128 v[160:163], v1 offset:30720\n"
    "v_mfma_f32_16x16x32_bf16 v[48:51], v[132:135], a[88:91], v[48:51]\n"
    "s_add_u32 s88, s88, s62\n"
    "ds_read_b128 v[164:167], v1 offset:31744\n"
    "v_mfma_f32_16x16x32_bf16 v[68:71], v[132:135], a[92:95], v[68:71]\n"
    "v_mfma_f32_16x16x32_bf16 v[68:71], v[128:131], a[84:87], v[68:71]\n"
    "v_mfma_f32_16x16x32_bf16 v[68:71], v[124:127], a[76:79], v[68:71]\n"
    "s_add_u32 m0, s86, 94208\n"
    "v_mfma_f32_16x16x32_bf16 v[68:71], v[120:123], a[68:71], v[68:71]\n"
    "s_waitcnt lgkmcnt(4)\n"
    "s_add_u32 s89, s88, s60\n"
    "v_mfma_f32_16x16x32_bf16 v[68:71], v[136:139], a[100:103], v[68:71]\n"
    "ds_read_b128 v[120:123], v1 offset:32768\n"
    "v_mfma_f32_16x16x32_bf16 v[68:71], v[140:143], a[108:111], v[68:71]\n"
    "ds_read_b128 v[124:127], v1 offset:33792\n"
    "v_mfma_f32_16x16x32_bf16 v[68:71], v[144:147], a[116:119], v[68:71]\n"
    "s_bfe_u32 s88, 0x18100800, s85\n"
    "buffer_load_dwordx4 v1, s[52:55], s89 offen sc1 lds\n"
    "v_mfma_f32_16x16x32_bf16 v[68:71], v[148:151], a[124:127], v[68:71]\n"
    "s_lshl_b32 s94, s88, 8\n"
    "ds_read_b128 v[128:131], v1 offset:34816\n"
    "ds_read_b128 v[132:135], v1 offset:35840\n"
    "v_mfma_f32_16x16x32_bf16 v[48:51], v[148:151], a[120:123], v[48:51]\n"
    "v_mfma_f32_16x16x32_bf16 v[48:51], v[144:147], a[112:115], v[48:51]\n"
    "s_add_u32 s62, s94, s87\n"
    "buffer_load_dwordx4 v31, s[52:55], s89 offen offset:1024 sc1 lds\n"
    "v_mfma_f32_16x16x32_bf16 v[48:51], v[140:143], a[104:107], v[48:51]\n"
    "v_mfma_f32_16x16x32_bf16 v[48:51], v[136:139], a[96:99], v[48:51]\n"
    "s_waitcnt lgkmcnt(4)\n"
    "s_and_b32 s62, s62, 0x3ff\n"
    "v_mfma_f32_16x16x32_bf16 v[48:51], v[152:155], a[128:131], v[48:51]\n"
    "ds_read_b128 v[136:139], v1 offset:36864\n"
    "v_mfma_f32_16x16x32_bf16 v[48:51], v[156:159], a[136:139], v[48:51]\n"
    "s_and_b32 s94, s94, 0x400\n"
    "ds_read_b128 v[140:143], v1 offset:37888\n"
    "v_mfma_f32_16x16x32_bf16 v[48:51], v[160:163], a[144:147], v[48:51]\n"
    "ds_read_b128 v[144:147], v1 offset:38912\n"
    "v_mfma_f32_16x16x32_bf16 v[48:51], v[164:167], a[152:155], v[48:51]\n"
    "s_or_b32 s62, s62, s94\n"
    "ds_read_b128 v[148:151], v1 offset:39936\n"
    "v_mfma_f32_16x16x32_bf16 v[68:71], v[164:167], a[156:159], v[68:71]\n"
    "buffer_load_dwordx4 v176, s[52:55], s89 offen offset:2048 sc1 lds\n"
    "v_mfma_f32_16x16x32_bf16 v[68:71], v[160:163], a[148:151], v[68:71]\n"
    "s_mul_i32 s62, s62, 80\n"
    "v_mfma_f32_16x16x32_bf16 v[68:71], v[156:159], a[140:143], v[68:71]\n"
    "v_mfma_f32_16x16x32_bf16 v[68:71], v[152:155], a[132:135], v[68:71]\n"
    "s_waitcnt lgkmcnt(4)\n"
    "s_lshr_b32 s88, s88, 3\n"
    "v_mfma_f32_16x16x32_bf16 v[68:71], v[120:123], a[164:167], v[68:71]\n"
    "ds_read_b128 v[152:155], v1 offset:40960\n"
    "v_mfma_f32_16x16x32_bf16 v[68:71], v[124:127], a[172:175], v[68:71]\n"
    "s_lshl_b32 s88, s88, 10\n"
    "buffer_load_dwordx4 v177, s[52:55], s89 offen offset:3072 sc1 lds\n"
    "v_mfma_f32_16x16x32_bf16 v[68:71], v[128:131], a[180:183], v[68:71]\n"
    "ds_read_b128 v[156:159], v1 offset:41984\n"
    "ds_read_b128 v[160:163], v1 offset:43008\n"
    "v_mfma_f32_16x16x32_bf16 v[68:71], v[132:135], a[188:191], v[68:71]\n"
    "s_add_u32 s88, s88, s62\n"
    "ds_read_b128 v[164:167], v1 offset:44032\n"
    "v_mfma_f32_16x16x32_bf16 v[48:51], v[132:135], a[184:187], v[48:51]\n"
    "v_mfma_f32_16x16x32_bf16 v[48:51], v[128:131], a[176:179], v[48:51]\n"
    "v_mfma_f32_16x16x32_bf16 v[48:51], v[124:127], a[168:171], v[48:51]\n"
    "s_waitcnt vmcnt(9)\n"
    "s_add_u32 m0, s86, 110592\n"
    "v_mfma_f32_16x16x32_bf16 v[48:51], v[120:123], a[160:163], v[48:51]\n"
    "s_waitcnt lgkmcnt(4)\n"
    "s_barrier\n"
    "s_add_u32 s89, s88, s73\n"
    "v_mfma_f32_16x16x32_bf16 v[48:51], v[136:139], a[192:195], v[48:51]\n"
    "ds_read_b128 v[120:123], v1 offset:45056\n"
    "v_mfma_f32_16x16x32_bf16 v[48:51], v[140:143], a[200:203], v[48:51]\n"
    "ds_read_b128 v[124:127], v1 offset:46080\n"
    "v_mfma_f32_16x16x32_bf16 v[48:51], v[144:147], a[208:211], v[48:51]\n"
    "s_bfe_u32 s88, 0x11090120, s85\n"
    "buffer_load_dwordx4 v1, s[52:55], s89 offen sc1 lds\n"
    "v_mfma_f32_16x16x32_bf16 v[48:51], v[148:151], a[216:219], v[48:51]\n"
    "s_lshl_b32 s94, s88, 8\n"
    "ds_read_b128 v[128:131], v1 offset:47104\n"
    "ds_read_b128 v[132:135], v1 offset:48128\n"
    "v_mfma_f32_16x16x32_bf16 v[68:71], v[148:151], a[220:223], v[68:71]\n"
    "v_mfma_f32_16x16x32_bf16 v[68:71], v[144:147], a[212:215], v[68:71]\n"
    "s_add_u32 s62, s94, s87\n"
    "buffer_load_dwordx4 v31, s[52:55], s89 offen offset:1024 sc1 lds\n"
    "v_mfma_f32_16x16x32_bf16 v[68:71], v[140:143], a[204:207], v[68:71]\n"
    "v_mfma_f32_16x16x32_bf16 v[68:71], v[136:139], a[196:199], v[68:71]\n"
    "s_waitcnt lgkmcnt(4)\n"
    "s_and_b32 s62, s62, 0x3ff\n"
    "v_mfma_f32_16x16x32_bf16 v[68:71], v[152:155], a[228:231], v[68:71]\n"
    "ds_read_b128 v[136:139], v1 offset:49152\n"
    "v_mfma_f32_16x16x32_bf16 v[68:71], v[156:159], a[236:239], v[68:71]\n"
    "s_and_b32 s94, s94, 0x400\n"
    "ds_read_b128 v[140:143], v1 offset:50176\n"
    "v_mfma_f32_16x16x32_bf16 v[68:71], v[160:163], a[244:247], v[68:71]\n"
    "ds_read_b128 v[144:147], v1 offset:51200\n"
    "v_mfma_f32_16x16x32_bf16 v[68:71], v[164:167], a[252:255], v[68:71]\n"
    "s_or_b32 s62, s62, s94\n"
    "ds_read_b128 v[148:151], v1 offset:52224\n"
    "v_mfma_f32_16x16x32_bf16 v[48:51], v[164:167], a[248:251], v[48:51]\n"
    "buffer_load_dwordx4 v176, s[52:55], s89 offen offset:2048 sc1 lds\n"
    "v_mfma_f32_16x16x32_bf16 v[48:51], v[160:163], a[240:243], v[48:51]\n"
    "s_mul_i32 s62, s62, 80\n"
    "v_mfma_f32_16x16x32_bf16 v[48:51], v[156:159], a[232:235], v[48:51]\n"
    "v_mfma_f32_16x16x32_bf16 v[48:51], v[152:155], a[224:227], v[48:51]\n"
    "s_waitcnt lgkmcnt(4)\n"
    "s_lshr_b32 s88, s88, 3\n"
    "v_mfma_f32_16x16x32_bf16 v[52:55], v[120:123], a[0:3], 0\n"
    "ds_read_b128 v[152:155], v1 offset:53248\n"
    "v_mfma_f32_16x16x32_bf16 v[52:55], v[124:127], a[8:11], v[52:55]\n"
    "s_lshl_b32 s88, s88, 10\n"
    "buffer_load_dwordx4 v177, s[52:55], s89 offen offset:3072 sc1 lds\n"
    "v_mfma_f32_16x16x32_bf16 v[52:55], v[128:131], a[16:19], v[52:55]\n"
    "ds_read_b128 v[156:159], v1 offset:54272\n"
    "ds_read_b128 v[160:163], v1 offset:55296\n"
    "v_mfma_f32_16x16x32_bf16 v[52:55], v[132:135], a[24:27], v[52:55]\n"
    "s_add_u32 s88, s88, s62\n"
    "ds_read_b128 v[164:167], v1 offset:56320\n"
    "v_mfma_f32_16x16x32_bf16 v[72:75], v[132:135], a[28:31], 0\n"
    "v_cvt_pk_bf16_f32 v234, v68, v69\n"
    "v_cvt_pk_bf16_f32 v235, v70, v71\n"
    "v_mfma_f32_16x16x32_bf16 v[72:75], v[128:131], a[20:23], v[72:75]\n"
    "v_cvt_pk_bf16_f32 v232, v48, v49\n"
    "v_cvt_pk_bf16_f32 v233, v50, v51\n"
    "v_mfma_f32_16x16x32_bf16 v[72:75], v[124:127], a[12:15], v[72:75]\n"
    "s_add_u32 m0, s86, 126976\n"
    "buffer_store_dwordx4 v[232:235], v1, s[28:31], s27 offen offset:2048 sc1\n"
    "v_mfma_f32_16x16x32_bf16 v[72:75], v[120:123], a[4:7], v[72:75]\n"
    "s_waitcnt lgkmcnt(4)\n"
    "s_add_u32 s89, s88, s73\n"
    "v_mfma_f32_16x16x32_bf16 v[72:75], v[136:139], a[36:39], v[72:75]\n"
    "ds_read_b128 v[120:123], v1 offset:57344\n"
    "v_mfma_f32_16x16x32_bf16 v[72:75], v[140:143], a[44:47], v[72:75]\n"
    "ds_read_b128 v[124:127], v1 offset:58368\n"
    "v_mfma_f32_16x16x32_bf16 v[72:75], v[144:147], a[52:55], v[72:75]\n"
    "s_bfe_u32 s88, 0xa022119, s85\n"
    "buffer_load_dwordx4 v1, s[52:55], s89 offen sc1 lds\n"
    "v_mfma_f32_16x16x32_bf16 v[72:75], v[148:151], a[60:63], v[72:75]\n"
    "s_lshl_b32 s94, s88, 8\n"
    "ds_read_b128 v[128:131], v1 offset:59392\n"
    "ds_read_b128 v[132:135], v1 offset:60416\n"
    "v_mfma_f32_16x16x32_bf16 v[52:55], v[148:151], a[56:59], v[52:55]\n"
    "v_mfma_f32_16x16x32_bf16 v[52:55], v[144:147], a[48:51], v[52:55]\n"
    "s_add_u32 s62, s94, s87\n"
    "buffer_load_dwordx4 v31, s[52:55], s89 offen offset:1024 sc1 lds\n"
    "v_mfma_f32_16x16x32_bf16 v[52:55], v[140:143], a[40:43], v[52:55]\n"
    "v_mfma_f32_16x16x32_bf16 v[52:55], v[136:139], a[32:35], v[52:55]\n"
    "s_waitcnt lgkmcnt(4)\n"
    "s_and_b32 s62, s62, 0x3ff\n"
    "v_mfma_f32_16x16x32_bf16 v[52:55], v[152:155], a[64:67], v[52:55]\n"
    "ds_read_b128 v[136:139], v1 offset:61440\n"
    "v_mfma_f32_16x16x32_bf16 v[52:55], v[156:159], a[72:75], v[52:55]\n"
    "s_and_b32 s94, s94, 0x400\n"
    "ds_read_b128 v[140:143], v1 offset:62464\n"
    "v_mfma_f32_16x16x32_bf16 v[52:55], v[160:163], a[80:83], v[52:55]\n"
    "ds_read_b128 v[144:147], v1 offset:63488\n"
    "v_mfma_f32_16x16x32_bf16 v[52:55], v[164:167], a[88:91], v[52:55]\n"
    "s_or_b32 s62, s62, s94\n"
    "ds_read_b128 v[148:151], v1 offset:64512\n"
    "v_mfma_f32_16x16x32_bf16 v[72:75], v[164:167], a[92:95], v[72:75]\n"
    "buffer_load_dwordx4 v176, s[52:55], s89 offen offset:2048 sc1 lds\n"
    "v_mfma_f32_16x16x32_bf16 v[72:75], v[160:163], a[84:87], v[72:75]\n"
    "s_mul_i32 s62, s62, 80\n"
    "v_mfma_f32_16x16x32_bf16 v[72:75], v[156:159], a[76:79], v[72:75]\n"
    "v_mfma_f32_16x16x32_bf16 v[72:75], v[152:155], a[68:71], v[72:75]\n"
    "s_waitcnt lgkmcnt(4)\n"
    "s_lshr_b32 s88, s88, 3\n"
    "v_mfma_f32_16x16x32_bf16 v[72:75], v[120:123], a[100:103], v[72:75]\n"
    "ds_read_b128 v[152:155], v27 offset:0\n"
    "v_mfma_f32_16x16x32_bf16 v[72:75], v[124:127], a[108:111], v[72:75]\n"
    "s_lshl_b32 s88, s88, 10\n"
    "buffer_load_dwordx4 v177, s[52:55], s89 offen offset:3072 sc1 lds\n"
    "v_mfma_f32_16x16x32_bf16 v[72:75], v[128:131], a[116:119], v[72:75]\n"
    "ds_read_b128 v[156:159], v27 offset:1024\n"
    "ds_read_b128 v[160:163], v27 offset:2048\n"
    "v_mfma_f32_16x16x32_bf16 v[72:75], v[132:135], a[124:127], v[72:75]\n"
    "s_add_u32 s88, s88, s62\n"
    "ds_read_b128 v[164:167], v27 offset:3072\n"
    "v_mfma_f32_16x16x32_bf16 v[52:55], v[132:135], a[120:123], v[52:55]\n"
    "v_mfma_f32_16x16x32_bf16 v[52:55], v[128:131], a[112:115], v[52:55]\n"
    "v_mfma_f32_16x16x32_bf16 v[52:55], v[124:127], a[104:107], v[52:55]\n"
    "s_add_u32 m0, s86, 143360\n"
    "v_mfma_f32_16x16x32_bf16 v[52:55], v[120:123], a[96:99], v[52:55]\n"
    "s_waitcnt lgkmcnt(4)\n"
    "s_add_u32 s89, s88, s73\n"
    "v_mfma_f32_16x16x32_bf16 v[52:55], v[136:139], a[128:131], v[52:55]\n"
    "ds_read_b128 v[120:123], v27 offset:4096\n"
    "v_mfma_f32_16x16x32_bf16 v[52:55], v[140:143], a[136:139], v[52:55]\n"
    "ds_read_b128 v[124:127], v27 offset:5120\n"
    "v_mfma_f32_16x16x32_bf16 v[52:55], v[144:147], a[144:147], v[52:55]\n"
    "s_bfe_u32 s88, 0x3221a12, s85\n"
    "buffer_load_dwordx4 v1, s[52:55], s89 offen sc1 lds\n"
    "v_mfma_f32_16x16x32_bf16 v[52:55], v[148:151], a[152:155], v[52:55]\n"
    "s_lshl_b32 s94, s88, 8\n"
    "ds_read_b128 v[128:131], v27 offset:6144\n"
    "ds_read_b128 v[132:135], v27 offset:7168\n"
    "v_mfma_f32_16x16x32_bf16 v[72:75], v[148:151], a[156:159], v[72:75]\n"
    "v_mfma_f32_16x16x32_bf16 v[72:75], v[144:147], a[148:151], v[72:75]\n"
    "s_add_u32 s62, s94, s87\n"
    "buffer_load_dwordx4 v31, s[52:55], s89 offen offset:1024 sc1 lds\n"
    "v_mfma_f32_16x16x32_bf16 v[72:75], v[140:143], a[140:143], v[72:75]\n"
    "v_mfma_f32_16x16x32_bf16 v[72:75], v[136:139], a[132:135], v[72:75]\n"
    "s_waitcnt lgkmcnt(4)\n"
    "s_and_b32 s62, s62, 0x3ff\n"
    "v_mfma_f32_16x16x32_bf16 v[72:75], v[152:155], a[164:167], v[72:75]\n"
    "ds_read_b128 v[136:139], v27 offset:8192\n"
    "v_mfma_f32_16x16x32_bf16 v[72:75], v[156:159], a[172:175], v[72:75]\n"
    "s_and_b32 s94, s94, 0x400\n"
    "ds_read_b128 v[140:143], v27 offset:9216\n"
    "v_mfma_f32_16x16x32_bf16 v[72:75], v[160:163], a[180:183], v[72:75]\n"
    "ds_read_b128 v[144:147], v27 offset:10240\n"
    "v_mfma_f32_16x16x32_bf16 v[72:75], v[164:167], a[188:191], v[72:75]\n"
    "s_or_b32 s62, s62, s94\n"
    "ds_read_b128 v[148:151], v27 offset:11264\n"
    "v_mfma_f32_16x16x32_bf16 v[52:55], v[164:167], a[184:187], v[52:55]\n"
    "buffer_load_dwordx4 v176, s[52:55], s89 offen offset:2048 sc1 lds\n"
    "v_mfma_f32_16x16x32_bf16 v[52:55], v[160:163], a[176:179], v[52:55]\n"
    "s_mul_i32 s62, s62, 80\n"
    "v_mfma_f32_16x16x32_bf16 v[52:55], v[156:159], a[168:171], v[52:55]\n"
    "s_waitcnt vmcnt(12)\n"
    "v_mfma_f32_16x16x32_bf16 v[52:55], v[152:155], a[160:163], v[52:55]\n"
    "s_waitcnt lgkmcnt(4)\n"
    "s_barrier\n"
    "s_lshr_b32 s88, s88, 3\n"
    "v_mfma_f32_16x16x32_bf16 v[52:55], v[120:123], a[192:195], v[52:55]\n"
    "s_cmp_lg_u32 s64, 0\n"
    "s_cbranch_scc1 L_l0_15\n"
    "s_lshr_b32 s91, s58, 3\n"
    "s_and_b32 s92, s58, 7\n"
    "s_mul_i32 s91, s91, 128\n"
    "s_mul_i32 s95, s57, 512\n"
    "s_add_u32 s91, s91, s95\n"
    "s_mul_i32 s95, s92, 8\n"
    "s_add_u32 s91, s91, s95\n"
    "s_mul_i32 s95, s56, 32768\n"
    "s_add_u32 s91, s91, s95\n"
    "s_add_u32 s91, s91, 786432\n"
    "s_mov_b64 exec, 0xff\n"
    "buffer_atomic_swap_x2 v[36:37], v39, s[52:55], s91 offen sc1\n"
    "s_mov_b64 exec, -1\n"
    "L_l0_15:\n"
    "v_mfma_f32_16x16x32_bf16 v[52:55], v[124:127], a[200:203], v[52:55]\n"
    "s_lshl_b32 s88, s88, 10\n"
    "ds_read_b128 v[152:155], v27 offset:12288\n"
    "buffer_load_dwordx4 v177, s[52:55], s89 offen offset:3072 sc1 lds\n"
    "v_mfma_f32_16x16x32_bf16 v[52:55], v[128:131], a[208:211], v[52:55]\n"
    "ds_read_b128 v[156:159], v27 offset:13312\n"
    "ds_read_b128 v[160:163], v27 offset:14336\n"
    "v_mfma_f32_16x16x32_bf16 v[52:55], v[132:135], a[216:219], v[52:55]\n"
    "s_add_u32 s88, s88, s62\n"
    "ds_read_b128 v[164:167], v27 offset:15360\n"
    "v_mfma_f32_16x16x32_bf16 v[72:75], v[132:135], a[220:223], v[72:75]\n"
    "v_mfma_f32_16x16x32_bf16 v[72:75], v[128:131], a[212:215], v[72:75]\n"
    "v_mfma_f32_16x16x32_bf16 v[72:75], v[124:127], a[204:207], v[72:75]\n"
    "s_add_u32 m0, s86, 12288\n"
    "v_mfma_f32_16x16x32_bf16 v[72:75], v[120:123], a[196:199], v[72:75]\n"
    "s_waitcnt lgkmcnt(4)\n"
    "s_xor_b32 s93, s12, 512\n"
    "s_add_u32 s89, s88, s73\n"
    "v_mfma_f32_16x16x32_bf16 v[72:75], v[136:139], a[228:231], v[72:75]\n"
    "s_add_u32 s93, s93, 917504\n"
    "ds_read_b128 v[120:123], v27 offset:16384\n"
    "v_mfma_f32_16x16x32_bf16 v[72:75], v[140:143], a[236:239], v[72:75]\n"
    "s_waitcnt vmcnt(8)\n"
    "ds_read_b128 v[124:127], v27 offset:17408\n"
    "v_mfma_f32_16x16x32_bf16 v[72:75], v[144:147], a[244:247], v[72:75]\n"
    "s_mov_b64 exec, 0x1\n"
    "buffer_atomic_swap_x2 v[36:37], v28, s[52:55], s93 offen sc1\n"
    "s_mov_b64 exec, -1\n"
    "v_mfma_f32_16x16x32_bf16 v[72:75], v[148:151], a[252:255], v[72:75]\n"
    "s_bfe_u32 s88, 0x231b130b, s85\n"
    "ds_read_b128 v[128:131], v27 offset:18432\n"
    "buffer_load_dwordx4 v1, s[52:55], s89 offen sc1 lds\n"
    "v_mfma_f32_16x16x32_bf16 v[52:55], v[148:151], a[248:251], v[52:55]\n"
    "s_lshl_b32 s94, s88, 8\n"
    "ds_read_b128 v[132:135], v27 offset:19456\n"
    "v_mfma_f32_16x16x32_bf16 v[52:55], v[144:147], a[240:243], v[52:55]\n"
    "s_add_u32 s62, s94, s87\n"
    "buffer_load_dwordx4 v31, s[52:55], s89 offen offset:1024 sc1 lds\n"
    "v_mfma_f32_16x16x32_bf16 v[52:55], v[140:143], a[232:235], v[52:55]\n"
    "v_mfma_f32_16x16x32_bf16 v[52:55], v[136:139], a[224:227], v[52:55]\n"
    "s_waitcnt lgkmcnt(4)\n"
    "s_and_b32 s62, s62, 0x3ff\n"
    "v_mfma_f32_16x16x32_bf16 v[56:59], v[152:155], a[0:3], 0\n"
    "ds_read_b128 v[136:139], v27 offset:20480\n"
    "v_mfma_f32_16x16x32_bf16 v[56:59], v[156:159], a[8:11], v[56:59]\n"
    "s_and_b32 s94, s94, 0x400\n"
    "ds_read_b128 v[140:143], v27 offset:21504\n"
    "v_mfma_f32_16x16x32_bf16 v[56:59], v[160:163], a[16:19], v[56:59]\n"
    "ds_read_b128 v[144:147], v27 offset:22528\n"
    "v_mfma_f32_16x16x32_bf16 v[56:59], v[164:167], a[24:27], v[56:59]\n"
    "s_or_b32 s62, s62, s94\n"
    "ds_read_b128 v[148:151], v27 offset:23552\n"
    "v_mfma_f32_16x16x32_bf16 v[76:79], v[164:167], a[28:31], 0\n"
    "buffer_load_dwordx4 v176, s[52:55], s89 offen offset:2048 sc1 lds\n"
    "v_mfma_f32_16x16x32_bf16 v[76:79], v[160:163], a[20:23], v[76:79]\n"
    "s_mul_i32 s62, s62, 80\n"
    "v_cvt_pk_bf16_f32 v238, v72, v73\n"
    "v_cvt_pk_bf16_f32 v239, v74, v75\n"
    "v_mfma_f32_16x16x32_bf16 v[76:79], v[156:159], a[12:15], v[76:79]\n"
    "v_cvt_pk_bf16_f32 v236, v52, v53\n"
    "v_cvt_pk_bf16_f32 v237, v54, v55\n"
    "v_mfma_f32_16x16x32_bf16 v[76:79], v[152:155], a[4:7], v[76:79]\n"
    "s_waitcnt lgkmcnt(4)\n"
    "buffer_store_dwordx4 v[236:239], v1, s[28:31], s27 offen offset:3072 sc1\n"
    "v_mfma_f32_16x16x32_bf16 v[76:79], v[120:123], a[36:39], v[76:79]\n"
    "s_lshr_b32 s88, s88, 3\n"
    "ds_read_b128 v[152:155], v27 offset:24576\n"
    "v_mfma_f32_16x16x32_bf16 v[76:79], v[124:127], a[44:47], v[76:79]\n"
    "s_lshl_b32 s88, s88, 10\n"
    "buffer_load_dwordx4 v177, s[52:55], s89 offen offset:3072 sc1 lds\n"
    "v_mfma_f32_16x16x32_bf16 v[76:79], v[128:131], a[52:55], v[76:79]\n"
    "ds_read_b128 v[156:159], v27 offset:25600\n"
    "ds_read_b128 v[160:163], v27 offset:26624\n"
    "v_mfma_f32_16x16x32_bf16 v[76:79], v[132:135], a[60:63], v[76:79]\n"
    "s_add_u32 s88, s88, s62\n"
    "ds_read_b128 v[164:167], v27 offset:27648\n"
    "v_mfma_f32_16x16x32_bf16 v[56:59], v[132:135], a[56:59], v[56:59]\n"
    "v_mfma_f32_16x16x32_bf16 v[56:59], v[128:131], a[48:51], v[56:59]\n"
    "v_mfma_f32_16x16x32_bf16 v[56:59], v[124:127], a[40:43], v[56:59]\n"
    "s_add_u32 m0, s86, 28672\n"
    "v_mfma_f32_16x16x32_bf16 v[56:59], v[120:123], a[32:35], v[56:59]\n"
    "s_waitcnt lgkmcnt(4)\n"
    "s_add_u32 s89, s88, s73\n"
    "v_mfma_f32_16x16x32_bf16 v[56:59], v[136:139], a[64:67], v[56:59]\n"
    "ds_read_b128 v[120:123], v27 offset:28672\n"
    "v_mfma_f32_16x16x32_bf16 v[56:59], v[140:143], a[72:75], v[56:59]\n"
    "ds_read_b128 v[124:127], v27 offset:29696\n"
    "v_mfma_f32_16x16x32_bf16 v[56:59], v[144:147], a[80:83], v[56:59]\n"
    "s_bfe_u32 s88, 0x1c140c04, s85\n"
    "buffer_load_dwordx4 v1, s[52:55], s89 offen sc1 lds\n"
    "v_mfma_f32_16x16x32_bf16 v[56:59], v[148:151], a[88:91], v[56:59]\n"
    "s_lshl_b32 s94, s88, 8\n"
    "ds_read_b128 v[128:131], v27 offset:30720\n"
    "ds_read_b128 v[132:135], v27 offset:31744\n"
    "v_mfma_f32_16x16x32_bf16 v[76:79], v[148:151], a[92:95], v[76:79]\n"
    "v_mfma_f32_16x16x32_bf16 v[76:79], v[144:147], a[84:87], v[76:79]\n"
    "s_add_u32 s62, s94, s87\n"
    "buffer_load_dwordx4 v31, s[52:55], s89 offen offset:1024 sc1 lds\n"
    "v_mfma_f32_16x16x32_bf16 v[76:79], v[140:143], a[76:79], v[76:79]\n"
    "v_mfma_f32_16x16x32_bf16 v[76:79], v[136:139], a[68:71], v[76:79]\n"
    "s_waitcnt lgkmcnt(4)\n"
    "s_and_b32 s62, s62, 0x3ff\n"
    "v_mfma_f32_16x16x32_bf16 v[76:79], v[152:155], a[100:103], v[76:79]\n"
    "ds_read_b128 v[136:139], v26 offset:0\n"
    "v_mfma_f32_16x16x32_bf16 v[76:79], v[156:159], a[108:111], v[76:79]\n"
    "s_and_b32 s94, s94, 0x400\n"
    "ds_read_b128 v[140:143], v26 offset:1024\n"
    "v_mfma_f32_16x16x32_bf16 v[76:79], v[160:163], a[116:119], v[76:79]\n"
    "ds_read_b128 v[144:147], v26 offset:2048\n"
    "v_mfma_f32_16x16x32_bf16 v[76:79], v[164:167], a[124:127], v[76:79]\n"
    "s_or_b32 s62, s62, s94\n"
    "ds_read_b128 v[148:151], v26 offset:3072\n"
    "v_mfma_f32_16x16x32_bf16 v[56:59], v[164:167], a[120:123], v[56:59]\n"
    "buffer_load_dwordx4 v176, s[52:55], s89 offen offset:2048 sc1 lds\n"
    "v_mfma_f32_16x16x32_bf16 v[56:59], v[160:163], a[112:115], v[56:59]\n"
    "s_mul_i32 s62, s62, 80\n"
    "v_mfma_f32_16x16x32_bf16 v[56:59], v[156:159], a[104:107], v[56:59]\n"
    "v_mfma_f32_16x16x32_bf16 v[56:59], v[152:155], a[96:99], v[56:59]\n"
    "s_waitcnt lgkmcnt(4)\n"
    "s_lshr_b32 s88, s88, 3\n"
    "v_mfma_f32_16x16x32_bf16 v[56:59], v[120:123], a[128:131], v[56:59]\n"
    "ds_read_b128 v[152:155], v26 offset:4096\n"
    "v_mfma_f32_16x16x32_bf16 v[56:59], v[124:127], a[136:139], v[56:59]\n"
    "s_lshl_b32 s88, s88, 10\n"
    "buffer_load_dwordx4 v177, s[52:55], s89 offen offset:3072 sc1 lds\n"
    "v_mfma_f32_16x16x32_bf16 v[56:59], v[128:131], a[144:147], v[56:59]\n"
    "ds_read_b128 v[156:159], v26 offset:5120\n"
    "ds_read_b128 v[160:163], v26 offset:6144\n"
    "v_mfma_f32_16x16x32_bf16 v[56:59], v[132:135], a[152:155], v[56:59]\n"
    "s_add_u32 s92, s12, 917504\n"
    "ds_read_b128 v[164:167], v26 offset:7168\n"
    "s_add_u32 s88, s88, s62\n"
    "v_mfma_f32_16x16x32_bf16 v[76:79], v[132:135], a[156:159], v[76:79]\n"
    "v_mov_b64 v[34:35], 0\n"
    "v_mfma_f32_16x16x32_bf16 v[76:79], v[128:131], a[148:151], v[76:79]\n"
    "s_mov_b64 exec, 0x1\n"
    "buffer_atomic_or_x2 v[34:35], v28, s[52:55], s92 offen sc0 sc1\n"
    "s_mov_b64 exec, -1\n"
    "v_mfma_f32_16x16x32_bf16 v[76:79], v[124:127], a[140:143], v[76:79]\n"
    "s_add_u32 m0, s86, 45056\n"
    "v_mfma_f32_16x16x32_bf16 v[76:79], v[120:123], a[132:135], v[76:79]\n"
    "s_waitcnt lgkmcnt(4)\n"
    "s_add_u32 s89, s88, s73\n"
    "v_mfma_f32_16x16x32_bf16 v[76:79], v[136:139], a[164:167], v[76:79]\n"
    "ds_read_b128 v[120:123], v26 offset:8192\n"
    "v_mfma_f32_16x16x32_bf16 v[76:79], v[140:143], a[172:175], v[76:79]\n"
    "ds_read_b128 v[124:127], v26 offset:9216\n"
    "v_mfma_f32_16x16x32_bf16 v[76:79], v[144:147], a[180:183], v[76:79]\n"
    "s_bfe_u32 s88, 0x150d0524, s85\n"
    "buffer_load_dwordx4 v1, s[52:55], s89 offen sc1 lds\n"
    "v_mfma_f32_16x16x32_bf16 v[76:79], v[148:151], a[188:191], v[76:79]\n"
    "s_lshl_b32 s94, s88, 8\n"
    "ds_read_b128 v[128:131], v26 offset:10240\n"
    "ds_read_b128 v[132:135], v26 offset:11264\n"
    "v_mfma_f32_16x16x32_bf16 v[56:59], v[148:151], a[184:187], v[56:59]\n"
    "v_mfma_f32_16x16x32_bf16 v[56:59], v[144:147], a[176:179], v[56:59]\n"
    "s_add_u32 s62, s94, s87\n"
    "buffer_load_dwordx4 v31, s[52:55], s89 offen offset:1024 sc1 lds\n"
    "v_mfma_f32_16x16x32_bf16 v[56:59], v[140:143], a[168:171], v[56:59]\n"
    "s_waitcnt vmcnt(17)\n"
    "v_mfma_f32_16x16x32_bf16 v[56:59], v[136:139], a[160:163], v[56:59]\n"
    "s_waitcnt lgkmcnt(4)\n"
    "s_barrier\n"
    "s_and_b32 s62, s62, 0x3ff\n"
    "v_mfma_f32_16x16x32_bf16 v[56:59], v[152:155], a[192:195], v[56:59]\n"
    "ds_read_b128 v[136:139], v26 offset:12288\n"
    "v_mfma_f32_16x16x32_bf16 v[56:59], v[156:159], a[200:203], v[56:59]\n"
    "s_and_b32 s94, s94, 0x400\n"
    "ds_read_b128 v[140:143], v26 offset:13312\n"
    "v_mfma_f32_16x16x32_bf16 v[56:59], v[160:163], a[208:211], v[56:59]\n"
    "ds_read_b128 v[144:147], v26 offset:14336\n"
    "v_mfma_f32_16x16x32_bf16 v[56:59], v[164:167], a[216:219], v[56:59]\n"
    "s_or_b32 s62, s62, s94\n"
    "ds_read_b128 v[148:151], v26 offset:15360\n"
    "v_mfma_f32_16x16x32_bf16 v[76:79], v[164:167], a[220:223], v[76:79]\n"
    "buffer_load_dwordx4 v176, s[52:55], s89 offen offset:2048 sc1 lds\n"
    "v_mfma_f32_16x16x32_bf16 v[76:79], v[160:163], a[212:215], v[76:79]\n"
    "s_mul_i32 s62, s62, 80\n"
    "v_mfma_f32_16x16x32_bf16 v[76:79], v[156:159], a[204:207], v[76:79]\n"
    "v_mfma_f32_16x16x32_bf16 v[76:79], v[152:155], a[196:199], v[76:79]\n"
    "s_waitcnt lgkmcnt(4)\n"
    "s_lshr_b32 s88, s88, 3\n"
    "v_mfma_f32_16x16x32_bf16 v[76:79], v[120:123], a[228:231], v[76:79]\n"
    "ds_read_b128 v[152:155], v26 offset:16384\n"
    "v_mfma_f32_16x16x32_bf16 v[76:79], v[124:127], a[236:239], v[76:79]\n"
    "s_lshl_b32 s88, s88, 10\n"
    "buffer_load_dwordx4 v177, s[52:55], s89 offen offset:3072 sc1 lds\n"
    "v_mfma_f32_16x16x32_bf16 v[76:79], v[128:131], a[244:247], v[76:79]\n"
    "ds_read_b128 v[156:159], v26 offset:17408\n"
    "ds_read_b128 v[160:163], v26 offset:18432\n"
    "v_mfma_f32_16x16x32_bf16 v[76:79], v[132:135], a[252:255], v[76:79]\n"
    "s_add_u32 s88, s88, s62\n"
    "ds_read_b128 v[164:167], v26 offset:19456\n"
    "v_mfma_f32_16x16x32_bf16 v[56:59], v[132:135], a[248:251], v[56:59]\n"
    "v_mfma_f32_16x16x32_bf16 v[56:59], v[128:131], a[240:243], v[56:59]\n"
    "v_mfma_f32_16x16x32_bf16 v[56:59], v[124:127], a[232:235], v[56:59]\n"
    "s_add_u32 m0, s86, 61440\n"
    "v_mfma_f32_16x16x32_bf16 v[56:59], v[120:123], a[224:227], v[56:59]\n"
    "s_waitcnt lgkmcnt(4)\n"
    "s_add_u32 s89, s88, s73\n"
    "v_mfma_f32_16x16x32_bf16 v[80:83], v[136:139], a[0:3], 0\n"
    "ds_read_b128 v[120:123], v26 offset:20480\n"
    "v_mfma_f32_16x16x32_bf16 v[80:83], v[140:143], a[8:11], v[80:83]\n"
    "ds_read_b128 v[124:127], v26 offset:21504\n"
    "v_mfma_f32_16x16x32_bf16 v[80:83], v[144:147], a[16:19], v[80:83]\n"
    "s_bfe_u32 s88, 0xe06251d, s85\n"
    "buffer_load_dwordx4 v1, s[52:55], s89 offen sc1 lds\n"
    "v_mfma_f32_16x16x32_bf16 v[80:83], v[148:151], a[24:27], v[80:83]\n"
    "s_lshl_b32 s94, s88, 8\n"
    "ds_read_b128 v[128:131], v26 offset:22528\n"
    "ds_read_b128 v[132:135], v26 offset:23552\n"
    "v_mfma_f32_16x16x32_bf16 v[100:103], v[148:151], a[28:31], 0\n"
    "v_cvt_pk_bf16_f32 v242, v76, v77\n"
    "v_cvt_pk_bf16_f32 v243, v78, v79\n"
    "v_mfma_f32_16x16x32_bf16 v[100:103], v[144:147], a[20:23], v[100:103]\n"
    "s_add_u32 s62, s94, s87\n"
    "v_cvt_pk_bf16_f32 v240, v56, v57\n"
    "v_cvt_pk_bf16_f32 v241, v58, v59\n"
    "v_mfma_f32_16x16x32_bf16 v[100:103], v[140:143], a[12:15], v[100:103]\n"
    "buffer_store_dwordx4 v[240:243], v31, s[28:31], s27 offen sc1\n"
    "v_mfma_f32_16x16x32_bf16 v[100:103], v[136:139], a[4:7], v[100:103]\n"
    "s_waitcnt lgkmcnt(4)\n"
    "buffer_load_dwordx4 v31, s[52:55], s89 offen offset:1024 sc1 lds\n"
    "v_mfma_f32_16x16x32_bf16 v[84:87], v[152:155], a[0:3], 0\n"
    "s_and_b32 s62, s62, 0x3ff\n"
    "ds_read_b128 v[136:139], v26 offset:24576\n"
    "v_mfma_f32_16x16x32_bf16 v[84:87], v[156:159], a[8:11], v[84:87]\n"
    "s_and_b32 s94, s94, 0x400\n"
    "ds_read_b128 v[140:143], v26 offset:25600\n"
    "v_mfma_f32_16x16x32_bf16 v[84:87], v[160:163], a[16:19], v[84:87]\n"
    "ds_read_b128 v[144:147], v26 offset:26624\n"
    "v_mfma_f32_16x16x32_bf16 v[84:87], v[164:167], a[24:27], v[84:87]\n"
    "s_or_b32 s62, s62, s94\n"
    "ds_read_b128 v[148:151], v26 offset:27648\n"
    "v_mfma_f32_16x16x32_bf16 v[104:107], v[164:167], a[28:31], 0\n"
    "buffer_load_dwordx4 v176, s[52:55], s89 offen offset:2048 sc1 lds\n"
    "v_mfma_f32_16x16x32_bf16 v[104:107], v[160:163], a[20:23], v[104:107]\n"
    "s_mul_i32 s62, s62, 80\n"
    "v_mfma_f32_16x16x32_bf16 v[104:107], v[156:159], a[12:15], v[104:107]\n"
    "v_mfma_f32_16x16x32_bf16 v[104:107], v[152:155], a[4:7], v[104:107]\n"
    "s_waitcnt lgkmcnt(4)\n"
    "s_lshr_b32 s88, s88, 3\n"
    "v_mfma_f32_16x16x32_bf16 v[88:91], v[120:123], a[0:3], 0\n"
    "ds_read_b128 v[152:155], v26 offset:28672\n"
    "v_mfma_f32_16x16x32_bf16 v[88:91], v[124:127], a[8:11], v[88:91]\n"
    "s_lshl_b32 s88, s88, 10\n"
    "buffer_load_dwordx4 v177, s[52:55], s89 offen offset:3072 sc1 lds\n"
    "v_mfma_f32_16x16x32_bf16 v[88:91], v[128:131], a[16:19], v[88:91]\n"
    "ds_read_b128 v[156:159], v26 offset:29696\n"
    "ds_read_b128 v[160:163], v26 offset:30720\n"
    "v_mfma_f32_16x16x32_bf16 v[88:91], v[132:135], a[24:27], v[88:91]\n"
    "s_add_u32 s88, s88, s62\n"
    "ds_read_b128 v[164:167], v26 offset:31744\n"
    "v_mfma_f32_16x16x32_bf16 v[108:111], v[132:135], a[28:31], 0\n"
    "v_mfma_f32_16x16x32_bf16 v[108:111], v[128:131], a[20:23], v[108:111]\n"
    "v_mfma_f32_16x16x32_bf16 v[108:111], v[124:127], a[12:15], v[108:111]\n"
    "s_add_u32 m0, s86, 77824\n"
    "v_mfma_f32_16x16x32_bf16 v[108:111], v[120:123], a[4:7], v[108:111]\n"
    "s_waitcnt vmcnt(9) lgkmcnt(4)\n"
    "s_add_u32 s89, s88, s73\n"
    "v_mfma_f32_16x16x32_bf16 v[92:95], v[136:139], a[0:3], 0\n"
    "v_cmp_ne_u64 vcc, s[66:67], v[34:35]\n"
    "ds_read_b128 v[120:123], v26 offset:32768\n"
    "v_mfma_f32_16x16x32_bf16 v[92:95], v[140:143], a[8:11], v[92:95]\n"
    "s_and_b64 vcc, vcc, 1\n"
    "s_cbranch_scc1 L_miss_17\n"
    "L_ok_16:\n"
    "ds_read_b128 v[124:127], v26 offset:33792\n"
    "v_mfma_f32_16x16x32_bf16 v[92:95], v[144:147], a[16:19], v[92:95]\n"
    "s_bfe_u32 s88, 0x1e0f1607, s85\n"
    "buffer_load_dwordx4 v1, s[52:55], s89 offen sc1 lds\n"
    "v_mfma_f32_16x16x32_bf16 v[92:95], v[148:151], a[24:27], v[92:95]\n"
    "ds_read_b128 v[128:131], v26 offset:34816\n"
    "ds_read_b128 v[132:135], v26 offset:35840\n"
    "v_mfma_f32_16x16x32_bf16 v[112:115], v[148:151], a[28:31], 0\n"
    "s_lshl_b32 s94, s88, 8\n"
    "buffer_load_dwordx4 v[224:227], v1, s[28:31], s26 offen sc1\n"
    "v_mfma_f32_16x16x32_bf16 v[112:115], v[144:147], a[20:23], v[112:115]\n"
    "buffer_load_dwordx4 v31, s[52:55], s89 offen offset:1024 sc1 lds\n"
    "v_mfma_f32_16x16x32_bf16 v[112:115], v[140:143], a[12:15], v[112:115]\n"
    "s_add_u32 s62, s94, s87\n"
    "v_mfma_f32_16x16x32_bf16 v[112:115], v[136:139], a[4:7], v[112:115]\n"
    "s_waitcnt lgkmcnt(4)\n"
    "v_mfma_f32_16x16x32_bf16 v[96:99], v[152:155], a[0:3], 0\n"
    "s_and_b32 s62, s62, 0x3ff\n"
    "ds_read_b128 v[136:139], v26 offset:36864\n"
    "v_mfma_f32_16x16x32_bf16 v[96:99], v[156:159], a[8:11], v[96:99]\n"
    "ds_read_b128 v[140:143], v26 offset:37888\n"
    "v_mfma_f32_16x16x32_bf16 v[96:99], v[160:163], a[16:19], v[96:99]\n"
    "s_and_b32 s94, s94, 0x400\n"
    "ds_read_b128 v[144:147], v26 offset:38912\n"
    "v_mfma_f32_16x16x32_bf16 v[96:99], v[164:167], a[24:27], v[96:99]\n"
    "ds_read_b128 v[148:151], v26 offset:39936\n"
    "v_mfma_f32_16x16x32_bf16 v[116:119], v[164:167], a[28:31], 0\n"
    "s_or_b32 s62, s62, s94\n"
    "s_cmp_lg_u32 s64, 0\n"
    "s_cbranch_scc1 L_l0_19\n"
    "s_add_u32 s92, s13, 655360\n"
    "v_mov_b64 v[34:35], 0\n"
    "s_mov_b64 exec, 0xff\n"
    "buffer_atomic_or_x2 v[34:35], v28, s[52:55], s92 offen sc0 sc1\n"
    "s_mov_b64 exec, -1\n"
    "L_l0_19:\n"
    "buffer_load_dwordx4 v176, s[52:55], s89 offen offset:2048 sc1 lds\n"
    "v_mfma_f32_16x16x32_bf16 v[116:119], v[160:163], a[20:23], v[116:119]\n"
    "v_mfma_f32_16x16x32_bf16 v[116:119], v[156:159], a[12:15], v[116:119]\n"
    "s_mul_i32 s62, s62, 80\n"
    "v_mfma_f32_16x16x32_bf16 v[116:119], v[152:155], a[4:7], v[116:119]\n"
    "s_waitcnt lgkmcnt(4)\n"
    "buffer_load_dwordx4 v177, s[52:55], s89 offen offset:3072 sc1 lds\n"
    "v_mfma_f32_16x16x32_bf16 v[80:83], v[120:123], a[32:35], v[80:83]\n"
    "s_lshr_b32 s88, s88, 3\n"
    "ds_read_b128 v[152:155], v26 offset:40960\n"
    "v_mfma_f32_16x16x32_bf16 v[80:83], v[124:127], a[40:43], v[80:83]\n"
    "ds_read_b128 v[156:159], v26 offset:41984\n"
    "v_mfma_f32_16x16x32_bf16 v[80:83], v[128:131], a[48:51], v[80:83]\n"
    "s_lshl_b32 s88, s88, 10\n"
    "ds_read_b128 v[160:163], v26 offset:43008\n"
    "v_mfma_f32_16x16x32_bf16 v[80:83], v[132:135], a[56:59], v[80:83]\n"
    "ds_read_b128 v[164:167], v26 offset:44032\n"
    "v_mfma_f32_16x16x32_bf16 v[100:103], v[132:135], a[60:63], v[100:103]\n"
    "s_add_u32 s88, s88, s62\n"
    "v_mfma_f32_16x16x32_bf16 v[100:103], v[128:131], a[52:55], v[100:103]\n"
    "v_mfma_f32_16x16x32_bf16 v[100:103], v[124:127], a[44:47], v[100:103]\n"
    "buffer_load_dwordx4 v[228:231], v1, s[28:31], s26 offen offset:1024 sc1\n"
    "v_mfma_f32_16x16x32_bf16 v[100:103], v[120:123], a[36:39], v[100:103]\n"
    "s_waitcnt lgkmcnt(4)\n"
    "s_barrier\n"
    "s_add_u32 m0, s86, 94208\n"
    "v_mfma_f32_16x16x32_bf16 v[84:87], v[136:139], a[32:35], v[84:87]\n"
    "s_add_u32 s89, s88, s73\n"
    "ds_read_b128 v[120:123], v26 offset:45056\n"
    "v_mfma_f32_16x16x32_bf16 v[84:87], v[140:143], a[40:43], v[84:87]\n"
    "ds_read_b128 v[124:127], v26 offset:46080\n"
    "v_mfma_f32_16x16x32_bf16 v[84:87], v[144:147], a[48:51], v[84:87]\n"
    "ds_read_b128 v[128:131], v26 offset:47104\n"
    "v_mfma_f32_16x16x32_bf16 v[84:87], v[148:151], a[56:59], v[84:87]\n"
    "s_bfe_u32 s88, 0x271f2617, s85\n"
    "buffer_load_dwordx4 v1, s[52:55], s89 offen sc1 lds\n"
    "v_mfma_f32_16x16x32_bf16 v[104:107], v[148:151], a[60:63], v[104:107]\n"
    "s_lshl_b32 s94, s88, 8\n"
    "ds_read_b128 v[132:135], v26 offset:48128\n"
    "v_mfma_f32_16x16x32_bf16 v[104:107], v[144:147], a[52:55], v[104:107]\n"
    "v_mfma_f32_16x16x32_bf16 v[104:107], v[140:143], a[44:47], v[104:107]\n"
    "s_add_u32 s62, s94, s87\n"
    "buffer_load_dwordx4 v31, s[52:55], s89 offen offset:1024 sc1 lds\n"
    "v_mfma_f32_16x16x32_bf16 v[104:107], v[136:139], a[36:39], v[104:107]\n"
    "s_waitcnt lgkmcnt(4)\n"
    "v_mfma_f32_16x16x32_bf16 v[88:91], v[152:155], a[32:35], v[88:91]\n"
    "s_and_b32 s62, s62, 0x3ff\n"
    "ds_read_b128 v[136:139], v26 offset:49152\n"
    "v_mfma_f32_16x16x32_bf16 v[88:91], v[156:159], a[40:43], v[88:91]\n"
    "ds_read_b128 v[140:143], v26 offset:50176\n"
    "v_mfma_f32_16x16x32_bf16 v[88:91], v[160:163], a[48:51], v[88:91]\n"
    "s_and_b32 s94, s94, 0x400\n"
    "ds_read_b128 v[144:147], v26 offset:51200\n"
    "v_mfma_f32_16x16x32_bf16 v[88:91], v[164:167], a[56:59], v[88:91]\n"
    "ds_read_b128 v[148:151], v26 offset:52224\n"
    "v_mfma_f32_16x16x32_bf16 v[108:111], v[164:167], a[60:63], v[108:111]\n"
    "s_or_b32 s62, s62, s94\n"
    "buffer_load_dwordx4 v176, s[52:55], s89 offen offset:2048 sc1 lds\n"
    "v_mfma_f32_16x16x32_bf16 v[108:111], v[160:163], a[52:55], v[108:111]\n"
    "s_mul_i32 s62, s62, 80\n"
    "v_mfma_f32_16x16x32_bf16 v[108:111], v[156:159], a[44:47], v[108:111]\n"
    "v_mfma_f32_16x16x32_bf16 v[108:111], v[152:155], a[36:39], v[108:111]\n"
    "s_waitcnt lgkmcnt(4)\n"
    "s_lshr_b32 s88, s88, 3\n"
    "v_mfma_f32_16x16x32_bf16 v[92:95], v[120:123], a[32:35], v[92:95]\n"
    "ds_read_b128 v[152:155], v26 offset:53248\n"
    "v_mfma_f32_16x16x32_bf16 v[92:95], v[124:127], a[40:43], v[92:95]\n"
    "s_lshl_b32 s88, s88, 10\n"
    "ds_read_b128 v[156:159], v26 offset:54272\n"
    "v_mfma_f32_16x16x32_bf16 v[92:95], v[128:131], a[48:51], v[92:95]\n"
    "buffer_load_dwordx4 v177, s[52:55], s89 offen offset:3072 sc1 lds\n"
    "v_mfma_f32_16x16x32_bf16 v[92:95], v[132:135], a[56:59], v[92:95]\n"
    "s_add_u32 s88, s88, s62\n"
    "ds_read_b128 v[160:163], v26 offset:55296\n"
    "ds_read_b128 v[164:167], v26 offset:56320\n"
    "v_mfma_f32_16x16x32_bf16 v[112:115], v[132:135], a[60:63], v[112:115]\n"
    "buffer_load_dwordx4 v[232:235], v1, s[28:31], s26 offen offset:2048 sc1\n"
    "v_mfma_f32_16x16x32_bf16 v[112:115], v[128:131], a[52:55], v[112:115]\n"
    "v_mfma_f32_16x16x32_bf16 v[112:115], v[124:127], a[44:47], v[112:115]\n"
    "s_add_u32 m0, s86, 110592\n"
    "v_mfma_f32_16x16x32_bf16 v[112:115], v[120:123], a[36:39], v[112:115]\n"
    "s_waitcnt lgkmcnt(4)\n"
    "s_add_u32 s89, s88, s73\n"
    "v_mfma_f32_16x16x32_bf16 v[96:99], v[136:139], a[32:35], v[96:99]\n"
    "ds_read_b128 v[120:123], v26 offset:57344\n"
    "v_mfma_f32_16x16x32_bf16 v[96:99], v[140:143], a[40:43], v[96:99]\n"
    "ds_read_b128 v[124:127], v26 offset:58368\n"
    "v_mfma_f32_16x16x32_bf16 v[96:99], v[144:147], a[48:51], v[96:99]\n"
    "buffer_load_dwordx4 v1, s[52:55], s89 offen sc1 lds\n"
    "v_mfma_f32_16x16x32_bf16 v[96:99], v[148:151], a[56:59], v[96:99]\n"
    "ds_read_b128 v[128:131], v26 offset:59392\n"
    "ds_read_b128 v[132:135], v26 offset:60416\n"
    "v_mfma_f32_16x16x32_bf16 v[116:119], v[148:151], a[60:63], v[116:119]\n"
    "v_mfma_f32_16x16x32_bf16 v[116:119], v[144:147], a[52:55], v[116:119]\n"
    "s_xor_b32 s93, s12, 512\n"
    "s_cmp_lg_u32 s64, 0\n"
    "s_cbranch_scc1 L_l0_20\n"
    "s_waitcnt vmcnt(9)\n"
    "v_cmp_ne_u64 vcc, s[66:67], v[34:35]\n"
    "s_nop 0\n"
    "s_and_b64 vcc, vcc, 255\n"
    "s_cbranch_scc1 L_miss_22\n"
    "L_ok_21:\n"
    "s_nop 1\n"
    "L_l0_20:\n"
    "v_mfma_f32_16x16x32_bf16 v[116:119], v[140:143], a[44:47], v[116:119]\n"
    "s_add_u32 s93, s93, 917512\n"
    "buffer_load_dwordx4 v31, s[52:55], s89 offen offset:1024 sc1 lds\n"
    "v_mfma_f32_16x16x32_bf16 v[116:119], v[136:139], a[36:39], v[116:119]\n"
    "s_waitcnt vmcnt(16) lgkmcnt(4)\n"
    "s_barrier\n"
    "v_mfma_f32_16x16x32_bf16 v[80:83], v[152:155], a[64:67], v[80:83]\n"
    "s_mov_b64 exec, 0x1\n"
    "buffer_atomic_swap_x2 v[36:37], v28, s[52:55], s93 offen sc1\n"
    "s_mov_b64 exec, -1\n"
    "v_mfma_f32_16x16x32_bf16 v[80:83], v[156:159], a[72:75], v[80:83]\n"
    "ds_read_b128 v[136:139], v1 offset:12288\n"
    "ds_read_b128 v[140:143], v1 offset:13312\n"
    "v_mfma_f32_16x16x32_bf16 v[80:83], v[160:163], a[80:83], v[80:83]\n"
    "ds_read_b128 v[144:147], v1 offset:14336\n"
    "v_mfma_f32_16x16x32_bf16 v[80:83], v[164:167], a[88:91], v[80:83]\n"
    "ds_read_b128 v[148:151], v1 offset:15360\n"
    "v_mfma_f32_16x16x32_bf16 v[100:103], v[164:167], a[92:95], v[100:103]\n"
    "buffer_load_dwordx4 v176, s[52:55], s89 offen offset:2048 sc1 lds\n"
    "v_mfma_f32_16x16x32_bf16 v[100:103], v[160:163], a[84:87], v[100:103]\n"
    "v_mfma_f32_16x16x32_bf16 v[100:103], v[156:159], a[76:79], v[100:103]\n"
    "v_mfma_f32_16x16x32_bf16 v[100:103], v[152:155], a[68:71], v[100:103]\n"
    "s_waitcnt lgkmcnt(4)\n"
    "buffer_load_dwordx4 v177, s[52:55], s89 offen offset:3072 sc1 lds\n"
    "v_mfma_f32_16x16x32_bf16 v[84:87], v[120:123], a[64:67], v[84:87]\n"
    "ds_read_b128 v[152:155], v1 offset:16384\n"
    "v_mfma_f32_16x16x32_bf16 v[84:87], v[124:127], a[72:75], v[84:87]\n"
    "ds_read_b128 v[156:159], v1 offset:17408\n"
    "v_mfma_f32_16x16x32_bf16 v[84:87], v[128:131], a[80:83], v[84:87]\n"
    "s_cmp_lg_u32 s64, 0\n"
    "s_cbranch_scc1 L_l0_24\n"
    "s_mul_i32 s91, s56, 1024\n"
    "s_add_u32 s91, s91, 159744\n"
    "s_mov_b32 m0, s91\n"
    "s_mul_i32 s91, s61, 3840\n"
    "s_mul_i32 s95, s56, 1024\n"
    "s_add_u32 s91, s91, s95\n"
    "s_add_u32 s91, s91, 4096\n"
    "buffer_load_dwordx4 v1, s[28:31], s91 offen sc1 lds\n"
    "s_getpc_b64 s[48:49]\n"
    "L_pc_25:\n"
    "s_and_b32 s49, s49, 0xffff\n"
    "s_mov_b32 s50, L_pc1-L_pc_25\n"
    "s_mov_b32 s51, 0x20000\n"
    "s_lshr_b32 s91, s63, 3\n"
    "s_mul_i32 s91, s91, 512\n"
    "s_mul_i32 s95, s56, 128\n"
    "s_add_u32 s91, s91, s95\n"
    "v_lshlrev_b32 v255, 3, v1\n"
    "s_mov_b64 exec, 0x1\n"
    "buffer_load_dword v255, v255, s[48:51], s91 offen\n"
    "s_mov_b64 exec, -1\n"
    "L_l0_24:\n"
    "v_mfma_f32_16x16x32_bf16 v[84:87], v[132:135], a[88:91], v[84:87]\n"
    "ds_read_b128 v[160:163], v1 offset:18432\n"
    "ds_read_b128 v[164:167], v1 offset:19456\n"
    "v_mfma_f32_16x16x32_bf16 v[104:107], v[132:135], a[92:95], v[104:107]\n"
    "buffer_load_dwordx4 a[0:3], v1, s[28:31], s81 offen sc1\n"
    "v_mfma_f32_16x16x32_bf16 v[104:107], v[128:131], a[84:87], v[104:107]\n"
    "v_mfma_f32_16x16x32_bf16 v[104:107], v[124:127], a[76:79], v[104:107]\n"
    "buffer_load_dwordx4 a[4:7], v1, s[28:31], s81 offen offset:1024 sc1\n"
    "v_mfma_f32_16x16x32_bf16 v[104:107], v[120:123], a[68:71], v[104:107]\n"
    "s_waitcnt lgkmcnt(4)\n"
    "v_mfma_f32_16x16x32_bf16 v[88:91], v[136:139], a[64:67], v[88:91]\n"
    "ds_read_b128 v[120:123], v1 offset:20480\n"
    "v_mfma_f32_16x16x32_bf16 v[88:91], v[140:143], a[72:75], v[88:91]\n"
    "ds_read_b128 v[124:127], v1 offset:21504\n"
    "v_mfma_f32_16x16x32_bf16 v[88:91], v[144:147], a[80:83], v[88:91]\n"
    "ds_read_b128 v[128:131], v1 offset:22528\n"
    "v_mfma_f32_16x16x32_bf16 v[88:91], v[148:151], a[88:91], v[88:91]\n"
    "ds_read_b128 v[132:135], v1 offset:23552\n"
    "v_mfma_f32_16x16x32_bf16 v[108:111], v[148:151], a[92:95], v[108:111]\n"
    "buffer_load_dwordx4 a[8:11], v1, s[28:31], s81 offen offset:2048 sc1\n"
    "v_mfma_f32_16x16x32_bf16 v[108:111], v[144:147], a[84:87], v[108:111]\n"
    "v_mfma_f32_16x16x32_bf16 v[108:111], v[140:143], a[76:79], v[108:111]\n"
    "v_mfma_f32_16x16x32_bf16 v[108:111], v[136:139], a[68:71], v[108:111]\n"
    "s_waitcnt lgkmcnt(4)\n"
    "v_mfma_f32_16x16x32_bf16 v[92:95], v[152:155], a[64:67], v[92:95]\n"
    "ds_read_b128 v[136:139], v1 offset:24576\n"
    "v_mfma_f32_16x16x32_bf16 v[92:95], v[156:159], a[72:75], v[92:95]\n"
    "s_waitcnt vmcnt(17)\n"
    "ds_read_b128 v[140:143], v1 offset:25600\n"
    "v_mfma_f32_16x16x32_bf16 v[92:95], v[160:163], a[80:83], v[92:95]\n"
    "ds_read_b128 v[144:147], v1 offset:26624\n"
    "v_lshlrev_b32 v40, 16, v224\n"
    "v_mfma_f32_16x16x32_bf16 v[92:95], v[164:167], a[88:91], v[92:95]\n"
    "buffer_load_dwordx4 a[12:15], v1, s[28:31], s81 offen offset:3072 sc1\n"
    "v_mfma_f32_16x16x32_bf16 v[112:115], v[164:167], a[92:95], v[112:115]\n"
    "s_add_u32 s90, s81, 4096\n"
    "ds_read_b128 v[148:151], v1 offset:27648\n"
    "v_and_b32 v41, 0xffff0000, v224\n"
    "v_mfma_f32_16x16x32_bf16 v[112:115], v[160:163], a[84:87], v[112:115]\n"
    "v_lshlrev_b32 v42, 16, v225\n"
    "v_and_b32 v43, 0xffff0000, v225\n"
    "v_mfma_f32_16x16x32_bf16 v[112:115], v[156:159], a[76:79], v[112:115]\n"
    "v_lshlrev_b32 v60, 16, v226\n"
    "v_and_b32 v61, 0xffff0000, v226\n"
    "v_mfma_f32_16x16x32_bf16 v[112:115], v[152:155], a[68:71], v[112:115]\n"
    "s_waitcnt lgkmcnt(4)\n"
    "s_barrier\n"
    "buffer_load_dwordx4 a[16:19], v1, s[28:31], s90 offen sc1\n"
    "v_mfma_f32_16x16x32_bf16 v[96:99], v[120:123], a[64:67], v[96:99]\n"
    "ds_read_b128 v[152:155], v1 offset:28672\n"
    "v_lshlrev_b32 v62, 16, v227\n"
    "v_mfma_f32_16x16x32_bf16 v[96:99], v[124:127], a[72:75], v[96:99]\n"
    "ds_read_b128 v[156:159], v1 offset:29696\n"
    "v_and_b32 v63, 0xffff0000, v227\n"
    "v_mfma_f32_16x16x32_bf16 v[96:99], v[128:131], a[80:83], v[96:99]\n"
    "ds_read_b128 v[160:163], v1 offset:30720\n"
    "v_mfma_f32_16x16x32_bf16 v[96:99], v[132:135], a[88:91], v[96:99]\n"
    "ds_read_b128 v[164:167], v1 offset:31744\n"
    "v_mfma_f32_16x16x32_bf16 v[116:119], v[132:135], a[92:95], v[116:119]\n"
    "buffer_load_dwordx4 a[20:23], v1, s[28:31], s90 offen offset:1024 sc1\n"
    "v_mfma_f32_16x16x32_bf16 v[116:119], v[128:131], a[84:87], v[116:119]\n"
    "v_mfma_f32_16x16x32_bf16 v[116:119], v[124:127], a[76:79], v[116:119]\n"
    "buffer_load_dwordx4 a[24:27], v1, s[28:31], s90 offen offset:2048 sc1\n"
    "v_mfma_f32_16x16x32_bf16 v[116:119], v[120:123], a[68:71], v[116:119]\n"
    "s_waitcnt lgkmcnt(4)\n"
    "v_mfma_f32_16x16x32_bf16 v[80:83], v[136:139], a[96:99], v[80:83]\n"
    "ds_read_b128 v[120:123], v1 offset:32768\n"
    "v_mfma_f32_16x16x32_bf16 v[80:83], v[140:143], a[104:107], v[80:83]\n"
    "ds_read_b128 v[124:127], v1 offset:33792\n"
    "v_mfma_f32_16x16x32_bf16 v[80:83], v[144:147], a[112:115], v[80:83]\n"
    "ds_read_b128 v[128:131], v1 offset:34816\n"
    "v_mfma_f32_16x16x32_bf16 v[80:83], v[148:151], a[120:123], v[80:83]\n"
    "ds_read_b128 v[132:135], v1 offset:35840\n"
    "v_mfma_f32_16x16x32_bf16 v[100:103], v[148:151], a[124:127], v[100:103]\n"
    "buffer_load_dwordx4 a[28:31], v1, s[28:31], s90 offen offset:3072 sc1\n"
    "v_mfma_f32_16x16x32_bf16 v[100:103], v[144:147], a[116:119], v[100:103]\n"
    "s_waitcnt vmcnt(18)\n"
    "v_mfma_f32_16x16x32_bf16 v[100:103], v[140:143], a[108:111], v[100:103]\n"
    "s_add_u32 s92, s12, 917512\n"
    "v_lshlrev_b32 v44, 16, v228\n"
    "v_and_b32 v45, 0xffff0000, v228\n"
    "v_mfma_f32_16x16x32_bf16 v[100:103], v[136:139], a[100:103], v[100:103]\n"
    "s_waitcnt lgkmcnt(4)\n"
    "v_mov_b64 v[34:35], 0\n"
    "v_lshlrev_b32 v46, 16, v229\n"
    "v_mfma_f32_16x16x32_bf16 v[84:87], v[152:155], a[96:99], v[84:87]\n"
    "s_mov_b64 exec, 0x1\n"
    "buffer_atomic_or_x2 v[34:35], v28, s[52:55], s92 offen sc0 sc1\n"
    "s_mov_b64 exec, -1\n"
    "v_mfma_f32_16x16x32_bf16 v[84:87], v[156:159], a[104:107], v[84:87]\n"
    "s_add_u32 s90, s81, 8192\n"
    "ds_read_b128 v[136:139], v1 offset:36864\n"
    "ds_read_b128 v[140:143], v1 offset:37888\n"
    "v_mfma_f32_16x16x32_bf16 v[84:87], v[160:163], a[112:115], v[84:87]\n"
    "buffer_load_dwordx4 a[32:35], v1, s[28:31], s90 offen sc1\n"
    "v_mfma_f32_16x16x32_bf16 v[84:87], v[164:167], a[120:123], v[84:87]\n"
    "ds_read_b128 v[144:147], v1 offset:38912\n"
    "ds_read_b128 v[148:151], v1 offset:39936\n"
    "v_mfma_f32_16x16x32_bf16 v[104:107], v[164:167], a[124:127], v[104:107]\n"
    "v_and_b32 v47, 0xffff0000, v229\n"
    "v_lshlrev_b32 v64, 16, v230\n"
    "v_mfma_f32_16x16x32_bf16 v[104:107], v[160:163], a[116:119], v[104:107]\n"
    "v_and_b32 v65, 0xffff0000, v230\n"
    "v_lshlrev_b32 v66, 16, v231\n"
    "v_mfma_f32_16x16x32_bf16 v[104:107], v[156:159], a[108:111], v[104:107]\n"
    "v_and_b32 v67, 0xffff0000, v231\n"
    "v_mfma_f32_16x16x32_bf16 v[104:107], v[152:155], a[100:103], v[104:107]\n"
    "s_waitcnt lgkmcnt(4)\n"
    "buffer_load_dwordx4 a[36:39], v1, s[28:31], s90 offen offset:1024 sc1\n"
    "v_mfma_f32_16x16x32_bf16 v[88:91], v[120:123], a[96:99], v[88:91]\n"
    "ds_read_b128 v[152:155], v1 offset:40960\n"
    "v_mfma_f32_16x16x32_bf16 v[88:91], v[124:127], a[104:107], v[88:91]\n"
    "ds_read_b128 v[156:159], v1 offset:41984\n"
    "v_mfma_f32_16x16x32_bf16 v[88:91], v[128:131], a[112:115], v[88:91]\n"
    "ds_read_b128 v[160:163], v1 offset:43008\n"
    "v_mfma_f32_16x16x32_bf16 v[88:91], v[132:135], a[120:123], v[88:91]\n"
    "buffer_load_dwordx4 a[40:43], v1, s[28:31], s90 offen offset:2048 sc1\n"
    "v_mfma_f32_16x16x32_bf16 v[108:111], v[132:135], a[124:127], v[108:111]\n"
    "ds_read_b128 v[164:167], v1 offset:44032\n"
    "v_mfma_f32_16x16x32_bf16 v[108:111], v[128:131], a[116:119], v[108:111]\n"
    "buffer_load_dwordx4 a[44:47], v1, s[28:31], s90 offen offset:3072 sc1\n"
    "v_mfma_f32_16x16x32_bf16 v[108:111], v[124:127], a[108:111], v[108:111]\n"
    "v_mfma_f32_16x16x32_bf16 v[108:111], v[120:123], a[100:103], v[108:111]\n"
    "s_waitcnt lgkmcnt(4)\n"
    "v_mfma_f32_16x16x32_bf16 v[92:95], v[136:139], a[96:99], v[92:95]\n"
    "ds_read_b128 v[120:123], v1 offset:45056\n"
    "v_mfma_f32_16x16x32_bf16 v[92:95], v[140:143], a[104:107], v[92:95]\n"
    "s_waitcnt vmcnt(18)\n"
    "ds_read_b128 v[124:127], v1 offset:46080\n"
    "s_add_u32 s90, s81, 12288\n"
    "v_mfma_f32_16x16x32_bf16 v[92:95], v[144:147], a[112:115], v[92:95]\n"
    "ds_read_b128 v[128:131], v1 offset:47104\n"
    "v_lshlrev_b32 v48, 16, v232\n"
    "v_mfma_f32_16x16x32_bf16 v[92:95], v[148:151], a[120:123], v[92:95]\n"
    "ds_read_b128 v[132:135], v1 offset:48128\n"
    "v_and_b32 v49, 0xffff0000, v232\n"
    "v_mfma_f32_16x16x32_bf16 v[112:115], v[148:151], a[124:127], v[112:115]\n"
    "v_lshlrev_b32 v50, 16, v233\n"
    "v_and_b32 v51, 0xffff0000, v233\n"
    "v_mfma_f32_16x16x32_bf16 v[112:115], v[144:147], a[116:119], v[112:115]\n"
    "buffer_load_dwordx4 a[48:51], v1, s[28:31], s90 offen sc1\n"
    "v_mfma_f32_16x16x32_bf16 v[112:115], v[140:143], a[108:111], v[112:115]\n"
    "v_lshlrev_b32 v68, 16, v234\n"
    "v_and_b32 v69, 0xffff0000, v234\n"
    "v_mfma_f32_16x16x32_bf16 v[112:115], v[136:139], a[100:103], v[112:115]\n"
    "s_waitcnt lgkmcnt(4)\n"
    "v_lshlrev_b32 v70, 16, v235\n"
    "v_and_b32 v71, 0xffff0000, v235\n"
    "v_mfma_f32_16x16x32_bf16 v[96:99], v[152:155], a[96:99], v[96:99]\n"
    "ds_read_b128 v[136:139], v1 offset:49152\n"
    "v_mfma_f32_16x16x32_bf16 v[96:99], v[156:159], a[104:107], v[96:99]\n"
    "ds_read_b128 v[140:143], v1 offset:50176\n"
    "v_mfma_f32_16x16x32_bf16 v[96:99], v[160:163], a[112:115], v[96:99]\n"
    "buffer_load_dwordx4 a[52:55], v1, s[28:31], s90 offen offset:1024 sc1\n"
    "v_mfma_f32_16x16x32_bf16 v[96:99], v[164:167], a[120:123], v[96:99]\n"
    "ds_read_b128 v[144:147], v1 offset:51200\n"
    "ds_read_b128 v[148:151], v1 offset:52224\n"
    "v_mfma_f32_16x16x32_bf16 v[116:119], v[164:167], a[124:127], v[116:119]\n"
    "buffer_load_dwordx4 a[56:59], v1, s[28:31], s90 offen offset:2048 sc1\n"
    "v_mfma_f32_16x16x32_bf16 v[116:119], v[160:163], a[116:119], v[116:119]\n"
    "v_mfma_f32_16x16x32_bf16 v[116:119], v[156:159], a[108:111], v[116:119]\n"
    "v_mfma_f32_16x16x32_bf16 v[116:119], v[152:155], a[100:103], v[116:119]\n"
    "s_waitcnt lgkmcnt(4)\n"
    "v_mfma_f32_16x16x32_bf16 v[80:83], v[120:123], a[128:131], v[80:83]\n"
    "ds_read_b128 v[152:155], v1 offset:53248\n"
    "v_mfma_f32_16x16x32_bf16 v[80:83], v[124:127], a[136:139], v[80:83]\n"
    "ds_read_b128 v[156:159], v1 offset:54272\n"
    "v_mfma_f32_16x16x32_bf16 v[80:83], v[128:131], a[144:147], v[80:83]\n"
    "ds_read_b128 v[160:163], v1 offset:55296\n"
    "v_mfma_f32_16x16x32_bf16 v[80:83], v[132:135], a[152:155], v[80:83]\n"
    "buffer_load_dwordx4 a[60:63], v1, s[28:31], s90 offen offset:3072 sc1\n"
    "v_mfma_f32_16x16x32_bf16 v[100:103], v[132:135], a[156:159], v[100:103]\n"
    "s_add_u32 s90, s81, 16384\n"
    "ds_read_b128 v[164:167], v1 offset:56320\n"
    "v_mfma_f32_16x16x32_bf16 v[100:103], v[128:131], a[148:151], v[100:103]\n"
    "buffer_load_dwordx4 a[64:67], v1, s[28:31], s90 offen sc1\n"
    "v_mfma_f32_16x16x32_bf16 v[100:103], v[124:127], a[140:143], v[100:103]\n"
    "v_mfma_f32_16x16x32_bf16 v[100:103], v[120:123], a[132:135], v[100:103]\n"
    "s_waitcnt lgkmcnt(4)\n"
    "v_mfma_f32_16x16x32_bf16 v[84:87], v[136:139], a[128:131], v[84:87]\n"
    "ds_read_b128 v[120:123], v1 offset:57344\n"
    "v_mfma_f32_16x16x32_bf16 v[84:87], v[140:143], a[136:139], v[84:87]\n"
    "ds_read_b128 v[124:127], v1 offset:58368\n"
    "v_mfma_f32_16x16x32_bf16 v[84:87], v[144:147], a[144:147], v[84:87]\n"
    "ds_read_b128 v[128:131], v1 offset:59392\n"
    "v_mfma_f32_16x16x32_bf16 v[84:87], v[148:151], a[152:155], v[84:87]\n"
    "ds_read_b128 v[132:135], v1 offset:60416\n"
    "v_mfma_f32_16x16x32_bf16 v[104:107], v[148:151], a[156:159], v[104:107]\n"
    "buffer_load_dwordx4 a[68:71], v1, s[28:31], s90 offen offset:1024 sc1\n"
    "v_mfma_f32_16x16x32_bf16 v[104:107], v[144:147], a[148:151], v[104:107]\n"
    "v_mfma_f32_16x16x32_bf16 v[104:107], v[140:143], a[140:143], v[104:107]\n"
    "buffer_load_dwordx4 a[72:75], v1, s[28:31], s90 offen offset:2048 sc1\n"
    "v_mfma_f32_16x16x32_bf16 v[104:107], v[136:139], a[132:135], v[104:107]\n"
    "s_waitcnt lgkmcnt(4)\n"
    "s_barrier\n"
    "v_mfma_f32_16x16x32_bf16 v[88:91], v[152:155], a[128:131], v[88:91]\n"
    "ds_read_b128 v[136:139], v1 offset:61440\n"
    "v_mfma_f32_16x16x32_bf16 v[88:91], v[156:159], a[136:139], v[88:91]\n"
    "ds_read_b128 v[140:143], v1 offset:62464\n"
    "v_mfma_f32_16x16x32_bf16 v[88:91], v[160:163], a[144:147], v[88:91]\n"
    "s_waitcnt vmcnt(11)\n"
    "ds_read_b128 v[144:147], v1 offset:63488\n"
    "v_mfma_f32_16x16x32_bf16 v[88:91], v[164:167], a[152:155], v[88:91]\n"
    "v_cmp_ne_u64 vcc, s[66:67], v[34:35]\n"
    "ds_read_b128 v[148:151], v1 offset:64512\n"
    "v_mfma_f32_16x16x32_bf16 v[108:111], v[164:167], a[156:159], v[108:111]\n"
    "s_and_b64 vcc, vcc, 1\n"
    "s_cbranch_scc1 L_miss_27\n"
    "L_ok_26:\n"
    "v_mfma_f32_16x16x32_bf16 v[108:111], v[160:163], a[148:151], v[108:111]\n"
    "buffer_load_dwordx4 v[240:243], v31, s[28:31], s26 offen sc1\n"
    "v_mfma_f32_16x16x32_bf16 v[108:111], v[156:159], a[140:143], v[108:111]\n"
    "buffer_load_dwordx4 a[76:79], v1, s[28:31], s90 offen offset:3072 sc1\n"
    "v_mfma_f32_16x16x32_bf16 v[108:111], v[152:155], a[132:135], v[108:111]\n"
    "s_waitcnt lgkmcnt(4)\n"
    "s_add_u32 s90, s81, 20480\n"
    "v_mfma_f32_16x16x32_bf16 v[92:95], v[120:123], a[128:131], v[92:95]\n"
    "ds_read_b128 v[152:155], v27 offset:0\n"
    "v_mfma_f32_16x16x32_bf16 v[92:95], v[124:127], a[136:139], v[92:95]\n"
    "ds_read_b128 v[156:159], v27 offset:1024\n"
    "v_mfma_f32_16x16x32_bf16 v[92:95], v[128:131], a[144:147], v[92:95]\n"
    "buffer_load_dwordx4 a[80:83], v1, s[28:31], s90 offen sc1\n"
    "v_mfma_f32_16x16x32_bf16 v[92:95], v[132:135], a[152:155], v[92:95]\n"
    "ds_read_b128 v[160:163], v27 offset:2048\n"
    "ds_read_b128 v[164:167], v27 offset:3072\n"
    "v_mfma_f32_16x16x32_bf16 v[112:115], v[132:135], a[156:159], v[112:115]\n"
    "buffer_load_dwordx4 a[84:87], v1, s[28:31], s90 offen offset:1024 sc1\n"
    "v_mfma_f32_16x16x32_bf16 v[112:115], v[128:131], a[148:151], v[112:115]\n"
    "v_mfma_f32_16x16x32_bf16 v[112:115], v[124:127], a[140:143], v[112:115]\n"
    "v_mfma_f32_16x16x32_bf16 v[112:115], v[120:123], a[132:135], v[112:115]\n"
    "s_waitcnt lgkmcnt(4)\n"
    "v_mfma_f32_16x16x32_bf16 v[96:99], v[136:139], a[128:131], v[96:99]\n"
    "ds_read_b128 v[120:123], v27 offset:4096\n"
    "v_mfma_f32_16x16x32_bf16 v[96:99], v[140:143], a[136:139], v[96:99]\n"
    "ds_read_b128 v[124:127], v27 offset:5120\n"
    "v_mfma_f32_16x16x32_bf16 v[96:99], v[144:147], a[144:147], v[96:99]\n"
    "ds_read_b128 v[128:131], v27 offset:6144\n"
    "v_mfma_f32_16x16x32_bf16 v[96:99], v[148:151], a[152:155], v[96:99]\n"
    "ds_read_b128 v[132:135], v27 offset:7168\n"
    "v_mfma_f32_16x16x32_bf16 v[116:119], v[148:151], a[156:159], v[116:119]\n"
    "buffer_load_dwordx4 a[88:91], v1, s[28:31], s90 offen offset:2048 sc1\n"
    "v_mfma_f32_16x16x32_bf16 v[116:119], v[144:147], a[148:151], v[116:119]\n"
    "buffer_load_dwordx4 a[92:95], v1, s[28:31], s90 offen offset:3072 sc1\n"
    "v_mfma_f32_16x16x32_bf16 v[116:119], v[140:143], a[140:143], v[116:119]\n"
    "v_mfma_f32_16x16x32_bf16 v[116:119], v[136:139], a[132:135], v[116:119]\n"
    "s_waitcnt lgkmcnt(4)\n"
    "v_mfma_f32_16x16x32_bf16 v[80:83], v[152:155], a[160:163], v[80:83]\n"
    "ds_read_b128 v[136:139], v27 offset:8192\n"
    "v_mfma_f32_16x16x32_bf16 v[80:83], v[156:159], a[168:171], v[80:83]\n"
    "s_add_u32 s90, s81, 24576\n"
    "ds_read_b128 v[140:143], v27 offset:9216\n"
    "v_mfma_f32_16x16x32_bf16 v[80:83], v[160:163], a[176:179], v[80:83]\n"
    "ds_read_b128 v[144:147], v27 offset:10240\n"
    "v_mfma_f32_16x16x32_bf16 v[80:83], v[164:167], a[184:187], v[80:83]\n"
    "ds_read_b128 v[148:151], v27 offset:11264\n"
    "v_mfma_f32_16x16x32_bf16 v[100:103], v[164:167], a[188:191], v[100:103]\n"
    "buffer_load_dwordx4 v[236:239], v1, s[28:31], s26 offen offset:3072 sc1\n"
    "v_mfma_f32_16x16x32_bf16 v[100:103], v[160:163], a[180:183], v[100:103]\n"
    "buffer_load_dwordx4 a[96:99], v1, s[28:31], s90 offen sc1\n"
    "v_mfma_f32_16x16x32_bf16 v[100:103], v[156:159], a[172:175], v[100:103]\n"
    "v_mfma_f32_16x16x32_bf16 v[100:103], v[152:155], a[164:167], v[100:103]\n"
    "s_waitcnt lgkmcnt(4)\n"
    "buffer_load_dwordx4 a[100:103], v1, s[28:31], s90 offen offset:1024 sc1\n"
    "v_mfma_f32_16x16x32_bf16 v[84:87], v[120:123], a[160:163], v[84:87]\n"
    "ds_read_b128 v[152:155], v27 offset:12288\n"
    "v_mfma_f32_16x16x32_bf16 v[84:87], v[124:127], a[168:171], v[84:87]\n"
    "ds_read_b128 v[156:159], v27 offset:13312\n"
    "v_mfma_f32_16x16x32_bf16 v[84:87], v[128:131], a[176:179], v[84:87]\n"
    "ds_read_b128 v[160:163], v27 offset:14336\n"
    "v_mfma_f32_16x16x32_bf16 v[84:87], v[132:135], a[184:187], v[84:87]\n"
    "ds_read_b128 v[164:167], v27 offset:15360\n"
    "v_mfma_f32_16x16x32_bf16 v[104:107], v[132:135], a[188:191], v[104:107]\n"
    "buffer_load_dwordx4 a[104:107], v1, s[28:31], s90 offen offset:2048 sc1\n"
    "v_mfma_f32_16x16x32_bf16 v[104:107], v[128:131], a[180:183], v[104:107]\n"
    "v_mfma_f32_16x16x32_bf16 v[104:107], v[124:127], a[172:175], v[104:107]\n"
    "v_mfma_f32_16x16x32_bf16 v[104:107], v[120:123], a[164:167], v[104:107]\n"
    "s_waitcnt lgkmcnt(4)\n"
    "v_mfma_f32_16x16x32_bf16 v[88:91], v[136:139], a[160:163], v[88:91]\n"
    "ds_read_b128 v[120:123], v27 offset:16384\n"
    "v_mfma_f32_16x16x32_bf16 v[88:91], v[140:143], a[168:171], v[88:91]\n"
    "ds_read_b128 v[124:127], v27 offset:17408\n"
    "v_mfma_f32_16x16x32_bf16 v[88:91], v[144:147], a[176:179], v[88:91]\n"
    "ds_read_b128 v[128:131], v27 offset:18432\n"
    "v_mfma_f32_16x16x32_bf16 v[88:91], v[148:151], a[184:187], v[88:91]\n"
    "ds_read_b128 v[132:135], v27 offset:19456\n"
    "v_mfma_f32_16x16x32_bf16 v[108:111], v[148:151], a[188:191], v[108:111]\n"
    "buffer_load_dwordx4 a[108:111], v1, s[28:31], s90 offen offset:3072 sc1\n"
    "v_mfma_f32_16x16x32_bf16 v[108:111], v[144:147], a[180:183], v[108:111]\n"
    "v_mfma_f32_16x16x32_bf16 v[108:111], v[140:143], a[172:175], v[108:111]\n"
    "s_add_u32 s90, s81, 28672\n"
    "v_mfma_f32_16x16x32_bf16 v[108:111], v[136:139], a[164:167], v[108:111]\n"
    "s_waitcnt lgkmcnt(4)\n"
    "buffer_load_dwordx4 a[112:115], v1, s[28:31], s90 offen sc1\n"
    "v_mfma_f32_16x16x32_bf16 v[92:95], v[152:155], a[160:163], v[92:95]\n"
    "ds_read_b128 v[136:139], v27 offset:20480\n"
    "v_mfma_f32_16x16x32_bf16 v[92:95], v[156:159], a[168:171], v[92:95]\n"
    "ds_read_b128 v[140:143], v27 offset:21504\n"
    "v_mfma_f32_16x16x32_bf16 v[92:95], v[160:163], a[176:179], v[92:95]\n"
    "ds_read_b128 v[144:147], v27 offset:22528\n"
    "v_mfma_f32_16x16x32_bf16 v[92:95], v[164:167], a[184:187], v[92:95]\n"
    "ds_read_b128 v[148:151], v27 offset:23552\n"
    "v_mfma_f32_16x16x32_bf16 v[112:115], v[164:167], a[188:191], v[112:115]\n"
    "v_mfma_f32_16x16x32_bf16 v[112:115], v[160:163], a[180:183], v[112:115]\n"
    "buffer_load_dwordx4 a[116:119], v1, s[28:31], s90 offen offset:1024 sc1\n"
    "v_mfma_f32_16x16x32_bf16 v[112:115], v[156:159], a[172:175], v[112:115]\n"
    "v_mfma_f32_16x16x32_bf16 v[112:115], v[152:155], a[164:167], v[112:115]\n"
    "s_waitcnt lgkmcnt(4)\n"
    "v_mfma_f32_16x16x32_bf16 v[96:99], v[120:123], a[160:163], v[96:99]\n"
    "ds_read_b128 v[152:155], v27 offset:24576\n"
    "v_mfma_f32_16x16x32_bf16 v[96:99], v[124:127], a[168:171], v[96:99]\n"
    "ds_read_b128 v[156:159], v27 offset:25600\n"
    "v_mfma_f32_16x16x32_bf16 v[96:99], v[128:131], a[176:179], v[96:99]\n"
    "ds_read_b128 v[160:163], v27 offset:26624\n"
    "v_mfma_f32_16x16x32_bf16 v[96:99], v[132:135], a[184:187], v[96:99]\n"
    "ds_read_b128 v[164:167], v27 offset:27648\n"
    "v_mfma_f32_16x16x32_bf16 v[116:119], v[132:135], a[188:191], v[116:119]\n"
    "buffer_load_dwordx4 a[120:123], v1, s[28:31], s90 offen offset:2048 sc1\n"
    "v_mfma_f32_16x16x32_bf16 v[116:119], v[128:131], a[180:183], v[116:119]\n"
    "v_mfma_f32_16x16x32_bf16 v[116:119], v[124:127], a[172:175], v[116:119]\n"
    "v_mfma_f32_16x16x32_bf16 v[116:119], v[120:123], a[164:167], v[116:119]\n"
    "s_waitcnt lgkmcnt(4)\n"
    "s_barrier\n"
    "v_mfma_f32_16x16x32_bf16 v[80:83], v[136:139], a[192:195], v[80:83]\n"
    "ds_read_b128 v[120:123], v27 offset:28672\n"
    "v_mfma_f32_16x16x32_bf16 v[80:83], v[140:143], a[200:203], v[80:83]\n"
    "ds_read_b128 v[124:127], v27 offset:29696\n"
    "v_mfma_f32_16x16x32_bf16 v[80:83], v[144:147], a[208:211], v[80:83]\n"
    "ds_read_b128 v[128:131], v27 offset:30720\n"
    "v_mfma_f32_16x16x32_bf16 v[80:83], v[148:151], a[216:219], v[80:83]\n"
    "buffer_load_dwordx4 a[124:127], v1, s[28:31], s90 offen offset:3072 sc1\n"
    "v_mfma_f32_16x16x32_bf16 v[100:103], v[148:151], a[220:223], v[100:103]\n"
    "ds_read_b128 v[132:135], v27 offset:31744\n"
    "v_mfma_f32_16x16x32_bf16 v[100:103], v[144:147], a[212:215], v[100:103]\n"
    "s_add_u32 s90, s81, 32768\n"
    "v_mfma_f32_16x16x32_bf16 v[100:103], v[140:143], a[204:207], v[100:103]\n"
    "buffer_load_dwordx4 a[128:131], v1, s[28:31], s90 offen sc1\n"
    "v_mfma_f32_16x16x32_bf16 v[100:103], v[136:139], a[196:199], v[100:103]\n"
    "s_waitcnt lgkmcnt(4)\n"
    "v_mfma_f32_16x16x32_bf16 v[84:87], v[152:155], a[192:195], v[84:87]\n"
    "ds_read_b128 v[136:139], v26 offset:0\n"
    "v_mfma_f32_16x16x32_bf16 v[84:87], v[156:159], a[200:203], v[84:87]\n"
    "ds_read_b128 v[140:143], v26 offset:1024\n"
    "v_mfma_f32_16x16x32_bf16 v[84:87], v[160:163], a[208:211], v[84:87]\n"
    "ds_read_b128 v[144:147], v26 offset:2048\n"
    "v_mfma_f32_16x16x32_bf16 v[84:87], v[164:167], a[216:219], v[84:87]\n"
    "ds_read_b128 v[148:151], v26 offset:3072\n"
    "v_mfma_f32_16x16x32_bf16 v[104:107], v[164:167], a[220:223], v[104:107]\n"
    "buffer_load_dwordx4 a[132:135], v1, s[28:31], s90 offen offset:1024 sc1\n"
    "v_mfma_f32_16x16x32_bf16 v[104:107], v[160:163], a[212:215], v[104:107]\n"
    "v_mfma_f32_16x16x32_bf16 v[104:107], v[156:159], a[204:207], v[104:107]\n"
    "v_mfma_f32_16x16x32_bf16 v[104:107], v[152:155], a[196:199], v[104:107]\n"
    "s_waitcnt lgkmcnt(4)\n"
    "v_mfma_f32_16x16x32_bf16 v[80:83], v[120:123], a[224:227], v[80:83]\n"
    "ds_read_b128 v[152:155], v26 offset:4096\n"
    "v_mfma_f32_16x16x32_bf16 v[80:83], v[124:127], a[232:235], v[80:83]\n"
    "ds_read_b128 v[156:159], v26 offset:5120\n"
    "v_mfma_f32_16x16x32_bf16 v[80:83], v[128:131], a[240:243], v[80:83]\n"
    "ds_read_b128 v[160:163], v26 offset:6144\n"
    "v_mfma_f32_16x16x32_bf16 v[80:83], v[132:135], a[248:251], v[80:83]\n"
    "ds_read_b128 v[164:167], v26 offset:7168\n"
    "v_mfma_f32_16x16x32_bf16 v[100:103], v[132:135], a[252:255], v[100:103]\n"
    "s_waitcnt vmcnt(16)\n"
    "buffer_load_dwordx4 a[136:139], v1, s[28:31], s90 offen offset:2048 sc1\n"
    "v_mfma_f32_16x16x32_bf16 v[100:103], v[128:131], a[244:247], v[100:103]\n"
    "v_lshlrev_b32 v56, 16, v240\n"
    "v_and_b32 v57, 0xffff0000, v240\n"
    "v_mfma_f32_16x16x32_bf16 v[100:103], v[124:127], a[236:239], v[100:103]\n"
    "v_lshlrev_b32 v58, 16, v241\n"
    "v_and_b32 v59, 0xffff0000, v241\n"
    "v_mfma_f32_16x16x32_bf16 v[100:103], v[120:123], a[228:231], v[100:103]\n"
    "s_waitcnt lgkmcnt(4)\n"
    "v_lshlrev_b32 v76, 16, v242\n"
    "v_and_b32 v77, 0xffff0000, v242\n"
    "v_mfma_f32_16x16x32_bf16 v[88:91], v[136:139], a[192:195], v[88:91]\n"
    "ds_read_b128 v[120:123], v26 offset:8192\n"
    "v_lshlrev_b32 v78, 16, v243\n"
    "v_mfma_f32_16x16x32_bf16 v[88:91], v[140:143], a[200:203], v[88:91]\n"
    "ds_read_b128 v[124:127], v26 offset:9216\n"
    "v_and_b32 v79, 0xffff0000, v243\n"
    "v_mfma_f32_16x16x32_bf16 v[88:91], v[144:147], a[208:211], v[88:91]\n"
    "ds_read_b128 v[128:131], v26 offset:10240\n"
    "v_add_f32 v96, v96, v56\n"
    "v_mfma_f32_16x16x32_bf16 v[88:91], v[148:151], a[216:219], v[88:91]\n"
    "buffer_load_dwordx4 a[140:143], v1, s[28:31], s90 offen offset:3072 sc1\n"
    "v_mfma_f32_16x16x32_bf16 v[108:111], v[148:151], a[220:223], v[108:111]\n"
    "s_add_u32 s90, s81, 36864\n"
    "ds_read_b128 v[132:135], v26 offset:11264\n"
    "v_add_f32 v97, v97, v57\n"
    "v_mfma_f32_16x16x32_bf16 v[108:111], v[144:147], a[212:215], v[108:111]\n"
    "v_add_f32 v98, v98, v58\n"
    "v_add_f32 v99, v99, v59\n"
    "v_mfma_f32_16x16x32_bf16 v[108:111], v[140:143], a[204:207], v[108:111]\n"
    "v_add_f32 v116, v116, v76\n"
    "v_add_f32 v117, v117, v77\n"
    "v_mfma_f32_16x16x32_bf16 v[108:111], v[136:139], a[196:199], v[108:111]\n"
    "s_waitcnt lgkmcnt(4)\n"
    "v_add_f32 v118, v118, v78\n"
    "v_add_f32 v119, v119, v79\n"
    "v_mfma_f32_16x16x32_bf16 v[84:87], v[152:155], a[224:227], v[84:87]\n"
    "buffer_load_dwordx4 a[144:147], v1, s[28:31], s90 offen sc1\n"
    "v_mfma_f32_16x16x32_bf16 v[84:87], v[156:159], a[232:235], v[84:87]\n"
    "ds_read_b128 v[136:139], v26 offset:12288\n"
    "ds_read_b128 v[140:143], v26 offset:13312\n"
    "v_mfma_f32_16x16x32_bf16 v[84:87], v[160:163], a[240:243], v[84:87]\n"
    "ds_read_b128 v[144:147], v26 offset:14336\n"
    "v_add_f32 v80, v80, v40\n"
    "v_mfma_f32_16x16x32_bf16 v[84:87], v[164:167], a[248:251], v[84:87]\n"
    "ds_read_b128 v[148:151], v26 offset:15360\n"
    "v_add_f32 v81, v81, v41\n"
    "v_mfma_f32_16x16x32_bf16 v[104:107], v[164:167], a[252:255], v[104:107]\n"
    "v_add_f32 v82, v82, v42\n"
    "v_add_f32 v83, v83, v43\n"
    "v_mfma_f32_16x16x32_bf16 v[104:107], v[160:163], a[244:247], v[104:107]\n"
    "v_cvt_pk_bf16_f32 v168, v80, v81\n"
    "v_cvt_pk_bf16_f32 v169, v82, v83\n"
    "v_mfma_f32_16x16x32_bf16 v[104:107], v[156:159], a[236:239], v[104:107]\n"
    "buffer_load_dwordx4 a[148:151], v1, s[28:31], s90 offen offset:1024 sc1\n"
    "v_mfma_f32_16x16x32_bf16 v[104:107], v[152:155], a[228:231], v[104:107]\n"
    "s_waitcnt lgkmcnt(4)\n"
    "v_add_f32 v100, v100, v60\n"
    "v_add_f32 v101, v101, v61\n"
    "v_mfma_f32_16x16x32_bf16 v[92:95], v[120:123], a[192:195], v[92:95]\n"
    "s_waitcnt vmcnt(14)\n"
    "ds_read_b128 v[152:155], v26 offset:16384\n"
    "v_add_f32 v102, v102, v62\n"
    "v_mfma_f32_16x16x32_bf16 v[92:95], v[124:127], a[200:203], v[92:95]\n"
    "ds_read_b128 v[156:159], v26 offset:17408\n"
    "v_lshlrev_b32 v52, 16, v236\n"
    "v_mfma_f32_16x16x32_bf16 v[92:95], v[128:131], a[208:211], v[92:95]\n"
    "ds_read_b128 v[160:163], v26 offset:18432\n"
    "v_and_b32 v53, 0xffff0000, v236\n"
    "v_mfma_f32_16x16x32_bf16 v[92:95], v[132:135], a[216:219], v[92:95]\n"
    "ds_read_b128 v[164:167], v26 offset:19456\n"
    "v_lshlrev_b32 v54, 16, v237\n"
    "v_mfma_f32_16x16x32_bf16 v[112:115], v[132:135], a[220:223], v[112:115]\n"
    "buffer_load_dwordx4 a[152:155], v1, s[28:31], s90 offen offset:2048 sc1\n"
    "v_mfma_f32_16x16x32_bf16 v[112:115], v[128:131], a[212:215], v[112:115]\n"
    "v_and_b32 v55, 0xffff0000, v237\n"
    "v_lshlrev_b32 v72, 16, v238\n"
    "v_mfma_f32_16x16x32_bf16 v[112:115], v[124:127], a[204:207], v[112:115]\n"
    "v_and_b32 v73, 0xffff0000, v238\n"
    "v_lshlrev_b32 v74, 16, v239\n"
    "v_mfma_f32_16x16x32_bf16 v[112:115], v[120:123], a[196:199], v[112:115]\n"
    "s_waitcnt lgkmcnt(4)\n"
    "v_and_b32 v75, 0xffff0000, v239\n"
    "v_add_f32 v103, v103, v63\n"
    "v_mfma_f32_16x16x32_bf16 v[88:91], v[136:139], a[224:227], v[88:91]\n"
    "ds_read_b128 v[120:123], v26 offset:20480\n"
    "v_cvt_pk_bf16_f32 v170, -v100, -v101\n"
    "v_mfma_f32_16x16x32_bf16 v[88:91], v[140:143], a[232:235], v[88:91]\n"
    "ds_read_b128 v[124:127], v26 offset:21504\n"
    "v_cvt_pk_bf16_f32 v171, -v102, -v103\n"
    "v_mfma_f32_16x16x32_bf16 v[88:91], v[144:147], a[240:243], v[88:91]\n"
    "buffer_load_dwordx4 a[156:159], v1, s[28:31], s90 offen offset:3072 sc1\n"
    "v_mfma_f32_16x16x32_bf16 v[88:91], v[148:151], a[248:251], v[88:91]\n"
    "s_add_u32 s90, s81, 40960\n"
    "ds_read_b128 v[128:131], v26 offset:22528\n"
    "ds_read_b128 v[132:135], v26 offset:23552\n"
    "v_mfma_f32_16x16x32_bf16 v[108:111], v[148:151], a[252:255], v[108:111]\n"
    "buffer_store_dwordx4 v[168:171], v1, s[28:31], s74 offen sc1\n"
    "v_mfma_f32_16x16x32_bf16 v[108:111], v[144:147], a[244:247], v[108:111]\n"
    "v_add_f32 v84, v84, v44\n"
    "v_add_f32 v85, v85, v45\n"
    "v_mfma_f32_16x16x32_bf16 v[108:111], v[140:143], a[236:239], v[108:111]\n"
    "v_add_f32 v86, v86, v46\n"
    "v_add_f32 v87, v87, v47\n"
    "v_mfma_f32_16x16x32_bf16 v[108:111], v[136:139], a[228:231], v[108:111]\n"
    "s_waitcnt lgkmcnt(4)\n"
    "buffer_load_dwordx4 a[160:163], v1, s[28:31], s90 offen sc1\n"
    "v_mfma_f32_16x16x32_bf16 v[96:99], v[152:155], a[192:195], v[96:99]\n"
    "ds_read_b128 v[136:139], v26 offset:24576\n"
    "v_cvt_pk_bf16_f32 v172, v84, v85\n"
    "v_mfma_f32_16x16x32_bf16 v[96:99], v[156:159], a[200:203], v[96:99]\n"
    "ds_read_b128 v[140:143], v26 offset:25600\n"
    "v_cvt_pk_bf16_f32 v173, v86, v87\n"
    "v_mfma_f32_16x16x32_bf16 v[96:99], v[160:163], a[208:211], v[96:99]\n"
    "ds_read_b128 v[144:147], v26 offset:26624\n"
    "v_add_f32 v104, v104, v64\n"
    "v_mfma_f32_16x16x32_bf16 v[96:99], v[164:167], a[216:219], v[96:99]\n"
    "ds_read_b128 v[148:151], v26 offset:27648\n"
    "v_add_f32 v105, v105, v65\n"
    "v_mfma_f32_16x16x32_bf16 v[116:119], v[164:167], a[220:223], v[116:119]\n"
    "v_add_f32 v106, v106, v66\n"
    "v_add_f32 v107, v107, v67\n"
    "v_mfma_f32_16x16x32_bf16 v[116:119], v[160:163], a[212:215], v[116:119]\n"
    "buffer_load_dwordx4 a[164:167], v1, s[28:31], s90 offen offset:1024 sc1\n"
    "v_mfma_f32_16x16x32_bf16 v[116:119], v[156:159], a[204:207], v[116:119]\n"
    "v_cvt_pk_bf16_f32 v174, -v104, -v105\n"
    "v_cvt_pk_bf16_f32 v175, -v106, -v107\n"
    "v_mfma_f32_16x16x32_bf16 v[116:119], v[152:155], a[196:199], v[116:119]\n"
    "s_waitcnt lgkmcnt(4)\n"
    "buffer_store_dwordx4 v[172:175], v1, s[28:31], s74 offen offset:1024 sc1\n"
    "v_mfma_f32_16x16x32_bf16 v[92:95], v[120:123], a[224:227], v[92:95]\n"
    "v_add_f32 v88, v88, v48\n"
    "v_add_f32 v89, v89, v49\n"
    "v_mfma_f32_16x16x32_bf16 v[92:95], v[124:127], a[232:235], v[92:95]\n"
    "v_add_f32 v90, v90, v50\n"
    "v_add_f32 v91, v91, v51\n"
    "v_mfma_f32_16x16x32_bf16 v[92:95], v[128:131], a[240:243], v[92:95]\n"
    "v_cvt_pk_bf16_f32 v168, v88, v89\n"
    "v_cvt_pk_bf16_f32 v169, v90, v91\n"
    "v_mfma_f32_16x16x32_bf16 v[92:95], v[132:135], a[248:251], v[92:95]\n"
    "buffer_load_dwordx4 a[168:171], v1, s[28:31], s90 offen offset:2048 sc1\n"
    "v_mfma_f32_16x16x32_bf16 v[112:115], v[132:135], a[252:255], v[112:115]\n"
    "v_add_f32 v108, v108, v68\n"
    "v_add_f32 v109, v109, v69\n"
    "v_mfma_f32_16x16x32_bf16 v[112:115], v[128:131], a[244:247], v[112:115]\n"
    "v_add_f32 v110, v110, v70\n"
    "v_add_f32 v111, v111, v71\n"
    "v_mfma_f32_16x16x32_bf16 v[112:115], v[124:127], a[236:239], v[112:115]\n"
    "v_cvt_pk_bf16_f32 v170, -v108, -v109\n"
    "v_cvt_pk_bf16_f32 v171, -v110, -v111\n"
    "v_mfma_f32_16x16x32_bf16 v[112:115], v[120:123], a[228:231], v[112:115]\n"
    "s_waitcnt lgkmcnt(0)\n"
    "buffer_store_dwordx4 v[168:171], v1, s[28:31], s74 offen offset:2048 sc1\n"
    "v_mfma_f32_16x16x32_bf16 v[96:99], v[136:139], a[224:227], v[96:99]\n"
    "v_add_f32 v92, v92, v52\n"
    "v_add_f32 v93, v93, v53\n"
    "v_mfma_f32_16x16x32_bf16 v[96:99], v[140:143], a[232:235], v[96:99]\n"
    "buffer_load_dwordx4 a[172:175], v1, s[28:31], s90 offen offset:3072 sc1\n"
    "v_mfma_f32_16x16x32_bf16 v[96:99], v[144:147], a[240:243], v[96:99]\n"
    "v_add_f32 v94, v94, v54\n"
    "v_add_f32 v95, v95, v55\n"
    "v_mfma_f32_16x16x32_bf16 v[96:99], v[148:151], a[248:251], v[96:99]\n"
    "v_cvt_pk_bf16_f32 v172, v92, v93\n"
    "v_cvt_pk_bf16_f32 v173, v94, v95\n"
    "v_mfma_f32_16x16x32_bf16 v[116:119], v[148:151], a[252:255], v[116:119]\n"
    "v_add_f32 v112, v112, v72\n"
    "v_add_f32 v113, v113, v73\n"
    "v_mfma_f32_16x16x32_bf16 v[116:119], v[144:147], a[244:247], v[116:119]\n"
    "v_add_f32 v114, v114, v74\n"
    "v_add_f32 v115, v115, v75\n"
    "v_mfma_f32_16x16x32_bf16 v[116:119], v[140:143], a[236:239], v[116:119]\n"
    "v_cvt_pk_bf16_f32 v174, -v112, -v113\n"
    "v_cvt_pk_bf16_f32 v175, -v114, -v115\n"
    "v_mfma_f32_16x16x32_bf16 v[116:119], v[136:139], a[228:231], v[116:119]\n"
    "buffer_store_dwordx4 v[172:175], v1, s[28:31], s74 offen offset:3072 sc1\n"
    "v_cvt_pk_bf16_f32 v168, v96, v97\n"
    "v_cvt_pk_bf16_f32 v169, v98, v99\n"
    "s_nop 4\n"
    "v_cvt_pk_bf16_f32 v170, -v116, -v117\n"
    "v_cvt_pk_bf16_f32 v171, -v118, -v119\n"
    "buffer_store_dwordx4 v[168:171], v31, s[28:31], s74 offen sc1\n"
    "s_add_u32 s90, s81, 45056\n"
    "buffer_load_dwordx4 a[176:179], v1, s[28:31], s90 offen sc1\n"
    "buffer_load_dwordx4 a[180:183], v1, s[28:31], s90 offen offset:1024 sc1\n"
    "buffer_load_dwordx4 a[184:187], v1, s[28:31], s90 offen offset:2048 sc1\n"
    "buffer_load_dwordx4 a[188:191], v1, s[28:31], s90 offen offset:3072 sc1\n"
    "s_add_u32 s90, s81, 49152\n"
    "buffer_load_dwordx4 a[192:195], v1, s[28:31], s90 offen sc1\n"
    "buffer_load_dwordx4 a[196:199], v1, s[28:31], s90 offen offset:1024 sc1\n"
    "s_add_u32 s66, s68, 3\n"
    "s_mov_b32 s67, s66\n"
    "v_mov_b32 v36, s66\n"
    "v_mov_b32 v37, s66\n"
    "s_add_u32 s91, s56, 1\n"
    "s_and_b32 s91, s91, 3\n"
    "s_mul_i32 s91, s91, 15360\n"
    "s_add_u32 s91, s91, 98304\n"
    "v_add_u32 v104, s91, v1\n"
    "s_add_u32 s91, s56, 2\n"
    "s_and_b32 s91, s91, 3\n"
    "s_mul_i32 s91, s91, 15360\n"
    "s_add_u32 s91, s91, 103424\n"
    "v_add_u32 v105, s91, v1\n"
    "s_add_u32 s91, s56, 3\n"
    "s_and_b32 s91, s91, 3\n"
    "s_mul_i32 s91, s91, 15360\n"
    "s_add_u32 s91, s91, 108544\n"
    "v_add_u32 v106, s91, v1\n"
    "s_add_u32 s91, s56, 4\n"
    "s_and_b32 s91, s91, 3\n"
    "s_mul_i32 s91, s91, 15360\n"
    "s_add_u32 s91, s91, 98304\n"
    "v_add_u32 v107, s91, v1\n"
    "s_cmp_eq_u32 s64, 0\n"
    "s_cselect_b32 s34, s34, 0\n"
    "s_cselect_b32 s38, s38, 0\n"
    "s_cselect_b32 s42, s42, 0\n"
    "s_cselect_b32 s46, s46, 0\n"
    "s_mov_b32 s87, 0\n"
    "s_waitcnt vmcnt(6)\n"
    "s_barrier\n"
    "s_mov_b64 exec, 0x3\n"
    "buffer_atomic_swap_x2 v[36:37], v29, s[52:55], s9 offen sc1\n"
    "s_mov_b64 exec, -1\n"
    "s_cmp_lg_u32 s64, 0\n"
    "s_cbranch_scc1 L_l0_29\n"
    "v_lshlrev_b32 v113, 2, v0\n"
    "v_add_u32 v113, 159744, v113\n"
    "ds_read_b32 v108, v113 offset:0\n"
    "ds_read_b32 v109, v113 offset:768\n"
    "ds_read_b32 v110, v113 offset:1536\n"
    "ds_read_b32 v111, v113 offset:2304\n"
    "ds_read_b32 v112, v113 offset:3072\n"
    "v_cmp_gt_u32 vcc, 192, v0\n"
    "s_waitcnt lgkmcnt(3)\n"
    "v_add_f32 v108, v108, v109\n"
    "s_waitcnt lgkmcnt(2)\n"
    "v_add_f32 v108, v108, v110\n"
    "s_waitcnt lgkmcnt(1)\n"
    "v_add_f32 v108, v108, v111\n"
    "s_waitcnt lgkmcnt(0)\n"
    "v_add_f32 v108, v108, v112\n"
    "s_cbranch_vccz L_cw_30\n"
    "ds_write_b32 v113, v108\n"
    "s_waitcnt lgkmcnt(0)\n"
    "L_cw_30:\n"
    "L_l0_29:\n"
    "s_add_u32 m0, s86, 0\n"
    "s_add_u32 s88, s83, 0\n"
    "buffer_load_dwordx4 v1, s[28:31], s88 offen sc1 lds\n"
    "buffer_load_dwordx4 v1, s[28:31], s88 offen offset:1024 sc1 lds\n"
    "buffer_load_dwordx4 v1, s[28:31], s88 offen offset:2048 sc1 lds\n"
    "buffer_load_dwordx4 v1, s[28:31], s88 offen offset:3072 sc1 lds\n"
    "s_waitcnt vmcnt(10)\n"
    "v_mov_b64 v[40:41], 0\n"
    "s_mov_b64 exec, 0x3\n"
    "buffer_atomic_or_x2 v[40:41], v28, s[52:55], s13 offen sc0 sc1\n"
    "s_mov_b64 exec, -1\n"
    "s_add_u32 m0, s86, 16384\n"
    "s_add_u32 s88, s83, 16384\n"
    "buffer_load_dwordx4 v1, s[28:31], s88 offen sc1 lds\n"
    "buffer_load_dwordx4 v1, s[28:31], s88 offen offset:1024 sc1 lds\n"
    "buffer_load_dwordx4 v1, s[28:31], s88 offen offset:2048 sc1 lds\n"
    "buffer_load_dwordx4 v1, s[28:31], s88 offen offset:3072 sc1 lds\n"
    "s_add_u32 m0, s86, 32768\n"
    "s_add_u32 s88, s83, 32768\n"
    "buffer_load_dwordx4 v1, s[28:31], s88 offen sc1 lds\n"
    "buffer_load_dwordx4 v1, s[28:31], s88 offen offset:1024 sc1 lds\n"
    "buffer_load_dwordx4 v1, s[28:31], s88 offen offset:2048 sc1 lds\n"
    "buffer_load_dwordx4 v1, s[28:31], s88 offen offset:3072 sc1 lds\n"
    "s_waitcnt vmcnt(8)\n"
    "v_cmp_ne_u64 vcc, s[66:67], v[40:41]\n"
    "s_nop 0\n"
    "s_and_b64 vcc, vcc, 3\n"
    "s_cbranch_scc0 L_ok_31\n"
    "v_mov_b64 v[40:41], 0\n"
    "s_mov_b64 exec, 0x3\n"
    "buffer_atomic_or_x2 v[40:41], v28, s[52:55], s13 offen sc0 sc1\n"
    "s_mov_b64 exec, -1\n"
    "s_branch L_spin_32\n"
    "L_ok_31:\n"
    "buffer_load_dwordx4 v[136:139], v1, s[28:31], s16 offen sc1\n"
    "s_nop 7\n"
    "buffer_load_dwordx4 v[140:143], v1, s[28:31], s16 offen offset:1024 sc1\n"
    "s_nop 7\n"
    "buffer_load_dwordx4 v[144:147], v1, s[28:31], s16 offen offset:2048 sc1\n"
    "s_nop 7\n"
    "buffer_load_dwordx4 v[148:151], v1, s[28:31], s16 offen offset:3072 sc1\n"
    "s_nop 7\n"
    "s_add_u32 s89, s16, 4096\n"
    "buffer_load_dwordx4 v[152:155], v1, s[28:31], s89 offen sc1\n"
    "s_nop 7\n"
    "buffer_load_dwordx4 v[156:159], v1, s[28:31], s89 offen offset:1024 sc1\n"
    "s_nop 7\n"
    "buffer_load_dwordx4 v[160:163], v1, s[28:31], s89 offen offset:2048 sc1\n"
    "s_nop 7\n"
    "buffer_load_dwordx4 v[164:167], v1, s[28:31], s89 offen offset:3072 sc1\n"
    "s_nop 7\n"
    "s_add_u32 s89, s16, 8192\n"
    "buffer_load_dwordx4 v[168:171], v1, s[28:31], s89 offen sc1\n"
    "s_nop 7\n"
    "buffer_load_dwordx4 v[172:175], v1, s[28:31], s89 offen offset:1024 sc1\n"
    "s_nop 7\n"
    "buffer_load_dwordx4 v[176:179], v1, s[28:31], s17 offen sc1\n"
    "s_waitcnt vmcnt(10)\n"
    "v_mfma_f32_16x16x32_bf16 v[40:43], a[0:3], v[136:139], 0\n"
    "v_mfma_f32_16x16x32_bf16 v[44:47], a[4:7], v[136:139], 0\n"
    "buffer_load_dwordx4 v[180:183], v1, s[28:31], s17 offen offset:1024 sc1\n"
    "v_mfma_f32_16x16x32_bf16 v[48:51], a[8:11], v[136:139], 0\n"
    "v_mfma_f32_16x16x32_bf16 v[52:55], a[12:15], v[136:139], 0\n"
    "v_mfma_f32_16x16x32_bf16 v[56:59], a[16:19], v[136:139], 0\n"
    "s_waitcnt vmcnt(10)\n"
    "v_mfma_f32_16x16x32_bf16 v[40:43], a[20:23], v[140:143], v[40:43]\n"
    "buffer_load_dwordx4 v[184:187], v1, s[28:31], s17 offen offset:2048 sc1\n"
    "v_mfma_f32_16x16x32_bf16 v[44:47], a[24:27], v[140:143], v[44:47]\n"
    "v_mfma_f32_16x16x32_bf16 v[48:51], a[28:31], v[140:143], v[48:51]\n"
    "v_mfma_f32_16x16x32_bf16 v[52:55], a[32:35], v[140:143], v[52:55]\n"
    "v_mfma_f32_16x16x32_bf16 v[56:59], a[36:39], v[140:143], v[56:59]\n"
    "s_waitcnt vmcnt(10)\n"
    "v_mfma_f32_16x16x32_bf16 v[40:43], a[40:43], v[144:147], v[40:43]\n"
    "buffer_load_dwordx4 v[188:191], v1, s[28:31], s17 offen offset:3072 sc1\n"
    "v_mfma_f32_16x16x32_bf16 v[44:47], a[44:47], v[144:147], v[44:47]\n"
    "v_mfma_f32_16x16x32_bf16 v[48:51], a[48:51], v[144:147], v[48:51]\n"
    "v_mfma_f32_16x16x32_bf16 v[52:55], a[52:55], v[144:147], v[52:55]\n"
    "v_mfma_f32_16x16x32_bf16 v[56:59], a[56:59], v[144:147], v[56:59]\n"
    "s_waitcnt vmcnt(10)\n"
    "s_add_u32 s89, s17, 4096\n"
    "v_mfma_f32_16x16x32_bf16 v[40:43], a[60:63], v[148:151], v[40:43]\n"
    "buffer_load_dwordx4 v[192:195], v1, s[28:31], s89 offen sc1\n"
    "v_mfma_f32_16x16x32_bf16 v[44:47], a[64:67], v[148:151], v[44:47]\n"
    "v_mfma_f32_16x16x32_bf16 v[48:51], a[68:71], v[148:151], v[48:51]\n"
    "v_mfma_f32_16x16x32_bf16 v[52:55], a[72:75], v[148:151], v[52:55]\n"
    "v_mfma_f32_16x16x32_bf16 v[56:59], a[76:79], v[148:151], v[56:59]\n"
    "s_waitcnt vmcnt(10)\n"
    "v_mfma_f32_16x16x32_bf16 v[40:43], a[80:83], v[152:155], v[40:43]\n"
    "buffer_load_dwordx4 v[196:199], v1, s[28:31], s89 offen offset:1024 sc1\n"
    "v_mfma_f32_16x16x32_bf16 v[44:47], a[84:87], v[152:155], v[44:47]\n"
    "v_mfma_f32_16x16x32_bf16 v[48:51], a[88:91], v[152:155], v[48:51]\n"
    "v_mfma_f32_16x16x32_bf16 v[52:55], a[92:95], v[152:155], v[52:55]\n"
    "v_mfma_f32_16x16x32_bf16 v[56:59], a[96:99], v[152:155], v[56:59]\n"
    "s_waitcnt vmcnt(10)\n"
    "buffer_load_dwordx4 v[200:203], v1, s[28:31], s89 offen offset:2048 sc1\n"
    "v_mfma_f32_16x16x32_bf16 v[40:43], a[100:103], v[156:159], v[40:43]\n"
    "v_mfma_f32_16x16x32_bf16 v[44:47], a[104:107], v[156:159], v[44:47]\n"
    "v_mfma_f32_16x16x32_bf16 v[48:51], a[108:111], v[156:159], v[48:51]\n"
    "v_mfma_f32_16x16x32_bf16 v[52:55], a[112:115], v[156:159], v[52:55]\n"
    "v_mfma_f32_16x16x32_bf16 v[56:59], a[116:119], v[156:159], v[56:59]\n"
    "s_waitcnt vmcnt(10)\n"
    "buffer_load_dwordx4 v[204:207], v1, s[28:31], s89 offen offset:3072 sc1\n"
    "v_mfma_f32_16x16x32_bf16 v[40:43], a[120:123], v[160:163], v[40:43]\n"
    "v_mfma_f32_16x16x32_bf16 v[44:47], a[124:127], v[160:163], v[44:47]\n"
    "v_mfma_f32_16x16x32_bf16 v[48:51], a[128:131], v[160:163], v[48:51]\n"
    "v_mfma_f32_16x16x32_bf16 v[52:55], a[132:135], v[160:163], v[52:55]\n"
    "s_add_u32 s89, s17, 8192\n"
    "v_mfma_f32_16x16x32_bf16 v[56:59], a[136:139], v[160:163], v[56:59]\n"
    "s_waitcnt vmcnt(10)\n"
    "buffer_load_dwordx4 v[208:211], v1, s[28:31], s89 offen sc1\n"
    "v_mfma_f32_16x16x32_bf16 v[40:43], a[140:143], v[164:167], v[40:43]\n"
    "v_mfma_f32_16x16x32_bf16 v[44:47], a[144:147], v[164:167], v[44:47]\n"
    "v_mfma_f32_16x16x32_bf16 v[48:51], a[148:151], v[164:167], v[48:51]\n"
    "v_mfma_f32_16x16x32_bf16 v[52:55], a[152:155], v[164:167], v[52:55]\n"
    "v_mfma_f32_16x16x32_bf16 v[56:59], a[156:159], v[164:167], v[56:59]\n"
    "s_waitcnt vmcnt(10)\n"
    "buffer_load_dwordx4 v[212:215], v1, s[28:31], s89 offen offset:1024 sc1\n"
    "v_mfma_f32_16x16x32_bf16 v[40:43], a[160:163], v[168:171], v[40:43]\n"
    "v_mfma_f32_16x16x32_bf16 v[44:47], a[164:167], v[168:171], v[44:47]\n"
    "v_mfma_f32_16x16x32_bf16 v[48:51], a[168:171], v[168:171], v[48:51]\n"
    "v_mfma_f32_16x16x32_bf16 v[52:55], a[172:175], v[168:171], v[52:55]\n"
    "buffer_load_dwordx4 v[216:219], v1, s[28:31], s18 offen sc1\n"
    "v_mfma_f32_16x16x32_bf16 v[56:59], a[176:179], v[168:171], v[56:59]\n"
    "s_waitcnt vmcnt(11)\n"
    "v_mfma_f32_16x16x32_bf16 v[40:43], a[180:183], v[172:175], v[40:43]\n"
    "v_mfma_f32_16x16x32_bf16 v[44:47], a[184:187], v[172:175], v[44:47]\n"
    "v_mfma_f32_16x16x32_bf16 v[48:51], a[188:191], v[172:175], v[48:51]\n"
    "v_mfma_f32_16x16x32_bf16 v[52:55], a[192:195], v[172:175], v[52:55]\n"
    "buffer_load_dwordx4 v[220:223], v1, s[28:31], s18 offen offset:1024 sc1\n"
    "v_mfma_f32_16x16x32_bf16 v[56:59], a[196:199], v[172:175], v[56:59]\n"
    "s_waitcnt vmcnt(11)\n"
    "v_mfma_f32_16x16x32_bf16 v[60:63], a[0:3], v[176:179], 0\n"
    "s_waitcnt vmcnt(10)\n"
    "v_mfma_f32_16x16x32_bf16 v[60:63], a[20:23], v[180:183], v[60:63]\n"
    "v_mfma_f32_16x16x32_bf16 v[64:67], a[4:7], v[176:179], 0\n"
    "v_mfma_f32_16x16x32_bf16 v[64:67], a[24:27], v[180:183], v[64:67]\n"
    "buffer_load_dwordx4 v[224:227], v1, s[28:31], s18 offen offset:2048 sc1\n"
    "v_mfma_f32_16x16x32_bf16 v[68:71], a[8:11], v[176:179], 0\n"
    "ds_write_b128 v104, v[40:43] offset:0\n"
    "v_mfma_f32_16x16x32_bf16 v[68:71], a[28:31], v[180:183], v[68:71]\n"
    "v_mfma_f32_16x16x32_bf16 v[72:75], a[12:15], v[176:179], 0\n"
    "v_mfma_f32_16x16x32_bf16 v[72:75], a[32:35], v[180:183], v[72:75]\n"
    "ds_write_b128 v104, v[44:47] offset:1024\n"
    "v_mfma_f32_16x16x32_bf16 v[76:79], a[16:19], v[176:179], 0\n"
    "buffer_load_dwordx4 v[228:231], v1, s[28:31], s18 offen offset:3072 sc1\n"
    "v_mfma_f32_16x16x32_bf16 v[76:79], a[36:39], v[180:183], v[76:79]\n"
    "s_waitcnt vmcnt(11)\n"
    "v_mfma_f32_16x16x32_bf16 v[60:63], a[40:43], v[184:187], v[60:63]\n"
    "s_waitcnt vmcnt(10)\n"
    "ds_write_b128 v104, v[48:51] offset:2048\n"
    "v_mfma_f32_16x16x32_bf16 v[60:63], a[60:63], v[188:191], v[60:63]\n"
    "s_add_u32 s89, s18, 4096\n"
    "v_mfma_f32_16x16x32_bf16 v[64:67], a[64:67], v[188:191], v[64:67]\n"
    "buffer_load_dwordx4 v[232:235], v1, s[28:31], s89 offen sc1\n"
    "v_mfma_f32_16x16x32_bf16 v[64:67], a[44:47], v[184:187], v[64:67]\n"
    "ds_write_b128 v104, v[52:55] offset:3072\n"
    "v_mfma_f32_16x16x32_bf16 v[68:71], a[48:51], v[184:187], v[68:71]\n"
    "v_mfma_f32_16x16x32_bf16 v[68:71], a[68:71], v[188:191], v[68:71]\n"
    "v_mfma_f32_16x16x32_bf16 v[72:75], a[72:75], v[188:191], v[72:75]\n"
    "ds_write_b128 v104, v[56:59] offset:4096\n"
    "v_mfma_f32_16x16x32_bf16 v[72:75], a[52:55], v[184:187], v[72:75]\n"
    "buffer_load_dwordx4 v[236:239], v1, s[28:31], s89 offen offset:1024 sc1\n"
    "v_mfma_f32_16x16x32_bf16 v[76:79], a[56:59], v[184:187], v[76:79]\n"
    "v_mfma_f32_16x16x32_bf16 v[76:79], a[76:79], v[188:191], v[76:79]\n"
    "s_waitcnt vmcnt(11)\n"
    "v_mfma_f32_16x16x32_bf16 v[60:63], a[80:83], v[192:195], v[60:63]\n"
    "s_waitcnt vmcnt(10)\n"
    "v_mfma_f32_16x16x32_bf16 v[60:63], a[100:103], v[196:199], v[60:63]\n"
    "v_mfma_f32_16x16x32_bf16 v[64:67], a[104:107], v[196:199], v[64:67]\n"
    "buffer_load_dwordx4 v[240:243], v1, s[28:31], s89 offen offset:2048 sc1\n"
    "v_mfma_f32_16x16x32_bf16 v[64:67], a[84:87], v[192:195], v[64:67]\n"
    "v_mfma_f32_16x16x32_bf16 v[68:71], a[88:91], v[192:195], v[68:71]\n"
    "v_mfma_f32_16x16x32_bf16 v[68:71], a[108:111], v[196:199], v[68:71]\n"
    "v_mfma_f32_16x16x32_bf16 v[72:75], a[112:115], v[196:199], v[72:75]\n"
    "v_mfma_f32_16x16x32_bf16 v[72:75], a[92:95], v[192:195], v[72:75]\n"
    "buffer_load_dwordx4 v[244:247], v1, s[28:31], s89 offen offset:3072 sc1\n"
    "v_mfma_f32_16x16x32_bf16 v[76:79], a[96:99], v[192:195], v[76:79]\n"
    "v_mfma_f32_16x16x32_bf16 v[76:79], a[116:119], v[196:199], v[76:79]\n"
    "s_waitcnt vmcnt(11)\n"
    "v_mfma_f32_16x16x32_bf16 v[60:63], a[120:123], v[200:203], v[60:63]\n"
    "s_waitcnt vmcnt(10)\n"
    "s_add_u32 s89, s18, 8192\n"
    "v_mfma_f32_16x16x32_bf16 v[60:63], a[140:143], v[204:207], v[60:63]\n"
    "buffer_load_dwordx4 v[248:251], v1, s[28:31], s89 offen sc1\n"
    "v_mfma_f32_16x16x32_bf16 v[64:67], a[144:147], v[204:207], v[64:67]\n"
    "v_mfma_f32_16x16x32_bf16 v[64:67], a[124:127], v[200:203], v[64:67]\n"
    "v_mfma_f32_16x16x32_bf16 v[68:71], a[128:131], v[200:203], v[68:71]\n"
    "v_mfma_f32_16x16x32_bf16 v[68:71], a[148:151], v[204:207], v[68:71]\n"
    "v_mfma_f32_16x16x32_bf16 v[72:75], a[152:155], v[204:207], v[72:75]\n"
    "buffer_load_dwordx4 v[252:255], v1, s[28:31], s89 offen offset:1024 sc1\n"
    "v_mfma_f32_16x16x32_bf16 v[72:75], a[132:135], v[200:203], v[72:75]\n"
    "v_mfma_f32_16x16x32_bf16 v[76:79], a[136:139], v[200:203], v[76:79]\n"
    "v_mfma_f32_16x16x32_bf16 v[76:79], a[156:159], v[204:207], v[76:79]\n"
    "s_waitcnt vmcnt(11)\n"
    "v_mfma_f32_16x16x32_bf16 v[60:63], a[160:163], v[208:211], v[60:63]\n"
    "s_waitcnt vmcnt(10)\n"
    "v_mfma_f32_16x16x32_bf16 v[60:63], a[180:183], v[212:215], v[60:63]\n"
    "buffer_load_dwordx4 v[136:139], v1, s[28:31], s19 offen sc1\n"
    "v_mfma_f32_16x16x32_bf16 v[64:67], a[184:187], v[212:215], v[64:67]\n"
    "v_mfma_f32_16x16x32_bf16 v[64:67], a[164:167], v[208:211], v[64:67]\n"
    "v_mfma_f32_16x16x32_bf16 v[68:71], a[168:171], v[208:211], v[68:71]\n"
    "v_mfma_f32_16x16x32_bf16 v[68:71], a[188:191], v[212:215], v[68:71]\n"
    "v_mfma_f32_16x16x32_bf16 v[72:75], a[192:195], v[212:215], v[72:75]\n"
    "buffer_load_dwordx4 v[140:143], v1, s[28:31], s19 offen offset:1024 sc1\n"
    "v_mfma_f32_16x16x32_bf16 v[72:75], a[172:175], v[208:211], v[72:75]\n"
    "v_mfma_f32_16x16x32_bf16 v[76:79], a[176:179], v[208:211], v[76:79]\n"
    "v_mfma_f32_16x16x32_bf16 v[76:79], a[196:199], v[212:215], v[76:79]\n"
    "s_waitcnt vmcnt(11)\n"
    "v_mfma_f32_16x16x32_bf16 v[40:43], a[0:3], v[216:219], 0\n"
    "s_waitcnt vmcnt(10)\n"
    "buffer_load_dwordx4 v[144:147], v1, s[28:31], s19 offen offset:2048 sc1\n"
    "v_mfma_f32_16x16x32_bf16 v[40:43], a[20:23], v[220:223], v[40:43]\n"
    "v_mfma_f32_16x16x32_bf16 v[44:47], a[4:7], v[216:219], 0\n"
    "v_mfma_f32_16x16x32_bf16 v[44:47], a[24:27], v[220:223], v[44:47]\n"
    "v_mfma_f32_16x16x32_bf16 v[48:51], a[8:11], v[216:219], 0\n"
    "ds_write_b128 v105, v[60:63] offset:0\n"
    "v_mfma_f32_16x16x32_bf16 v[48:51], a[28:31], v[220:223], v[48:51]\n"
    "buffer_load_dwordx4 v[148:151], v1, s[28:31], s19 offen offset:3072 sc1\n"
    "v_mfma_f32_16x16x32_bf16 v[52:55], a[12:15], v[216:219], 0\n"
    "v_mfma_f32_16x16x32_bf16 v[52:55], a[32:35], v[220:223], v[52:55]\n"
    "ds_write_b128 v105, v[64:67] offset:1024\n"
    "v_mfma_f32_16x16x32_bf16 v[56:59], a[16:19], v[216:219], 0\n"
    "v_mfma_f32_16x16x32_bf16 v[56:59], a[36:39], v[220:223], v[56:59]\n"
    "s_waitcnt vmcnt(11)\n"
    "s_add_u32 s89, s19, 4096\n"
    "v_mfma_f32_16x16x32_bf16 v[40:43], a[40:43], v[224:227], v[40:43]\n"
    "s_waitcnt vmcnt(10)\n"
    "buffer_load_dwordx4 v[152:155], v1, s[28:31], s89 offen sc1\n"
    "v_mfma_f32_16x16x32_bf16 v[40:43], a[60:63], v[228:231], v[40:43]\n"
    "ds_write_b128 v105, v[68:71] offset:2048\n"
    "v_mfma_f32_16x16x32_bf16 v[44:47], a[64:67], v[228:231], v[44:47]\n"
    "v_mfma_f32_16x16x32_bf16 v[44:47], a[44:47], v[224:227], v[44:47]\n"
    "ds_write_b128 v105, v[72:75] offset:3072\n"
    "v_mfma_f32_16x16x32_bf16 v[48:51], a[48:51], v[224:227], v[48:51]\n"
    "v_mfma_f32_16x16x32_bf16 v[48:51], a[68:71], v[228:231], v[48:51]\n"
    "buffer_load_dwordx4 v[156:159], v1, s[28:31], s89 offen offset:1024 sc1\n"
    "v_mfma_f32_16x16x32_bf16 v[52:55], a[72:75], v[228:231], v[52:55]\n"
    "ds_write_b128 v105, v[76:79] offset:4096\n"
    "v_mfma_f32_16x16x32_bf16 v[52:55], a[52:55], v[224:227], v[52:55]\n"
    "v_mfma_f32_16x16x32_bf16 v[56:59], a[56:59], v[224:227], v[56:59]\n"
    "v_mfma_f32_16x16x32_bf16 v[56:59], a[76:79], v[228:231], v[56:59]\n"
    "s_waitcnt vmcnt(11)\n"
    "buffer_load_dwordx4 v[160:163], v1, s[28:31], s89 offen offset:2048 sc1\n"
    "v_mfma_f32_16x16x32_bf16 v[40:43], a[80:83], v[232:235], v[40:43]\n"
    "s_waitcnt vmcnt(11)\n"
    "v_mfma_f32_16x16x32_bf16 v[40:43], a[100:103], v[236:239], v[40:43]\n"
    "v_mfma_f32_16x16x32_bf16 v[44:47], a[104:107], v[236:239], v[44:47]\n"
    "v_mfma_f32_16x16x32_bf16 v[44:47], a[84:87], v[232:235], v[44:47]\n"
    "v_mfma_f32_16x16x32_bf16 v[48:51], a[88:91], v[232:235], v[48:51]\n"
    "buffer_load_dwordx4 v[164:167], v1, s[28:31], s89 offen offset:3072 sc1\n"
    "v_mfma_f32_16x16x32_bf16 v[48:51], a[108:111], v[236:239], v[48:51]\n"
    "v_mfma_f32_16x16x32_bf16 v[52:55], a[112:115], v[236:239], v[52:55]\n"
    "v_mfma_f32_16x16x32_bf16 v[52:55], a[92:95], v[232:235], v[52:55]\n"
    "v_mfma_f32_16x16x32_bf16 v[56:59], a[96:99], v[232:235], v[56:59]\n"
    "s_add_u32 s89, s19, 8192\n"
    "v_mfma_f32_16x16x32_bf16 v[56:59], a[116:119], v[236:239], v[56:59]\n"
    "s_waitcnt vmcnt(11)\n"
    "buffer_load_dwordx4 v[168:171], v1, s[28:31], s89 offen sc1\n"
    "v_mfma_f32_16x16x32_bf16 v[40:43], a[120:123], v[240:243], v[40:43]\n"
    "s_waitcnt vmcnt(11)\n"
    "v_mfma_f32_16x16x32_bf16 v[40:43], a[140:143], v[244:247], v[40:43]\n"
    "v_mfma_f32_16x16x32_bf16 v[44:47], a[144:147], v[244:247], v[44:47]\n"
    "v_mfma_f32_16x16x32_bf16 v[44:47], a[124:127], v[240:243], v[44:47]\n"
    "v_mfma_f32_16x16x32_bf16 v[48:51], a[128:131], v[240:243], v[48:51]\n"
    "buffer_load_dwordx4 v[172:175], v1, s[28:31], s89 offen offset:1024 sc1\n"
    "v_mfma_f32_16x16x32_bf16 v[48:51], a[148:151], v[244:247], v[48:51]\n"
    "v_mfma_f32_16x16x32_bf16 v[52:55], a[152:155], v[244:247], v[52:55]\n"
    "v_mfma_f32_16x16x32_bf16 v[52:55], a[132:135], v[240:243], v[52:55]\n"
    "s_add_u32 m0, s86, 49152\n"
    "s_add_u32 s88, s83, 49152\n"
    "v_mfma_f32_16x16x32_bf16 v[56:59], a[136:139], v[240:243], v[56:59]\n"
    "buffer_load_dwordx4 v1, s[28:31], s88 offen sc1 lds\n"
    "v_mfma_f32_16x16x32_bf16 v[56:59], a[156:159], v[244:247], v[56:59]\n"
    "s_waitcnt vmcnt(12)\n"
    "v_mfma_f32_16x16x32_bf16 v[40:43], a[160:163], v[248:251], v[40:43]\n"
    "s_waitcnt vmcnt(11)\n"
    "v_mfma_f32_16x16x32_bf16 v[40:43], a[180:183], v[252:255], v[40:43]\n"
    "v_mfma_f32_16x16x32_bf16 v[44:47], a[184:187], v[252:255], v[44:47]\n"
    "v_mfma_f32_16x16x32_bf16 v[44:47], a[164:167], v[248:251], v[44:47]\n"
    "buffer_load_dwordx4 v1, s[28:31], s88 offen offset:1024 sc1 lds\n"
    "v_mfma_f32_16x16x32_bf16 v[48:51], a[168:171], v[248:251], v[48:51]\n"
    "v_mfma_f32_16x16x32_bf16 v[48:51], a[188:191], v[252:255], v[48:51]\n"
    "v_mfma_f32_16x16x32_bf16 v[52:55], a[192:195], v[252:255], v[52:55]\n"
    "v_mfma_f32_16x16x32_bf16 v[52:55], a[172:175], v[248:251], v[52:55]\n"
    "v_mfma_f32_16x16x32_bf16 v[56:59], a[176:179], v[248:251], v[56:59]\n"
    "buffer_load_dwordx4 v1, s[28:31], s88 offen offset:2048 sc1 lds\n"
    "v_mfma_f32_16x16x32_bf16 v[56:59], a[196:199], v[252:255], v[56:59]\n"
    "v_mfma_f32_16x16x32_bf16 v[60:63], v[8:11], v[4:7], 0\n"
    "v_mfma_f32_16x16x32_bf16 v[64:67], v[8:11], v[2:5], 0\n"
    "v_mfma_f32_16x16x32_bf16 v[68:71], v[12:15], v[4:7], 0\n"
    "v_mfma_f32_16x16x32_bf16 v[72:75], v[12:15], v[2:5], 0\n"
    "buffer_load_dwordx4 v1, s[28:31], s88 offen offset:3072 sc1 lds\n"
    "v_mfma_f32_16x16x32_bf16 v[76:79], v[16:19], v[4:7], 0\n"
    "s_waitcnt vmcnt(13)\n"
    "ds_write_b128 v106, v[40:43] offset:0\n"
    "v_mfma_f32_16x16x32_bf16 v[60:63], a[0:3], v[136:139], v[60:63]\n"
    "s_waitcnt vmcnt(12)\n"
    "v_mfma_f32_16x16x32_bf16 v[60:63], a[20:23], v[140:143], v[60:63]\n"
    "s_add_u32 m0, s86, 65536\n"
    "s_add_u32 s88, s83, 65536\n"
    "v_mfma_f32_16x16x32_bf16 v[64:67], a[24:27], v[140:143], v[64:67]\n"
    "buffer_load_dwordx4 v1, s[28:31], s88 offen sc1 lds\n"
    "v_mfma_f32_16x16x32_bf16 v[64:67], a[4:7], v[136:139], v[64:67]\n"
    "ds_write_b128 v106, v[44:47] offset:1024\n"
    "v_mfma_f32_16x16x32_bf16 v[68:71], a[8:11], v[136:139], v[68:71]\n"
    "ds_write_b128 v106, v[48:51] offset:2048\n"
    "v_mfma_f32_16x16x32_bf16 v[68:71], a[28:31], v[140:143], v[68:71]\n"
    "ds_write_b128 v106, v[52:55] offset:3072\n"
    "v_mfma_f32_16x16x32_bf16 v[72:75], a[32:35], v[140:143], v[72:75]\n"
    "v_mfma_f32_16x16x32_bf16 v[72:75], a[12:15], v[136:139], v[72:75]\n"
    "buffer_load_dwordx4 v1, s[28:31], s88 offen offset:1024 sc1 lds\n"
    "v_mfma_f32_16x16x32_bf16 v[76:79], a[16:19], v[136:139], v[76:79]\n"
    "ds_write_b128 v106, v[56:59] offset:4096\n"
    "v_mfma_f32_16x16x32_bf16 v[76:79], a[36:39], v[140:143], v[76:79]\n"
    "s_waitcnt vmcnt(13)\n"
    "v_mfma_f32_16x16x32_bf16 v[60:63], a[40:43], v[144:147], v[60:63]\n"
    "s_waitcnt vmcnt(12)\n"
    "v_mfma_f32_16x16x32_bf16 v[60:63], a[60:63], v[148:151], v[60:63]\n"
    "v_mfma_f32_16x16x32_bf16 v[64:67], a[64:67], v[148:151], v[64:67]\n"
    "s_waitcnt lgkmcnt(0)\n"
    "s_barrier\n"
    "v_mfma_f32_16x16x32_bf16 v[64:67], a[44:47], v[144:147], v[64:67]\n"
    "buffer_load_dwordx4 v1, s[28:31], s88 offen offset:2048 sc1 lds\n"
    "v_mfma_f32_16x16x32_bf16 v[68:71], a[48:51], v[144:147], v[68:71]\n"
    "ds_read_b128 v[176:179], v107 offset:0\n"
    "ds_read_b128 v[180:183], v107 offset:1024\n"
    "v_mfma_f32_16x16x32_bf16 v[68:71], a[68:71], v[148:151], v[68:71]\n"
    "ds_read_b128 v[184:187], v107 offset:2048\n"
    "ds_read_b128 v[188:191], v107 offset:3072\n"
    "v_mfma_f32_16x16x32_bf16 v[72:75], a[72:75], v[148:151], v[72:75]\n"
    "ds_read_b128 v[192:195], v107 offset:4096\n"
    "ds_read_b128 v[196:199], v107 offset:5120\n"
    "v_mfma_f32_16x16x32_bf16 v[72:75], a[52:55], v[144:147], v[72:75]\n"
    "buffer_load_dwordx4 v1, s[28:31], s88 offen offset:3072 sc1 lds\n"
    "v_mfma_f32_16x16x32_bf16 v[76:79], a[56:59], v[144:147], v[76:79]\n"
    "ds_read_b128 v[200:203], v107 offset:6144\n"
    "ds_read_b128 v[204:207], v107 offset:7168\n"
    "v_mfma_f32_16x16x32_bf16 v[76:79], a[76:79], v[148:151], v[76:79]\n"
    "s_waitcnt vmcnt(13)\n"
    "ds_read_b128 v[208:211], v107 offset:8192\n"
    "ds_read_b128 v[212:215], v107 offset:9216\n"
    "v_mfma_f32_16x16x32_bf16 v[60:63], a[80:83], v[152:155], v[60:63]\n"
    "s_waitcnt vmcnt(12)\n"
    "ds_read_b128 v[216:219], v107 offset:10240\n"
    "ds_read_b128 v[220:223], v107 offset:11264\n"
    "v_mfma_f32_16x16x32_bf16 v[60:63], a[100:103], v[156:159], v[60:63]\n"
    "buffer_load_dwordx4 a[0:3], v1, s[28:31], s82 offen sc1\n"
    "v_mfma_f32_16x16x32_bf16 v[64:67], a[104:107], v[156:159], v[64:67]\n"
    "ds_read_b128 v[224:227], v107 offset:12288\n"
    "ds_read_b128 v[228:231], v107 offset:13312\n"
    "v_mfma_f32_16x16x32_bf16 v[64:67], a[84:87], v[152:155], v[64:67]\n"
    "ds_read_b128 v[232:235], v107 offset:14336\n"
    "v_mfma_f32_16x16x32_bf16 v[68:71], a[88:91], v[152:155], v[68:71]\n"
    "v_mfma_f32_16x16x32_bf16 v[68:71], a[108:111], v[156:159], v[68:71]\n"
    "s_waitcnt lgkmcnt(5)\n"
    "v_add_f32 v176, v176, v196\n"
    "v_add_f32 v177, v177, v197\n"
    "v_mfma_f32_16x16x32_bf16 v[72:75], a[112:115], v[156:159], v[72:75]\n"
    "buffer_load_dwordx4 a[4:7], v1, s[28:31], s82 offen offset:1024 sc1\n"
    "v_mfma_f32_16x16x32_bf16 v[72:75], a[92:95], v[152:155], v[72:75]\n"
    "v_add_f32 v178, v178, v198\n"
    "v_add_f32 v179, v179, v199\n"
    "v_mfma_f32_16x16x32_bf16 v[76:79], a[96:99], v[152:155], v[76:79]\n"
    "v_add_f32 v180, v180, v200\n"
    "v_add_f32 v181, v181, v201\n"
    "v_mfma_f32_16x16x32_bf16 v[76:79], a[116:119], v[156:159], v[76:79]\n"
    "s_waitcnt vmcnt(13)\n"
    "v_add_f32 v182, v182, v202\n"
    "v_add_f32 v183, v183, v203\n"
    "v_mfma_f32_16x16x32_bf16 v[60:63], a[120:123], v[160:163], v[60:63]\n"
    "s_waitcnt vmcnt(12)\n"
    "v_add_f32 v184, v184, v204\n"
    "v_add_f32 v185, v185, v205\n"
    "v_mfma_f32_16x16x32_bf16 v[60:63], a[140:143], v[164:167], v[60:63]\n"
    "buffer_load_dwordx4 a[8:11], v1, s[28:31], s82 offen offset:2048 sc1\n"
    "v_mfma_f32_16x16x32_bf16 v[64:67], a[144:147], v[164:167], v[64:67]\n"
    "v_add_f32 v186, v186, v206\n"
    "v_add_f32 v187, v187, v207\n"
    "v_mfma_f32_16x16x32_bf16 v[64:67], a[124:127], v[160:163], v[64:67]\n"
    "v_add_f32 v188, v188, v208\n"
    "v_add_f32 v189, v189, v209\n"
    "v_mfma_f32_16x16x32_bf16 v[68:71], a[128:131], v[160:163], v[68:71]\n"
    "v_add_f32 v190, v190, v210\n"
    "v_add_f32 v191, v191, v211\n"
    "v_mfma_f32_16x16x32_bf16 v[68:71], a[148:151], v[164:167], v[68:71]\n"
    "v_add_f32 v192, v192, v212\n"
    "v_add_f32 v193, v193, v213\n"
    "v_mfma_f32_16x16x32_bf16 v[72:75], a[152:155], v[164:167], v[72:75]\n"
    "buffer_load_dwordx4 a[12:15], v1, s[28:31], s82 offen offset:3072 sc1\n"
    "v_mfma_f32_16x16x32_bf16 v[72:75], a[132:135], v[160:163], v[72:75]\n"
    "v_add_f32 v194, v194, v214\n"
    "v_add_f32 v195, v195, v215\n"
    "v_mfma_f32_16x16x32_bf16 v[76:79], a[136:139], v[160:163], v[76:79]\n"
    "s_waitcnt lgkmcnt(0)\n"
    "v_add_f32 v176, v176, v216\n"
    "v_add_f32 v177, v177, v217\n"
    "v_mfma_f32_16x16x32_bf16 v[76:79], a[156:159], v[164:167], v[76:79]\n"
    "s_waitcnt vmcnt(13)\n"
    "s_add_u32 s90, s82, 4096\n"
    "v_add_f32 v178, v178, v218\n"
    "v_mfma_f32_16x16x32_bf16 v[60:63], a[160:163], v[168:171], v[60:63]\n"
    "s_waitcnt vmcnt(12)\n"
    "buffer_load_dwordx4 a[16:19], v1, s[28:31], s90 offen sc1\n"
    "v_mfma_f32_16x16x32_bf16 v[60:63], a[180:183], v[172:175], v[60:63]\n"
    "v_add_f32 v179, v179, v219\n"
    "v_add_f32 v180, v180, v220\n"
    "v_mfma_f32_16x16x32_bf16 v[64:67], a[184:187], v[172:175], v[64:67]\n"
    "v_add_f32 v181, v181, v221\n"
    "v_add_f32 v182, v182, v222\n"
    "v_mfma_f32_16x16x32_bf16 v[64:67], a[164:167], v[168:171], v[64:67]\n"
    "v_add_f32 v183, v183, v223\n"
    "v_add_f32 v184, v184, v224\n"
    "v_mfma_f32_16x16x32_bf16 v[68:71], a[168:171], v[168:171], v[68:71]\n"
    "v_add_f32 v185, v185, v225\n"
    "v_add_f32 v186, v186, v226\n"
    "v_mfma_f32_16x16x32_bf16 v[68:71], a[188:191], v[172:175], v[68:71]\n"
    "buffer_load_dwordx4 a[20:23], v1, s[28:31], s90 offen offset:1024 sc1\n"
    "v_mfma_f32_16x16x32_bf16 v[72:75], a[192:195], v[172:175], v[72:75]\n"
    "v_add_f32 v187, v187, v227\n"
    "v_add_f32 v188, v188, v228\n"
    "v_mfma_f32_16x16x32_bf16 v[72:75], a[172:175], v[168:171], v[72:75]\n"
    "v_add_f32 v189, v189, v229\n"
    "v_add_f32 v190, v190, v230\n"
    "v_mfma_f32_16x16x32_bf16 v[76:79], a[176:179], v[168:171], v[76:79]\n"
    "v_add_f32 v191, v191, v231\n"
    "v_add_f32 v192, v192, v232\n"
    "v_mfma_f32_16x16x32_bf16 v[76:79], a[196:199], v[172:175], v[76:79]\n"
    "v_add_f32 v193, v193, v233\n"
    "v_add_f32 v194, v194, v234\n"
    "v_add_f32 v195, v195, v235\n"
    "v_pk_add_f32 v[176:177], v[176:177], v[60:61]\n"
    "v_cvt_pk_bf16_f32 v8, v176, v177\n"
    "v_pk_add_f32 v[178:179], v[178:179], v[62:63]\n"
    "v_cvt_pk_bf16_f32 v9, v178, v179\n"
    "v_pk_add_f32 v[180:181], v[180:181], v[64:65]\n"
    "v_cvt_pk_bf16_f32 v10, v180, v181\n"
    "v_pk_add_f32 v[182:183], v[182:183], v[66:67]\n"
    "v_cvt_pk_bf16_f32 v11, v182, v183\n"
    "buffer_store_dwordx4 v[8:11], v1, s[28:31], s75 offen sc1\n"
    "v_pk_add_f32 v[184:185], v[184:185], v[68:69]\n"
    "v_cvt_pk_bf16_f32 v12, v184, v185\n"
    "v_pk_add_f32 v[186:187], v[186:187], v[70:71]\n"
    "v_cvt_pk_bf16_f32 v13, v186, v187\n"
    "v_pk_add_f32 v[188:189], v[188:189], v[72:73]\n"
    "v_cvt_pk_bf16_f32 v14, v188, v189\n"
    "v_pk_add_f32 v[190:191], v[190:191], v[74:75]\n"
    "v_cvt_pk_bf16_f32 v15, v190, v191\n"
    "buffer_store_dwordx4 v[12:15], v1, s[28:31], s75 offen offset:1024 sc1\n"
    "v_pk_add_f32 v[192:193], v[192:193], v[76:77]\n"
    "v_cvt_pk_bf16_f32 v16, v192, v193\n"
    "v_pk_add_f32 v[194:195], v[194:195], v[78:79]\n"
    "v_cvt_pk_bf16_f32 v17, v194, v195\n"
    "buffer_store_dwordx2 v[16:17], v30, s[28:31], s76 offen sc1\n"
    "s_add_u32 m0, s86, 81920\n"
    "s_add_u32 s88, s83, 81920\n"
    "buffer_load_dwordx4 v1, s[28:31], s88 offen sc1 lds\n"
    "buffer_load_dwordx4 v1, s[28:31], s88 offen offset:1024 sc1 lds\n"
    "buffer_load_dwordx4 v1, s[28:31], s88 offen offset:2048 sc1 lds\n"
    "buffer_load_dwordx4 v1, s[28:31], s88 offen offset:3072 sc1 lds\n"
    "buffer_load_dwordx4 a[24:27], v1, s[28:31], s90 offen offset:2048 sc1\n"
    "buffer_load_dwordx4 a[28:31], v1, s[28:31], s90 offen offset:3072 sc1\n"
    "s_add_u32 s90, s82, 8192\n"
    "buffer_load_dwordx4 a[32:35], v1, s[28:31], s90 offen sc1\n"
    "buffer_load_dwordx4 a[36:39], v1, s[28:31], s90 offen offset:1024 sc1\n"
    "s_add_u32 s66, s68, 5\n"
    "s_mov_b32 s67, s66\n"
    "v_mov_b32 v36, s66\n"
    "v_mov_b32 v37, s66\n"
    "s_sub_u32 s66, s66, 1\n"
    "v_lshlrev_b32 v40, 16, v8\n"
    "v_and_b32 v41, 0xffff0000, v8\n"
    "v_lshlrev_b32 v42, 16, v9\n"
    "v_and_b32 v43, 0xffff0000, v9\n"
    "v_lshlrev_b32 v44, 16, v10\n"
    "v_and_b32 v45, 0xffff0000, v10\n"
    "v_lshlrev_b32 v46, 16, v11\n"
    "v_and_b32 v47, 0xffff0000, v11\n"
    "v_lshlrev_b32 v48, 16, v12\n"
    "v_and_b32 v49, 0xffff0000, v12\n"
    "v_lshlrev_b32 v50, 16, v13\n"
    "v_and_b32 v51, 0xffff0000, v13\n"
    "v_lshlrev_b32 v52, 16, v14\n"
    "v_and_b32 v53, 0xffff0000, v14\n"
    "v_lshlrev_b32 v54, 16, v15\n"
    "v_and_b32 v55, 0xffff0000, v15\n"
    "v_lshlrev_b32 v56, 16, v16\n"
    "v_and_b32 v57, 0xffff0000, v16\n"
    "v_lshlrev_b32 v58, 16, v17\n"
    "v_and_b32 v59, 0xffff0000, v17\n"
    "v_mov_b32 v65, s66\n"
    "v_mov_b32 v67, s66\n"
    "v_pk_add_f32 v[60:61], v[40:41], v[42:43]\n"
    "v_pk_add_f32 v[62:63], v[44:45], v[46:47]\n"
    "v_pk_add_f32 v[60:61], v[60:61], v[48:49]\n"
    "v_pk_add_f32 v[62:63], v[62:63], v[50:51]\n"
    "v_pk_add_f32 v[60:61], v[60:61], v[52:53]\n"
    "v_pk_add_f32 v[62:63], v[62:63], v[54:55]\n"
    "v_pk_add_f32 v[60:61], v[60:61], v[56:57]\n"
    "v_pk_add_f32 v[62:63], v[62:63], v[58:59]\n"
    "v_pk_add_f32 v[60:61], v[60:61], v[62:63]\n"
    "v_add_f32 v64, v60, v61\n"
    "v_mov_b32 v60, v64\n"
    "s_nop 1\n"
    "v_permlane16_swap_b32 v60, v64\n"
    "v_add_f32 v64, v60, v64\n"
    "v_mov_b32 v60, v64\n"
    "s_nop 1\n"
    "v_permlane32_swap_b32 v60, v64\n"
    "v_add_f32 v64, v60, v64\n"
    "v_mul_f32 v64, 0x3c4ccccd, v64\n"
    "v_pk_add_f32 v[40:41], v[40:41], v[64:65] op_sel_hi:[1,0] neg_lo:[0,1] neg_hi:[0,1]\n"
    "v_pk_add_f32 v[42:43], v[42:43], v[64:65] op_sel_hi:[1,0] neg_lo:[0,1] neg_hi:[0,1]\n"
    "v_pk_add_f32 v[44:45], v[44:45], v[64:65] op_sel_hi:[1,0] neg_lo:[0,1] neg_hi:[0,1]\n"
    "v_pk_add_f32 v[46:47], v[46:47], v[64:65] op_sel_hi:[1,0] neg_lo:[0,1] neg_hi:[0,1]\n"
    "v_pk_add_f32 v[48:49], v[48:49], v[64:65] op_sel_hi:[1,0] neg_lo:[0,1] neg_hi:[0,1]\n"
    "v_pk_add_f32 v[50:51], v[50:51], v[64:65] op_sel_hi:[1,0] neg_lo:[0,1] neg_hi:[0,1]\n"
    "v_pk_add_f32 v[52:53], v[52:53], v[64:65] op_sel_hi:[1,0] neg_lo:[0,1] neg_hi:[0,1]\n"
    "v_pk_add_f32 v[54:55], v[54:55], v[64:65] op_sel_hi:[1,0] neg_lo:[0,1] neg_hi:[0,1]\n"
    "v_pk_add_f32 v[56:57], v[56:57], v[64:65] op_sel_hi:[1,0] neg_lo:[0,1] neg_hi:[0,1]\n"
    "v_pk_add_f32 v[58:59], v[58:59], v[64:65] op_sel_hi:[1,0] neg_lo:[0,1] neg_hi:[0,1]\n"
    "v_pk_mul_f32 v[60:61], v[40:41], v[40:41]\n"
    "v_pk_mul_f32 v[62:63], v[42:43], v[42:43]\n"
    "v_pk_fma_f32 v[60:61], v[44:45], v[44:45], v[60:61]\n"
    "v_pk_fma_f32 v[62:63], v[46:47], v[46:47], v[62:63]\n"
    "v_pk_fma_f32 v[60:61], v[48:49], v[48:49], v[60:61]\n"
    "v_pk_fma_f32 v[62:63], v[50:51], v[50:51], v[62:63]\n"
    "v_pk_fma_f32 v[60:61], v[52:53], v[52:53], v[60:61]\n"
    "v_pk_fma_f32 v[62:63], v[54:55], v[54:55], v[62:63]\n"
    "v_pk_fma_f32 v[60:61], v[56:57], v[56:57], v[60:61]\n"
    "v_pk_fma_f32 v[62:63], v[58:59], v[58:59], v[62:63]\n"
    "v_pk_add_f32 v[60:61], v[60:61], v[62:63]\n"
    "v_add_f32 v66, v60, v61\n"
    "v_mov_b32 v60, v66\n"
    "s_nop 1\n"
    "v_permlane16_swap_b32 v60, v66\n"
    "v_add_f32 v66, v60, v66\n"
    "v_mov_b32 v60, v66\n"
    "s_nop 1\n"
    "v_permlane32_swap_b32 v60, v66\n"
    "v_add_f32 v66, v60, v66\n"
    "s_add_u32 s91, s69, 262144\n"
    "s_mov_b64 exec, 0xffff\n"
    "buffer_store_dwordx4 v[64:67], v1, s[52:55], s91 offen sc1\n"
    "s_mov_b64 exec, -1\n"
    "s_add_u32 s66, s66, 1\n"
    "s_cmp_lg_u32 s64, 0\n"
    "s_cbranch_scc1 L_l0_33\n"
    "s_getpc_b64 s[48:49]\n"
    "L_pc_34:\n"
    "s_and_b32 s49, s49, 0xffff\n"
    "s_mov_b32 s50, L_pc1-L_pc_34\n"
    "s_mov_b32 s51, 0x20000\n"
    "s_lshr_b32 s91, s63, 3\n"
    "s_mul_i32 s91, s91, 1024\n"
    "s_mul_i32 s95, s56, 256\n"
    "s_add_u32 s91, s91, s95\n"
    "v_lshlrev_b32 v255, 3, v1\n"
    "s_mov_b64 exec, 0x3\n"
    "buffer_load_dword v255, v255, s[48:51], s91 offen\n"
    "s_mov_b64 exec, -1\n"
    "L_l0_33:\n"
    "v_bfe_u32 v190, v0, 4, 2\n"
    "v_lshlrev_b32 v190, 9, v190\n"
    "v_and_b32 v191, 15, v0\n"
    "v_lshl_add_u32 v190, v191, 4, v190\n"
    "v_and_b32 v191, 48, v0\n"
    "v_add_u32 v191, 159744, v191\n"
    "s_waitcnt vmcnt(9)\n"
    "s_barrier\n"
    "s_mov_b64 exec, 0xf\n"
    "buffer_atomic_swap_x2 v[36:37], v29, s[52:55], s10 offen sc1\n"
    "s_mov_b64 exec, -1\n"
    "s_add_u32 m0, s86, 98304\n"
    "s_add_u32 s88, s83, 98304\n"
    "buffer_load_dwordx4 v1, s[28:31], s88 offen sc1 lds\n"
    "buffer_load_dwordx4 v1, s[28:31], s88 offen offset:1024 sc1 lds\n"
    "buffer_load_dwordx4 v1, s[28:31], s88 offen offset:2048 sc1 lds\n"
    "buffer_load_dwordx4 v1, s[28:31], s88 offen offset:3072 sc1 lds\n"
    "s_add_u32 m0, s86, 114688\n"
    "s_add_u32 s88, s83, 114688\n"
    "buffer_load_dwordx4 v1, s[28:31], s88 offen sc1 lds\n"
    "buffer_load_dwordx4 v1, s[28:31], s88 offen offset:1024 sc1 lds\n"
    "buffer_load_dwordx4 v1, s[28:31], s88 offen offset:2048 sc1 lds\n"
    "buffer_load_dwordx4 v1, s[28:31], s88 offen offset:3072 sc1 lds\n"
    "s_add_u32 m0, s86, 131072\n"
    "s_add_u32 s88, s83, 131072\n"
    "buffer_load_dwordx4 v1, s[28:31], s88 offen sc1 lds\n"
    "buffer_load_dwordx4 v1, s[28:31], s88 offen offset:1024 sc1 lds\n"
    "buffer_load_dwordx4 v1, s[28:31], s88 offen offset:2048 sc1 lds\n"
    "buffer_load_dwordx4 v1, s[28:31], s88 offen offset:3072 sc1 lds\n"
    "s_waitcnt vmcnt(12)\n"
    "v_mov_b64 v[40:41], 0\n"
    "s_mov_b64 exec, 0xff\n"
    "buffer_atomic_or_x2 v[40:41], v28, s[52:55], s14 offen sc0 sc1\n"
    "s_mov_b64 exec, -1\n"
    "buffer_load_dwordx4 a[40:43], v1, s[28:31], s90 offen offset:2048 sc1\n"
    "buffer_load_dwordx4 a[44:47], v1, s[28:31], s90 offen offset:3072 sc1\n"
    "s_add_u32 s90, s82, 12288\n"
    "buffer_load_dwordx4 a[48:51], v1, s[28:31], s90 offen sc1\n"
    "buffer_load_dwordx4 a[52:55], v1, s[28:31], s90 offen offset:1024 sc1\n"
    "s_waitcnt vmcnt(4)\n"
    "v_cmp_ne_u64 vcc, s[66:67], v[40:41]\n"
    "s_nop 0\n"
    "s_and_b64 vcc, vcc, 255\n"
    "s_cbranch_scc0 L_ok_35\n"
    "v_mov_b64 v[40:41], 0\n"
    "s_mov_b64 exec, 0xff\n"
    "buffer_atomic_or_x2 v[40:41], v28, s[52:55], s14 offen sc0 sc1\n"
    "s_mov_b64 exec, -1\n"
    "s_branch L_spin_36\n"
    "L_ok_35:\n"
    "buffer_load_dwordx4 a[160:163], v1, s[28:31], s77 offen sc1\n"
    "s_nop 7\n"
    "buffer_load_dwordx4 a[164:167], v1, s[28:31], s78 offen sc1\n"
    "s_nop 7\n"
    "buffer_load_dwordx4 a[168:171], v1, s[28:31], s77 offen offset:1024 sc1\n"
    "s_nop 7\n"
    "buffer_load_dwordx4 a[172:175], v1, s[28:31], s78 offen offset:1024 sc1\n"
    "s_nop 7\n"
    "buffer_load_dwordx4 a[176:179], v1, s[28:31], s77 offen offset:2048 sc1\n"
    "s_nop 7\n"
    "buffer_load_dwordx4 a[180:183], v1, s[28:31], s78 offen offset:2048 sc1\n"
    "s_nop 7\n"
    "buffer_load_dwordx4 v[88:91], v190, s[52:55], s71 offen sc1\n"
    "s_nop 7\n"
    "buffer_load_dwordx4 v[92:95], v190, s[52:55], s71 offen offset:256 sc1\n"
    "s_nop 7\n"
    "buffer_load_dwordx4 v[96:99], v190, s[52:55], s71 offen offset:2048 sc1\n"
    "s_nop 7\n"
    "buffer_load_dwordx4 v[100:103], v190, s[52:55], s71 offen offset:2304 sc1\n"
    "s_nop 7\n"
    "buffer_load_dwordx4 a[184:187], v1, s[28:31], s77 offen offset:3072 sc1\n"
    "s_nop 7\n"
    "buffer_load_dwordx4 a[188:191], v1, s[28:31], s78 offen offset:3072 sc1\n"
    "s_nop 7\n"
    "s_add_u32 s89, s77, 4096\n"
    "buffer_load_dwordx4 a[192:195], v1, s[28:31], s89 offen sc1\n"
    "s_nop 7\n"
    "s_add_u32 s94, s78, 4096\n"
    "buffer_load_dwordx4 a[196:199], v1, s[28:31], s94 offen sc1\n"
    "ds_read_b128 v[136:139], v1 offset:0\n"
    "ds_read_b128 v[140:143], v1 offset:1024\n"
    "ds_read_b128 v[144:147], v1 offset:2048\n"
    "ds_read_b128 v[148:151], v1 offset:3072\n"
    "ds_read_b128 v[152:155], v1 offset:4096\n"
    "ds_read_b128 v[156:159], v1 offset:5120\n"
    "s_sub_u32 s66, s66, 1\n"
    "s_mul_i32 s62, s56, 15360\n"
    "s_add_u32 s62, s62, 98304\n"
    "s_waitcnt vmcnt(12) lgkmcnt(4)\n"
    "v_mfma_f32_16x16x32_bf16 v[40:43], v[136:139], a[160:163], 0\n"
    "ds_read_b128 v[160:163], v1 offset:6144\n"
    "v_mfma_f32_16x16x32_bf16 v[44:47], v[136:139], a[164:167], 0\n"
    "buffer_load_dwordx4 a[200:203], v1, s[28:31], s89 offen offset:1024 sc1\n"
    "v_mfma_f32_16x16x32_bf16 v[48:51], v[140:143], a[160:163], 0\n"
    "ds_read_b128 v[164:167], v1 offset:7168\n"
    "v_mfma_f32_16x16x32_bf16 v[52:55], v[140:143], a[164:167], 0\n"
    "s_waitcnt lgkmcnt(4)\n"
    "v_mfma_f32_16x16x32_bf16 v[56:59], v[144:147], a[160:163], 0\n"
    "ds_read_b128 v[136:139], v1 offset:8192\n"
    "v_mfma_f32_16x16x32_bf16 v[60:63], v[144:147], a[164:167], 0\n"
    "buffer_load_dwordx4 a[204:207], v1, s[28:31], s94 offen offset:1024 sc1\n"
    "v_mfma_f32_16x16x32_bf16 v[64:67], v[148:151], a[160:163], 0\n"
    "ds_read_b128 v[140:143], v1 offset:9216\n"
    "v_mfma_f32_16x16x32_bf16 v[68:71], v[148:151], a[164:167], 0\n"
    "s_waitcnt lgkmcnt(4)\n"
    "v_mfma_f32_16x16x32_bf16 v[72:75], v[152:155], a[160:163], 0\n"
    "ds_read_b128 v[144:147], v1 offset:10240\n"
    "v_mfma_f32_16x16x32_bf16 v[76:79], v[152:155], a[164:167], 0\n"
    "v_mfma_f32_16x16x32_bf16 v[80:83], v[156:159], a[160:163], 0\n"
    "buffer_load_dwordx4 a[208:211], v1, s[28:31], s89 offen offset:2048 sc1\n"
    "v_mfma_f32_16x16x32_bf16 v[84:87], v[156:159], a[164:167], 0\n"
    "s_waitcnt vmcnt(13) lgkmcnt(3)\n"
    "ds_read_b128 v[148:151], v1 offset:11264\n"
    "v_mfma_f32_16x16x32_bf16 v[40:43], v[160:163], a[168:171], v[40:43]\n"
    "ds_read_b128 v[152:155], v1 offset:12288\n"
    "v_mfma_f32_16x16x32_bf16 v[44:47], v[160:163], a[172:175], v[44:47]\n"
    "v_mfma_f32_16x16x32_bf16 v[48:51], v[164:167], a[168:171], v[48:51]\n"
    "buffer_load_dwordx4 a[212:215], v1, s[28:31], s94 offen offset:2048 sc1\n"
    "v_mfma_f32_16x16x32_bf16 v[52:55], v[164:167], a[172:175], v[52:55]\n"
    "s_waitcnt lgkmcnt(3)\n"
    "ds_read_b128 v[156:159], v1 offset:13312\n"
    "v_mfma_f32_16x16x32_bf16 v[56:59], v[136:139], a[168:171], v[56:59]\n"
    "ds_read_b128 v[160:163], v1 offset:14336\n"
    "v_mfma_f32_16x16x32_bf16 v[60:63], v[136:139], a[172:175], v[60:63]\n"
    "v_mfma_f32_16x16x32_bf16 v[64:67], v[140:143], a[168:171], v[64:67]\n"
    "ds_read_b128 v[164:167], v1 offset:15360\n"
    "v_mfma_f32_16x16x32_bf16 v[68:71], v[140:143], a[172:175], v[68:71]\n"
    "s_waitcnt lgkmcnt(4)\n"
    "buffer_load_dwordx4 a[216:219], v1, s[28:31], s89 offen offset:3072 sc1\n"
    "v_mfma_f32_16x16x32_bf16 v[72:75], v[144:147], a[168:171], v[72:75]\n"
    "ds_read_b128 v[136:139], v1 offset:16384\n"
    "v_mfma_f32_16x16x32_bf16 v[76:79], v[144:147], a[172:175], v[76:79]\n"
    "v_mfma_f32_16x16x32_bf16 v[80:83], v[148:151], a[168:171], v[80:83]\n"
    "ds_read_b128 v[140:143], v1 offset:17408\n"
    "v_mfma_f32_16x16x32_bf16 v[84:87], v[148:151], a[172:175], v[84:87]\n"
    "s_waitcnt vmcnt(13) lgkmcnt(4)\n"
    "buffer_load_dwordx4 a[220:223], v1, s[28:31], s94 offen offset:3072 sc1\n"
    "v_mfma_f32_16x16x32_bf16 v[40:43], v[152:155], a[176:179], v[40:43]\n"
    "ds_read_b128 v[144:147], v1 offset:18432\n"
    "v_mfma_f32_16x16x32_bf16 v[44:47], v[152:155], a[180:183], v[44:47]\n"
    "v_mfma_f32_16x16x32_bf16 v[48:51], v[156:159], a[176:179], v[48:51]\n"
    "ds_read_b128 v[148:151], v1 offset:19456\n"
    "v_mfma_f32_16x16x32_bf16 v[52:55], v[156:159], a[180:183], v[52:55]\n"
    "s_waitcnt lgkmcnt(4)\n"
    "s_add_u32 s89, s77, 8192\n"
    "v_mfma_f32_16x16x32_bf16 v[56:59], v[160:163], a[176:179], v[56:59]\n"
    "buffer_load_dwordx4 a[224:227], v1, s[28:31], s89 offen sc1\n"
    "v_mfma_f32_16x16x32_bf16 v[60:63], v[160:163], a[180:183], v[60:63]\n"
    "ds_read_b128 v[152:155], v1 offset:20480\n"
    "v_mfma_f32_16x16x32_bf16 v[64:67], v[164:167], a[176:179], v[64:67]\n"
    "ds_read_b128 v[156:159], v1 offset:21504\n"
    "v_mfma_f32_16x16x32_bf16 v[68:71], v[164:167], a[180:183], v[68:71]\n"
    "s_waitcnt lgkmcnt(4)\n"
    "s_add_u32 s94, s78, 8192\n"
    "v_mfma_f32_16x16x32_bf16 v[72:75], v[136:139], a[176:179], v[72:75]\n"
    "buffer_load_dwordx4 a[228:231], v1, s[28:31], s94 offen sc1\n"
    "v_mfma_f32_16x16x32_bf16 v[76:79], v[136:139], a[180:183], v[76:79]\n"
    "ds_read_b128 v[160:163], v1 offset:22528\n"
    "v_mfma_f32_16x16x32_bf16 v[80:83], v[140:143], a[176:179], v[80:83]\n"
    "ds_read_b128 v[164:167], v1 offset:23552\n"
    "v_mfma_f32_16x16x32_bf16 v[84:87], v[140:143], a[180:183], v[84:87]\n"
    "s_waitcnt vmcnt(10) lgkmcnt(4)\n"
    "v_mfma_f32_16x16x32_bf16 v[40:43], v[144:147], a[184:187], v[40:43]\n"
    "ds_read_b128 v[136:139], v1 offset:24576\n"
    "v_xor_b32 v182, s66, v89\n"
    "v_mfma_f32_16x16x32_bf16 v[44:47], v[144:147], a[188:191], v[44:47]\n"
    "buffer_load_dwordx4 a[232:235], v1, s[28:31], s89 offen offset:1024 sc1\n"
    "v_mfma_f32_16x16x32_bf16 v[48:51], v[148:151], a[184:187], v[48:51]\n"
    "ds_read_b128 v[140:143], v1 offset:25600\n"
    "v_xor_b32 v183, s66, v93\n"
    "v_mfma_f32_16x16x32_bf16 v[52:55], v[148:151], a[188:191], v[52:55]\n"
    "s_waitcnt lgkmcnt(4)\n"
    "v_or_b32 v182, v182, v183\n"
    "v_xor_b32 v183, s66, v97\n"
    "v_mfma_f32_16x16x32_bf16 v[56:59], v[152:155], a[184:187], v[56:59]\n"
    "ds_read_b128 v[144:147], v1 offset:26624\n"
    "v_or_b32 v182, v182, v183\n"
    "v_mfma_f32_16x16x32_bf16 v[60:63], v[152:155], a[188:191], v[60:63]\n"
    "buffer_load_dwordx4 a[236:239], v1, s[28:31], s94 offen offset:1024 sc1\n"
    "v_mfma_f32_16x16x32_bf16 v[64:67], v[156:159], a[184:187], v[64:67]\n"
    "ds_read_b128 v[148:151], v1 offset:27648\n"
    "v_xor_b32 v183, s66, v101\n"
    "v_mfma_f32_16x16x32_bf16 v[68:71], v[156:159], a[188:191], v[68:71]\n"
    "s_waitcnt lgkmcnt(4)\n"
    "v_or_b32 v182, v182, v183\n"
    "v_xor_b32 v183, s66, v91\n"
    "v_mfma_f32_16x16x32_bf16 v[72:75], v[160:163], a[184:187], v[72:75]\n"
    "ds_read_b128 v[152:155], v1 offset:28672\n"
    "v_or_b32 v182, v182, v183\n"
    "v_mfma_f32_16x16x32_bf16 v[76:79], v[160:163], a[188:191], v[76:79]\n"
    "v_xor_b32 v183, s66, v95\n"
    "v_or_b32 v182, v182, v183\n"
    "v_mfma_f32_16x16x32_bf16 v[80:83], v[164:167], a[184:187], v[80:83]\n"
    "ds_read_b128 v[156:159], v1 offset:29696\n"
    "v_xor_b32 v183, s66, v99\n"
    "v_mfma_f32_16x16x32_bf16 v[84:87], v[164:167], a[188:191], v[84:87]\n"
    "s_waitcnt vmcnt(10) lgkmcnt(4)\n"
    "buffer_load_dwordx4 a[240:243], v1, s[28:31], s89 offen offset:2048 sc1\n"
    "v_mfma_f32_16x16x32_bf16 v[40:43], v[136:139], a[192:195], v[40:43]\n"
    "ds_read_b128 v[160:163], v1 offset:30720\n"
    "v_or_b32 v182, v182, v183\n"
    "v_mfma_f32_16x16x32_bf16 v[44:47], v[136:139], a[196:199], v[44:47]\n"
    "v_xor_b32 v183, s66, v103\n"
    "v_or_b32 v182, v182, v183\n"
    "v_mfma_f32_16x16x32_bf16 v[48:51], v[140:143], a[192:195], v[48:51]\n"
    "ds_read_b128 v[164:167], v1 offset:31744\n"
    "v_cmp_ne_u32 vcc, 0, v182\n"
    "v_mfma_f32_16x16x32_bf16 v[52:55], v[140:143], a[196:199], v[52:55]\n"
    "s_waitcnt lgkmcnt(4)\n"
    "s_barrier\n"
    "s_nop 0\n"
    "v_mfma_f32_16x16x32_bf16 v[56:59], v[144:147], a[192:195], v[56:59]\n"
    "s_cbranch_vccnz L_gspin_38\n"
    "L_gok_37:\n"
    "buffer_load_dwordx4 a[244:247], v1, s[28:31], s94 offen offset:2048 sc1\n"
    "v_mfma_f32_16x16x32_bf16 v[60:63], v[144:147], a[196:199], v[60:63]\n"
    "s_add_u32 s66, s68, 6\n"
    "s_mov_b32 s67, s66\n"
    "v_mov_b32 v36, s66\n"
    "v_mov_b32 v37, s66\n"
    "v_mfma_f32_16x16x32_bf16 v[64:67], v[148:151], a[192:195], v[64:67]\n"
    "ds_read_b128 v[136:139], v1 offset:32768\n"
    "ds_read_b128 v[140:143], v1 offset:33792\n"
    "v_mfma_f32_16x16x32_bf16 v[68:71], v[148:151], a[196:199], v[68:71]\n"
    "s_waitcnt lgkmcnt(4)\n"
    "buffer_load_dwordx4 a[248:251], v1, s[28:31], s89 offen offset:3072 sc1\n"
    "v_mfma_f32_16x16x32_bf16 v[72:75], v[152:155], a[192:195], v[72:75]\n"
    "ds_read_b128 v[144:147], v1 offset:34816\n"
    "v_add_f32 v176, v88, v92\n"
    "v_mfma_f32_16x16x32_bf16 v[76:79], v[152:155], a[196:199], v[76:79]\n"
    "v_add_f32 v179, v96, v100\n"
    "v_mov_b32 v182, v176\n"
    "v_mfma_f32_16x16x32_bf16 v[80:83], v[156:159], a[192:195], v[80:83]\n"
    "ds_read_b128 v[148:151], v1 offset:35840\n"
    "v_mov_b32 v183, v179\n"
    "v_mfma_f32_16x16x32_bf16 v[84:87], v[156:159], a[196:199], v[84:87]\n"
    "s_waitcnt vmcnt(11) lgkmcnt(4)\n"
    "buffer_load_dwordx4 a[252:255], v1, s[28:31], s94 offen offset:3072 sc1\n"
    "v_mfma_f32_16x16x32_bf16 v[40:43], v[160:163], a[200:203], v[40:43]\n"
    "ds_read_b128 v[152:155], v1 offset:36864\n"
    "v_permlane16_swap_b32 v182, v176\n"
    "v_mfma_f32_16x16x32_bf16 v[44:47], v[160:163], a[204:207], v[44:47]\n"
    "v_permlane16_swap_b32 v183, v179\n"
    "v_add_f32 v176, v182, v176\n"
    "v_mfma_f32_16x16x32_bf16 v[48:51], v[164:167], a[200:203], v[48:51]\n"
    "ds_read_b128 v[156:159], v1 offset:37888\n"
    "v_add_f32 v179, v183, v179\n"
    "v_mfma_f32_16x16x32_bf16 v[52:55], v[164:167], a[204:207], v[52:55]\n"
    "s_waitcnt lgkmcnt(4)\n"
    "s_add_u32 s89, s77, 12288\n"
    "v_mov_b32 v182, v176\n"
    "v_mfma_f32_16x16x32_bf16 v[56:59], v[136:139], a[200:203], v[56:59]\n"
    "ds_read_b128 v[160:163], v1 offset:38912\n"
    "v_mov_b32 v183, v179\n"
    "v_mfma_f32_16x16x32_bf16 v[60:63], v[136:139], a[204:207], v[60:63]\n"
    "buffer_load_dwordx4 v[192:195], v1, s[28:31], s89 offen sc1\n"
    "v_mfma_f32_16x16x32_bf16 v[64:67], v[140:143], a[200:203], v[64:67]\n"
    "ds_read_b128 v[164:167], v1 offset:39936\n"
    "v_permlane32_swap_b32 v182, v176\n"
    "v_mfma_f32_16x16x32_bf16 v[68:71], v[140:143], a[204:207], v[68:71]\n"
    "s_waitcnt lgkmcnt(4)\n"
    "s_add_u32 s94, s78, 12288\n"
    "v_permlane32_swap_b32 v183, v179\n"
    "v_mfma_f32_16x16x32_bf16 v[72:75], v[144:147], a[200:203], v[72:75]\n"
    "ds_read_b128 v[136:139], v1 offset:40960\n"
    "v_add_f32 v176, v182, v176\n"
    "v_mfma_f32_16x16x32_bf16 v[76:79], v[144:147], a[204:207], v[76:79]\n"
    "buffer_load_dwordx4 v[196:199], v1, s[28:31], s94 offen sc1\n"
    "v_mfma_f32_16x16x32_bf16 v[80:83], v[148:151], a[200:203], v[80:83]\n"
    "ds_read_b128 v[140:143], v1 offset:41984\n"
    "v_add_f32 v179, v183, v179\n"
    "v_mfma_f32_16x16x32_bf16 v[84:87], v[148:151], a[204:207], v[84:87]\n"
    "s_waitcnt vmcnt(12) lgkmcnt(4)\n"
    "v_mul_f32 v176, 0x3e000000, v176\n"
    "v_mul_f32 v179, 0x3e000000, v179\n"
    "v_mfma_f32_16x16x32_bf16 v[40:43], v[152:155], a[208:211], v[40:43]\n"
    "ds_read_b128 v[144:147], v1 offset:43008\n"
    "v_sub_f32 v88, v88, v176\n"
    "v_mfma_f32_16x16x32_bf16 v[44:47], v[152:155], a[212:215], v[44:47]\n"
    "buffer_load_dwordx4 v[200:203], v1, s[28:31], s89 offen offset:1024 sc1\n"
    "v_mfma_f32_16x16x32_bf16 v[48:51], v[156:159], a[208:211], v[48:51]\n"
    "ds_read_b128 v[148:151], v1 offset:44032\n"
    "v_sub_f32 v96, v96, v179\n"
    "v_mfma_f32_16x16x32_bf16 v[52:55], v[156:159], a[212:215], v[52:55]\n"
    "s_waitcnt lgkmcnt(4)\n"
    "v_sub_f32 v92, v92, v176\n"
    "v_sub_f32 v100, v100, v179\n"
    "v_mfma_f32_16x16x32_bf16 v[56:59], v[160:163], a[208:211], v[56:59]\n"
    "ds_read_b128 v[152:155], v1 offset:45056\n"
    "v_mul_f32 v88, v88, v88\n"
    "v_mfma_f32_16x16x32_bf16 v[60:63], v[160:163], a[212:215], v[60:63]\n"
    "buffer_load_dwordx4 v[204:207], v1, s[28:31], s94 offen offset:1024 sc1\n"
    "v_mfma_f32_16x16x32_bf16 v[64:67], v[164:167], a[208:211], v[64:67]\n"
    "ds_read_b128 v[156:159], v1 offset:46080\n"
    "v_mul_f32 v96, v96, v96\n"
    "v_mfma_f32_16x16x32_bf16 v[68:71], v[164:167], a[212:215], v[68:71]\n"
    "s_waitcnt lgkmcnt(4)\n"
    "v_fmac_f32 v88, v92, v92\n"
    "v_fmac_f32 v96, v100, v100\n"
    "v_mfma_f32_16x16x32_bf16 v[72:75], v[136:139], a[208:211], v[72:75]\n"
    "ds_read_b128 v[160:163], v1 offset:47104\n"
    "v_add_f32 v178, v90, v94\n"
    "v_mfma_f32_16x16x32_bf16 v[76:79], v[136:139], a[212:215], v[76:79]\n"
    "v_add_f32 v181, v98, v102\n"
    "v_fmac_f32 v178, 0x42a00000, v88\n"
    "v_mfma_f32_16x16x32_bf16 v[80:83], v[140:143], a[208:211], v[80:83]\n"
    "ds_read_b128 v[164:167], v1 offset:48128\n"
    "v_fmac_f32 v181, 0x42a00000, v96\n"
    "v_mfma_f32_16x16x32_bf16 v[84:87], v[140:143], a[212:215], v[84:87]\n"
    "s_waitcnt vmcnt(12) lgkmcnt(4)\n"
    "buffer_load_dwordx4 v[208:211], v1, s[28:31], s89 offen offset:2048 sc1\n"
    "v_mfma_f32_16x16x32_bf16 v[40:43], v[144:147], a[216:219], v[40:43]\n"
    "ds_read_b128 v[136:139], v1 offset:49152\n"
    "v_mov_b32 v182, v178\n"
    "v_mfma_f32_16x16x32_bf16 v[44:47], v[144:147], a[220:223], v[44:47]\n"
    "v_mov_b32 v183, v181\n"
    "v_permlane16_swap_b32 v182, v178\n"
    "v_mfma_f32_16x16x32_bf16 v[48:51], v[148:151], a[216:219], v[48:51]\n"
    "ds_read_b128 v[140:143], v1 offset:50176\n"
    "v_permlane16_swap_b32 v183, v181\n"
    "v_mfma_f32_16x16x32_bf16 v[52:55], v[148:151], a[220:223], v[52:55]\n"
    "s_waitcnt lgkmcnt(4)\n"
    "buffer_load_dwordx4 v[212:215], v1, s[28:31], s94 offen offset:2048 sc1\n"
    "v_mfma_f32_16x16x32_bf16 v[56:59], v[152:155], a[216:219], v[56:59]\n"
    "ds_read_b128 v[144:147], v1 offset:51200\n"
    "v_add_f32 v178, v182, v178\n"
    "v_mfma_f32_16x16x32_bf16 v[60:63], v[152:155], a[220:223], v[60:63]\n"
    "v_add_f32 v181, v183, v181\n"
    "v_mov_b32 v182, v178\n"
    "v_mfma_f32_16x16x32_bf16 v[64:67], v[156:159], a[216:219], v[64:67]\n"
    "ds_read_b128 v[148:151], v1 offset:52224\n"
    "v_mov_b32 v183, v181\n"
    "v_mfma_f32_16x16x32_bf16 v[68:71], v[156:159], a[220:223], v[68:71]\n"
    "s_waitcnt lgkmcnt(4)\n"
    "buffer_load_dwordx4 v[216:219], v1, s[28:31], s89 offen offset:3072 sc1\n"
    "v_mfma_f32_16x16x32_bf16 v[72:75], v[160:163], a[216:219], v[72:75]\n"
    "ds_read_b128 v[152:155], v1 offset:53248\n"
    "v_permlane32_swap_b32 v182, v178\n"
    "v_mfma_f32_16x16x32_bf16 v[76:79], v[160:163], a[220:223], v[76:79]\n"
    "v_permlane32_swap_b32 v183, v181\n"
    "ds_read_b128 v[168:171], v191 offset:0\n"
    "v_mfma_f32_16x16x32_bf16 v[80:83], v[164:167], a[216:219], v[80:83]\n"
    "ds_read_b128 v[156:159], v1 offset:54272\n"
    "v_add_f32 v178, v182, v178\n"
    "v_mfma_f32_16x16x32_bf16 v[84:87], v[164:167], a[220:223], v[84:87]\n"
    "s_waitcnt vmcnt(13) lgkmcnt(5)\n"
    "buffer_load_dwordx4 v[220:223], v1, s[28:31], s94 offen offset:3072 sc1\n"
    "v_mfma_f32_16x16x32_bf16 v[40:43], v[136:139], a[224:227], v[40:43]\n"
    "ds_read_b128 v[160:163], v1 offset:55296\n"
    "v_add_f32 v181, v183, v181\n"
    "v_mfma_f32_16x16x32_bf16 v[44:47], v[136:139], a[228:231], v[44:47]\n"
    "v_mov_b32 v88, 0x3727c5ac\n"
    "v_mov_b32 v96, 0x3727c5ac\n"
    "v_mfma_f32_16x16x32_bf16 v[48:51], v[140:143], a[224:227], v[48:51]\n"
    "ds_read_b128 v[164:167], v1 offset:56320\n"
    "v_fmac_f32 v88, 0x3acccccd, v178\n"
    "v_mfma_f32_16x16x32_bf16 v[52:55], v[140:143], a[228:231], v[52:55]\n"
    "s_waitcnt lgkmcnt(5)\n"
    "s_add_u32 s89, s77, 16384\n"
    "v_fmac_f32 v96, 0x3acccccd, v181\n"
    "v_mfma_f32_16x16x32_bf16 v[56:59], v[144:147], a[224:227], v[56:59]\n"
    "buffer_load_dwordx4 v[224:227], v1, s[28:31], s89 offen sc1\n"
    "v_mfma_f32_16x16x32_bf16 v[60:63], v[144:147], a[228:231], v[60:63]\n"
    "v_rsq_f32 v177, v88\n"
    "v_mfma_f32_16x16x32_bf16 v[64:67], v[148:151], a[224:227], v[64:67]\n"
    "ds_read_b128 v[136:139], v1 offset:57344\n"
    "ds_read_b128 v[140:143], v1 offset:58368\n"
    "v_mfma_f32_16x16x32_bf16 v[68:71], v[148:151], a[228:231], v[68:71]\n"
    "s_waitcnt lgkmcnt(4)\n"
    "v_rsq_f32 v180, v96\n"
    "v_mfma_f32_16x16x32_bf16 v[72:75], v[152:155], a[224:227], v[72:75]\n"
    "s_add_u32 s94, s78, 16384\n"
    "ds_read_b128 v[144:147], v1 offset:59392\n"
    "v_mul_f32 v178, v88, v177\n"
    "v_mfma_f32_16x16x32_bf16 v[76:79], v[152:155], a[228:231], v[76:79]\n"
    "buffer_load_dwordx4 v[228:231], v1, s[28:31], s94 offen sc1\n"
    "v_mfma_f32_16x16x32_bf16 v[80:83], v[156:159], a[224:227], v[80:83]\n"
    "ds_read_b128 v[148:151], v1 offset:60416\n"
    "v_mul_f32 v181, v96, v180\n"
    "v_mfma_f32_16x16x32_bf16 v[84:87], v[156:159], a[228:231], v[84:87]\n"
    "s_waitcnt vmcnt(14) lgkmcnt(4)\n"
    "v_mul_f32 v177, 0xbfb8aa3b, v177\n"
    "v_mul_f32 v180, 0xbfb8aa3b, v180\n"
    "v_mfma_f32_16x16x32_bf16 v[40:43], v[160:163], a[232:235], v[40:43]\n"
    "ds_read_b128 v[172:175], v191 offset:384\n"
    "ds_read_b128 v[152:155], v1 offset:61440\n"
    "v_mfma_f32_16x16x32_bf16 v[44:47], v[160:163], a[236:239], v[44:47]\n"
    "buffer_load_dwordx4 v[232:235], v1, s[28:31], s89 offen offset:1024 sc1\n"
    "v_mfma_f32_16x16x32_bf16 v[48:51], v[164:167], a[232:235], v[48:51]\n"
    "ds_read_b128 v[156:159], v1 offset:62464\n"
    "v_mfma_f32_16x16x32_bf16 v[52:55], v[164:167], a[236:239], v[52:55]\n"
    "s_waitcnt lgkmcnt(5)\n"
    "v_mfma_f32_16x16x32_bf16 v[56:59], v[136:139], a[232:235], v[56:59]\n"
    "ds_read_b128 v[160:163], v1 offset:63488\n"
    "v_fma_f32 v40, -v176, v168, v40\n"
    "v_mfma_f32_16x16x32_bf16 v[60:63], v[136:139], a[236:239], v[60:63]\n"
    "buffer_load_dwordx4 v[236:239], v1, s[28:31], s94 offen offset:1024 sc1\n"
    "v_mfma_f32_16x16x32_bf16 v[64:67], v[140:143], a[232:235], v[64:67]\n"
    "ds_read_b128 v[164:167], v1 offset:64512\n"
    "v_fma_f32 v41, -v176, v169, v41\n"
    "v_mfma_f32_16x16x32_bf16 v[68:71], v[140:143], a[236:239], v[68:71]\n"
    "s_waitcnt lgkmcnt(5)\n"
    "v_fma_f32 v42, -v176, v170, v42\n"
    "v_fma_f32 v43, -v176, v171, v43\n"
    "v_mfma_f32_16x16x32_bf16 v[72:75], v[144:147], a[232:235], v[72:75]\n"
    "v_fma_f32 v44, -v179, v168, v44\n"
    "v_fma_f32 v45, -v179, v169, v45\n"
    "v_mfma_f32_16x16x32_bf16 v[76:79], v[144:147], a[236:239], v[76:79]\n"
    "v_fma_f32 v46, -v179, v170, v46\n"
    "v_fma_f32 v47, -v179, v171, v47\n"
    "v_mfma_f32_16x16x32_bf16 v[80:83], v[148:151], a[232:235], v[80:83]\n"
    "ds_read_b128 v[168:171], v191 offset:64\n"
    "ds_read_b128 v[136:139], v27 offset:0\n"
    "v_mfma_f32_16x16x32_bf16 v[84:87], v[148:151], a[236:239], v[84:87]\n"
    "s_waitcnt vmcnt(14) lgkmcnt(4)\n"
    "buffer_load_dwordx4 v[240:243], v1, s[28:31], s89 offen offset:2048 sc1\n"
    "v_mfma_f32_16x16x32_bf16 v[40:43], v[152:155], a[240:243], v[40:43]\n"
    "ds_read_b128 v[140:143], v27 offset:1024\n"
    "ds_read_b128 v[144:147], v27 offset:2048\n"
    "v_mfma_f32_16x16x32_bf16 v[44:47], v[152:155], a[244:247], v[44:47]\n"
    "v_mfma_f32_16x16x32_bf16 v[48:51], v[156:159], a[240:243], v[48:51]\n"
    "buffer_load_dwordx4 v[244:247], v1, s[28:31], s94 offen offset:2048 sc1\n"
    "v_mfma_f32_16x16x32_bf16 v[52:55], v[156:159], a[244:247], v[52:55]\n"
    "s_waitcnt lgkmcnt(4)\n"
    "ds_read_b128 v[148:151], v27 offset:3072\n"
    "v_mfma_f32_16x16x32_bf16 v[56:59], v[160:163], a[240:243], v[56:59]\n"
    "ds_read_b128 v[152:155], v27 offset:4096\n"
    "v_mfma_f32_16x16x32_bf16 v[60:63], v[160:163], a[244:247], v[60:63]\n"
    "v_mfma_f32_16x16x32_bf16 v[64:67], v[164:167], a[240:243], v[64:67]\n"
    "ds_read_b128 v[156:159], v27 offset:5120\n"
    "v_fma_f32 v48, -v176, v172, v48\n"
    "v_mfma_f32_16x16x32_bf16 v[68:71], v[164:167], a[244:247], v[68:71]\n"
    "s_waitcnt lgkmcnt(4)\n"
    "buffer_load_dwordx4 v[248:251], v1, s[28:31], s89 offen offset:3072 sc1\n"
    "v_mfma_f32_16x16x32_bf16 v[72:75], v[136:139], a[240:243], v[72:75]\n"
    "ds_read_b128 v[160:163], v27 offset:6144\n"
    "v_fma_f32 v49, -v176, v173, v49\n"
    "v_mfma_f32_16x16x32_bf16 v[76:79], v[136:139], a[244:247], v[76:79]\n"
    "v_fma_f32 v50, -v176, v174, v50\n"
    "v_fma_f32 v51, -v176, v175, v51\n"
    "v_mfma_f32_16x16x32_bf16 v[80:83], v[140:143], a[240:243], v[80:83]\n"
    "v_fma_f32 v52, -v179, v172, v52\n"
    "v_fma_f32 v53, -v179, v173, v53\n"
    "v_mfma_f32_16x16x32_bf16 v[84:87], v[140:143], a[244:247], v[84:87]\n"
    "s_waitcnt vmcnt(15) lgkmcnt(3)\n"
    "v_fma_f32 v54, -v179, v174, v54\n"
    "v_fma_f32 v55, -v179, v175, v55\n"
    "v_mfma_f32_16x16x32_bf16 v[40:43], v[144:147], a[248:251], v[40:43]\n"
    "ds_read_b128 v[172:175], v191 offset:448\n"
    "ds_read_b128 v[164:167], v27 offset:7168\n"
    "v_mfma_f32_16x16x32_bf16 v[44:47], v[144:147], a[252:255], v[44:47]\n"
    "buffer_load_dwordx4 v[252:255], v1, s[28:31], s94 offen offset:3072 sc1\n"
    "v_mfma_f32_16x16x32_bf16 v[48:51], v[148:151], a[248:251], v[48:51]\n"
    "ds_read_b128 v[136:139], v27 offset:8192\n"
    "ds_read_b128 v[140:143], v27 offset:9216\n"
    "v_mfma_f32_16x16x32_bf16 v[52:55], v[148:151], a[252:255], v[52:55]\n"
    "s_waitcnt lgkmcnt(5)\n"
    "v_mfma_f32_16x16x32_bf16 v[56:59], v[152:155], a[248:251], v[56:59]\n"
    "buffer_load_dwordx4 a[56:59], v1, s[28:31], s90 offen offset:2048 sc1\n"
    "v_mfma_f32_16x16x32_bf16 v[60:63], v[152:155], a[252:255], v[60:63]\n"
    "ds_read_b128 v[144:147], v27 offset:10240\n"
    "v_mfma_f32_16x16x32_bf16 v[64:67], v[156:159], a[248:251], v[64:67]\n"
    "ds_read_b128 v[148:151], v27 offset:11264\n"
    "v_mfma_f32_16x16x32_bf16 v[68:71], v[156:159], a[252:255], v[68:71]\n"
    "s_waitcnt lgkmcnt(4)\n"
    "v_mfma_f32_16x16x32_bf16 v[72:75], v[160:163], a[248:251], v[72:75]\n"
    "ds_read_b128 v[152:155], v27 offset:12288\n"
    "v_fma_f32 v56, -v176, v168, v56\n"
    "v_mfma_f32_16x16x32_bf16 v[76:79], v[160:163], a[252:255], v[76:79]\n"
    "buffer_load_dwordx4 a[60:63], v1, s[28:31], s90 offen offset:3072 sc1\n"
    "v_mfma_f32_16x16x32_bf16 v[80:83], v[164:167], a[248:251], v[80:83]\n"
    "ds_read_b128 v[156:159], v27 offset:13312\n"
    "v_fma_f32 v57, -v176, v169, v57\n"
    "v_mfma_f32_16x16x32_bf16 v[84:87], v[164:167], a[252:255], v[84:87]\n"
    "s_waitcnt vmcnt(16) lgkmcnt(4)\n"
    "v_fma_f32 v58, -v176, v170, v58\n"
    "v_fma_f32 v59, -v176, v171, v59\n"
    "v_mfma_f32_16x16x32_bf16 v[40:43], v[136:139], v[192:195], v[40:43]\n"
    "s_add_u32 s90, s82, 16384\n"
    "v_fma_f32 v60, -v179, v168, v60\n"
    "v_fma_f32 v61, -v179, v169, v61\n"
    "v_mfma_f32_16x16x32_bf16 v[44:47], v[136:139], v[196:199], v[44:47]\n"
    "v_fma_f32 v62, -v179, v170, v62\n"
    "v_fma_f32 v63, -v179, v171, v63\n"
    "v_mfma_f32_16x16x32_bf16 v[48:51], v[140:143], v[192:195], v[48:51]\n"
    "ds_read_b128 v[168:171], v191 offset:128\n"
    "ds_read_b128 v[160:163], v27 offset:14336\n"
    "v_mfma_f32_16x16x32_bf16 v[52:55], v[140:143], v[196:199], v[52:55]\n"
    "s_waitcnt lgkmcnt(4)\n"
    "buffer_load_dwordx4 a[64:67], v1, s[28:31], s90 offen sc1\n"
    "v_mfma_f32_16x16x32_bf16 v[56:59], v[144:147], v[192:195], v[56:59]\n"
    "ds_read_b128 v[164:167], v27 offset:15360\n"
    "ds_read_b128 v[136:139], v27 offset:16384\n"
    "v_mfma_f32_16x16x32_bf16 v[60:63], v[144:147], v[196:199], v[60:63]\n"
    "buffer_load_dwordx4 a[68:71], v1, s[28:31], s90 offen offset:1024 sc1\n"
    "v_mfma_f32_16x16x32_bf16 v[64:67], v[148:151], v[192:195], v[64:67]\n"
    "ds_read_b128 v[140:143], v27 offset:17408\n"
    "v_mfma_f32_16x16x32_bf16 v[68:71], v[148:151], v[196:199], v[68:71]\n"
    "s_waitcnt lgkmcnt(5)\n"
    "v_mfma_f32_16x16x32_bf16 v[72:75], v[152:155], v[192:195], v[72:75]\n"
    "ds_read_b128 v[144:147], v27 offset:18432\n"
    "v_mfma_f32_16x16x32_bf16 v[76:79], v[152:155], v[196:199], v[76:79]\n"
    "v_mfma_f32_16x16x32_bf16 v[80:83], v[156:159], v[192:195], v[80:83]\n"
    "ds_read_b128 v[148:151], v27 offset:19456\n"
    "v_fma_f32 v64, -v176, v172, v64\n"
    "v_mfma_f32_16x16x32_bf16 v[84:87], v[156:159], v[196:199], v[84:87]\n"
    "s_waitcnt vmcnt(16) lgkmcnt(4)\n"
    "buffer_load_dwordx4 a[72:75], v1, s[28:31], s90 offen offset:2048 sc1\n"
    "v_mfma_f32_16x16x32_bf16 v[40:43], v[160:163], v[200:203], v[40:43]\n"
    "ds_read_b128 v[152:155], v27 offset:20480\n"
    "v_fma_f32 v65, -v176, v173, v65\n"
    "v_mfma_f32_16x16x32_bf16 v[44:47], v[160:163], v[204:207], v[44:47]\n"
    "v_fma_f32 v66, -v176, v174, v66\n"
    "v_fma_f32 v67, -v176, v175, v67\n"
    "v_mfma_f32_16x16x32_bf16 v[48:51], v[164:167], v[200:203], v[48:51]\n"
    "v_fma_f32 v68, -v179, v172, v68\n"
    "v_fma_f32 v69, -v179, v173, v69\n"
    "v_mfma_f32_16x16x32_bf16 v[52:55], v[164:167], v[204:207], v[52:55]\n"
    "s_waitcnt lgkmcnt(3)\n"
    "v_fma_f32 v70, -v179, v174, v70\n"
    "v_fma_f32 v71, -v179, v175, v71\n"
    "v_mfma_f32_16x16x32_bf16 v[56:59], v[136:139], v[200:203], v[56:59]\n"
    "ds_read_b128 v[172:175], v191 offset:512\n"
    "ds_read_b128 v[156:159], v27 offset:21504\n"
    "v_mfma_f32_16x16x32_bf16 v[60:63], v[136:139], v[204:207], v[60:63]\n"
    "buffer_load_dwordx4 a[76:79], v1, s[28:31], s90 offen offset:3072 sc1\n"
    "v_mfma_f32_16x16x32_bf16 v[64:67], v[140:143], v[200:203], v[64:67]\n"
    "s_add_u32 s90, s82, 20480\n"
    "ds_read_b128 v[160:163], v27 offset:22528\n"
    "ds_read_b128 v[164:167], v27 offset:23552\n"
    "v_mfma_f32_16x16x32_bf16 v[68:71], v[140:143], v[204:207], v[68:71]\n"
    "s_waitcnt lgkmcnt(5)\n"
    "buffer_load_dwordx4 a[80:83], v1, s[28:31], s90 offen sc1\n"
    "v_mfma_f32_16x16x32_bf16 v[72:75], v[144:147], v[200:203], v[72:75]\n"
    "ds_read_b128 v[136:139], v27 offset:24576\n"
    "v_mfma_f32_16x16x32_bf16 v[76:79], v[144:147], v[204:207], v[76:79]\n"
    "v_mfma_f32_16x16x32_bf16 v[80:83], v[148:151], v[200:203], v[80:83]\n"
    "ds_read_b128 v[140:143], v27 offset:25600\n"
    "v_mfma_f32_16x16x32_bf16 v[84:87], v[148:151], v[204:207], v[84:87]\n"
    "s_waitcnt vmcnt(17) lgkmcnt(4)\n"
    "buffer_load_dwordx4 a[84:87], v1, s[28:31], s90 offen offset:1024 sc1\n"
    "v_mfma_f32_16x16x32_bf16 v[40:43], v[152:155], v[208:211], v[40:43]\n"
    "ds_read_b128 v[144:147], v27 offset:26624\n"
    "v_fma_f32 v72, -v176, v168, v72\n"
    "v_mfma_f32_16x16x32_bf16 v[44:47], v[152:155], v[212:215], v[44:47]\n"
    "v_fma_f32 v73, -v176, v169, v73\n"
    "v_fma_f32 v74, -v176, v170, v74\n"
    "v_mfma_f32_16x16x32_bf16 v[48:51], v[156:159], v[208:211], v[48:51]\n"
    "ds_read_b128 v[148:151], v27 offset:27648\n"
    "v_fma_f32 v75, -v176, v171, v75\n"
    "v_mfma_f32_16x16x32_bf16 v[52:55], v[156:159], v[212:215], v[52:55]\n"
    "s_waitcnt lgkmcnt(4)\n"
    "v_fma_f32 v76, -v179, v168, v76\n"
    "v_fma_f32 v77, -v179, v169, v77\n"
    "v_mfma_f32_16x16x32_bf16 v[56:59], v[160:163], v[208:211], v[56:59]\n"
    "v_fma_f32 v78, -v179, v170, v78\n"
    "v_fma_f32 v79, -v179, v171, v79\n"
    "v_mfma_f32_16x16x32_bf16 v[60:63], v[160:163], v[212:215], v[60:63]\n"
    "ds_read_b128 v[168:171], v191 offset:192\n"
    "ds_read_b128 v[152:155], v27 offset:28672\n"
    "v_mfma_f32_16x16x32_bf16 v[64:67], v[164:167], v[208:211], v[64:67]\n"
    "buffer_load_dwordx4 a[88:91], v1, s[28:31], s90 offen offset:2048 sc1\n"
    "v_mfma_f32_16x16x32_bf16 v[68:71], v[164:167], v[212:215], v[68:71]\n"
    "s_waitcnt lgkmcnt(4)\n"
    "ds_read_b128 v[156:159], v27 offset:29696\n"
    "v_mfma_f32_16x16x32_bf16 v[72:75], v[136:139], v[208:211], v[72:75]\n"
    "buffer_load_dwordx4 a[92:95], v1, s[28:31], s90 offen offset:3072 sc1\n"
    "v_mfma_f32_16x16x32_bf16 v[76:79], v[136:139], v[212:215], v[76:79]\n"
    "ds_read_b128 v[160:163], v27 offset:30720\n"
    "v_mfma_f32_16x16x32_bf16 v[80:83], v[140:143], v[208:211], v[80:83]\n"
    "ds_read_b128 v[164:167], v27 offset:31744\n"
    "v_mfma_f32_16x16x32_bf16 v[84:87], v[140:143], v[212:215], v[84:87]\n"
    "s_waitcnt vmcnt(18) lgkmcnt(5)\n"
    "v_mfma_f32_16x16x32_bf16 v[40:43], v[144:147], v[216:219], v[40:43]\n"
    "s_add_u32 s90, s82, 24576\n"
    "ds_read_b128 v[136:139], v26 offset:0\n"
    "v_mfma_f32_16x16x32_bf16 v[44:47], v[144:147], v[220:223], v[44:47]\n"
    "buffer_load_dwordx4 a[96:99], v1, s[28:31], s90 offen sc1\n"
    "v_mfma_f32_16x16x32_bf16 v[48:51], v[148:151], v[216:219], v[48:51]\n"
    "ds_read_b128 v[140:143], v26 offset:1024\n"
    "v_fma_f32 v80, -v176, v172, v80\n"
    "v_mfma_f32_16x16x32_bf16 v[52:55], v[148:151], v[220:223], v[52:55]\n"
    "s_waitcnt lgkmcnt(4)\n"
    "v_fma_f32 v81, -v176, v173, v81\n"
    "v_fma_f32 v82, -v176, v174, v82\n"
    "v_mfma_f32_16x16x32_bf16 v[56:59], v[152:155], v[216:219], v[56:59]\n"
    "ds_read_b128 v[144:147], v26 offset:2048\n"
    "v_fma_f32 v83, -v176, v175, v83\n"
    "v_mfma_f32_16x16x32_bf16 v[60:63], v[152:155], v[220:223], v[60:63]\n"
    "buffer_load_dwordx4 a[100:103], v1, s[28:31], s90 offen offset:1024 sc1\n"
    "v_mfma_f32_16x16x32_bf16 v[64:67], v[156:159], v[216:219], v[64:67]\n"
    "v_fma_f32 v84, -v179, v172, v84\n"
    "v_fma_f32 v85, -v179, v173, v85\n"
    "v_mfma_f32_16x16x32_bf16 v[68:71], v[156:159], v[220:223], v[68:71]\n"
    "s_waitcnt lgkmcnt(3)\n"
    "v_fma_f32 v86, -v179, v174, v86\n"
    "v_fma_f32 v87, -v179, v175, v87\n"
    "v_mfma_f32_16x16x32_bf16 v[72:75], v[160:163], v[216:219], v[72:75]\n"
    "ds_read_b128 v[148:151], v26 offset:3072\n"
    "ds_read_b128 v[152:155], v26 offset:4096\n"
    "v_mfma_f32_16x16x32_bf16 v[76:79], v[160:163], v[220:223], v[76:79]\n"
    "ds_read_b128 v[172:175], v191 offset:576\n"
    "v_mul_f32_e64 v88, -v176, v168\n"
    "v_mfma_f32_16x16x32_bf16 v[80:83], v[164:167], v[216:219], v[80:83]\n"
    "ds_read_b128 v[156:159], v26 offset:5120\n"
    "v_mul_f32_e64 v89, -v176, v169\n"
    "v_mfma_f32_16x16x32_bf16 v[84:87], v[164:167], v[220:223], v[84:87]\n"
    "s_waitcnt vmcnt(18) lgkmcnt(5)\n"
    "v_mul_f32_e64 v90, -v176, v170\n"
    "v_mul_f32_e64 v91, -v176, v171\n"
    "v_mfma_f32_16x16x32_bf16 v[40:43], v[136:139], v[224:227], v[40:43]\n"
    "ds_read_b128 v[160:163], v26 offset:6144\n"
    "v_mul_f32_e64 v92, -v179, v168\n"
    "v_mfma_f32_16x16x32_bf16 v[44:47], v[136:139], v[228:231], v[44:47]\n"
    "s_barrier\n"
    "v_mul_f32_e64 v93, -v179, v169\n"
    "v_mul_f32_e64 v94, -v179, v170\n"
    "v_mfma_f32_16x16x32_bf16 v[48:51], v[140:143], v[224:227], v[48:51]\n"
    "ds_read_b128 v[164:167], v26 offset:7168\n"
    "v_mul_f32_e64 v95, -v179, v171\n"
    "v_mfma_f32_16x16x32_bf16 v[52:55], v[140:143], v[228:231], v[52:55]\n"
    "s_waitcnt lgkmcnt(5)\n"
    "s_add_u32 m0, s86, 0\n"
    "s_add_u32 s88, s83, 147456\n"
    "v_mfma_f32_16x16x32_bf16 v[56:59], v[144:147], v[224:227], v[56:59]\n"
    "ds_read_b128 v[136:139], v26 offset:8192\n"
    "ds_read_b128 v[168:171], v191 offset:256\n"
    "v_mfma_f32_16x16x32_bf16 v[60:63], v[144:147], v[228:231], v[60:63]\n"
    "buffer_load_dwordx4 v1, s[28:31], s88 offen sc1 lds\n"
    "v_mfma_f32_16x16x32_bf16 v[64:67], v[148:151], v[224:227], v[64:67]\n"
    "ds_read_b128 v[140:143], v26 offset:9216\n"
    "s_waitcnt lgkmcnt(6)\n"
    "v_mul_f32_e64 v96, -v176, v172\n"
    "v_mfma_f32_16x16x32_bf16 v[68:71], v[148:151], v[228:231], v[68:71]\n"
    "s_waitcnt lgkmcnt(5)\n"
    "v_mul_f32_e64 v97, -v176, v173\n"
    "v_mul_f32_e64 v98, -v176, v174\n"
    "v_mfma_f32_16x16x32_bf16 v[72:75], v[152:155], v[224:227], v[72:75]\n"
    "ds_read_b128 v[144:147], v26 offset:10240\n"
    "v_mul_f32_e64 v99, -v176, v175\n"
    "v_mfma_f32_16x16x32_bf16 v[76:79], v[152:155], v[228:231], v[76:79]\n"
    "buffer_load_dwordx4 v1, s[28:31], s88 offen offset:1024 sc1 lds\n"
    "v_mfma_f32_16x16x32_bf16 v[80:83], v[156:159], v[224:227], v[80:83]\n"
    "ds_read_b128 v[148:151], v26 offset:11264\n"
    "v_mul_f32_e64 v100, -v179, v172\n"
    "v_mfma_f32_16x16x32_bf16 v[84:87], v[156:159], v[228:231], v[84:87]\n"
    "s_waitcnt vmcnt(18) lgkmcnt(5)\n"
    "v_mul_f32_e64 v101, -v179, v173\n"
    "v_mul_f32_e64 v102, -v179, v174\n"
    "v_mfma_f32_16x16x32_bf16 v[40:43], v[160:163], v[232:235], v[40:43]\n"
    "ds_read_b128 v[152:155], v26 offset:12288\n"
    "v_mul_f32_e64 v103, -v179, v175\n"
    "v_mfma_f32_16x16x32_bf16 v[44:47], v[160:163], v[236:239], v[44:47]\n"
    "buffer_load_dwordx4 v1, s[28:31], s88 offen offset:2048 sc1 lds\n"
    "v_mfma_f32_16x16x32_bf16 v[48:51], v[164:167], v[232:235], v[48:51]\n"
    "ds_read_b128 v[156:159], v26 offset:13312\n"
    "ds_read_b128 v[172:175], v191 offset:640\n"
    "v_mfma_f32_16x16x32_bf16 v[52:55], v[164:167], v[236:239], v[52:55]\n"
    "s_waitcnt lgkmcnt(5)\n"
    "v_mul_f32_e64 v104, -v176, v168\n"
    "v_mul_f32_e64 v105, -v176, v169\n"
    "v_mfma_f32_16x16x32_bf16 v[56:59], v[136:139], v[232:235], v[56:59]\n"
    "ds_read_b128 v[160:163], v26 offset:14336\n"
    "v_mul_f32_e64 v106, -v176, v170\n"
    "v_mfma_f32_16x16x32_bf16 v[60:63], v[136:139], v[236:239], v[60:63]\n"
    "buffer_load_dwordx4 v1, s[28:31], s88 offen offset:3072 sc1 lds\n"
    "v_mfma_f32_16x16x32_bf16 v[64:67], v[140:143], v[232:235], v[64:67]\n"
    "ds_read_b128 v[164:167], v26 offset:15360\n"
    "v_mul_f32_e64 v107, -v176, v171\n"
    "v_mfma_f32_16x16x32_bf16 v[68:71], v[140:143], v[236:239], v[68:71]\n"
    "s_waitcnt lgkmcnt(5)\n"
    "v_mul_f32_e64 v108, -v179, v168\n"
    "v_mul_f32_e64 v109, -v179, v169\n"
    "v_mfma_f32_16x16x32_bf16 v[72:75], v[144:147], v[232:235], v[72:75]\n"
    "ds_read_b128 v[136:139], v26 offset:16384\n"
    "v_mul_f32_e64 v110, -v179, v170\n"
    "v_mfma_f32_16x16x32_bf16 v[76:79], v[144:147], v[236:239], v[76:79]\n"
    "s_add_u32 m0, s86, 16384\n"
    "s_add_u32 s88, s83, 163840\n"
    "v_mul_f32_e64 v111, -v179, v171\n"
    "v_mfma_f32_16x16x32_bf16 v[80:83], v[148:151], v[232:235], v[80:83]\n"
    "ds_read_b128 v[140:143], v26 offset:17408\n"
    "ds_read_b128 v[168:171], v191 offset:320\n"
    "v_mfma_f32_16x16x32_bf16 v[84:87], v[148:151], v[236:239], v[84:87]\n"
    "s_waitcnt vmcnt(18) lgkmcnt(6)\n"
    "buffer_load_dwordx4 v1, s[28:31], s88 offen sc1 lds\n"
    "v_mfma_f32_16x16x32_bf16 v[40:43], v[152:155], v[240:243], v[40:43]\n"
    "ds_read_b128 v[144:147], v26 offset:18432\n"
    "s_waitcnt lgkmcnt(6)\n"
    "v_mul_f32_e64 v112, -v176, v172\n"
    "v_mfma_f32_16x16x32_bf16 v[44:47], v[152:155], v[244:247], v[44:47]\n"
    "v_mul_f32_e64 v113, -v176, v173\n"
    "v_mul_f32_e64 v114, -v176, v174\n"
    "v_mfma_f32_16x16x32_bf16 v[48:51], v[156:159], v[240:243], v[48:51]\n"
    "ds_read_b128 v[148:151], v26 offset:19456\n"
    "v_mul_f32_e64 v115, -v176, v175\n"
    "v_mfma_f32_16x16x32_bf16 v[52:55], v[156:159], v[244:247], v[52:55]\n"
    "s_waitcnt lgkmcnt(5)\n"
    "buffer_load_dwordx4 v1, s[28:31], s88 offen offset:1024 sc1 lds\n"
    "v_mfma_f32_16x16x32_bf16 v[56:59], v[160:163], v[240:243], v[56:59]\n"
    "ds_read_b128 v[152:155], v26 offset:20480\n"
    "v_mul_f32_e64 v116, -v179, v172\n"
    "v_mfma_f32_16x16x32_bf16 v[60:63], v[160:163], v[244:247], v[60:63]\n"
    "v_mul_f32_e64 v117, -v179, v173\n"
    "v_mul_f32_e64 v118, -v179, v174\n"
    "v_mfma_f32_16x16x32_bf16 v[64:67], v[164:167], v[240:243], v[64:67]\n"
    "ds_read_b128 v[156:159], v26 offset:21504\n"
    "v_mul_f32_e64 v119, -v179, v175\n"
    "v_mfma_f32_16x16x32_bf16 v[68:71], v[164:167], v[244:247], v[68:71]\n"
    "s_waitcnt lgkmcnt(5)\n"
    "buffer_load_dwordx4 v1, s[28:31], s88 offen offset:2048 sc1 lds\n"
    "v_mfma_f32_16x16x32_bf16 v[72:75], v[136:139], v[240:243], v[72:75]\n"
    "ds_read_b128 v[160:163], v26 offset:22528\n"
    "ds_read_b128 v[172:175], v191 offset:704\n"
    "v_mfma_f32_16x16x32_bf16 v[76:79], v[136:139], v[244:247], v[76:79]\n"
    "s_waitcnt lgkmcnt(6)\n"
    "v_mul_f32_e64 v120, -v176, v168\n"
    "v_mul_f32_e64 v121, -v176, v169\n"
    "v_mfma_f32_16x16x32_bf16 v[80:83], v[140:143], v[240:243], v[80:83]\n"
    "ds_read_b128 v[164:167], v26 offset:23552\n"
    "v_mul_f32_e64 v122, -v176, v170\n"
    "v_mfma_f32_16x16x32_bf16 v[84:87], v[140:143], v[244:247], v[84:87]\n"
    "s_waitcnt vmcnt(19) lgkmcnt(5)\n"
    "buffer_load_dwordx4 v1, s[28:31], s88 offen offset:3072 sc1 lds\n"
    "v_mfma_f32_16x16x32_bf16 v[40:43], v[144:147], v[248:251], v[40:43]\n"
    "ds_read_b128 v[136:139], v26 offset:24576\n"
    "v_mul_f32_e64 v123, -v176, v171\n"
    "v_mfma_f32_16x16x32_bf16 v[44:47], v[144:147], v[252:255], v[44:47]\n"
    "v_mul_f32_e64 v124, -v179, v168\n"
    "v_mul_f32_e64 v125, -v179, v169\n"
    "v_mfma_f32_16x16x32_bf16 v[48:51], v[148:151], v[248:251], v[48:51]\n"
    "ds_read_b128 v[140:143], v26 offset:25600\n"
    "v_mul_f32_e64 v126, -v179, v170\n"
    "v_mfma_f32_16x16x32_bf16 v[52:55], v[148:151], v[252:255], v[52:55]\n"
    "s_waitcnt lgkmcnt(5)\n"
    "s_add_u32 m0, s86, 32768\n"
    "s_add_u32 s88, s83, 180224\n"
    "v_mfma_f32_16x16x32_bf16 v[56:59], v[152:155], v[248:251], v[56:59]\n"
    "ds_read_b128 v[144:147], v26 offset:26624\n"
    "v_mul_f32_e64 v127, -v179, v171\n"
    "v_mfma_f32_16x16x32_bf16 v[60:63], v[152:155], v[252:255], v[60:63]\n"
    "buffer_load_dwordx4 v1, s[28:31], s88 offen sc1 lds\n"
    "v_mfma_f32_16x16x32_bf16 v[64:67], v[156:159], v[248:251], v[64:67]\n"
    "ds_read_b128 v[148:151], v26 offset:27648\n"
    "s_waitcnt lgkmcnt(5)\n"
    "v_mul_f32_e64 v128, -v176, v172\n"
    "v_mfma_f32_16x16x32_bf16 v[68:71], v[156:159], v[252:255], v[68:71]\n"
    "s_waitcnt lgkmcnt(4)\n"
    "v_mul_f32_e64 v129, -v176, v173\n"
    "v_mul_f32_e64 v130, -v176, v174\n"
    "v_mfma_f32_16x16x32_bf16 v[72:75], v[160:163], v[248:251], v[72:75]\n"
    "v_mul_f32_e64 v131, -v176, v175\n"
    "v_mul_f32_e64 v132, -v179, v172\n"
    "v_mfma_f32_16x16x32_bf16 v[76:79], v[160:163], v[252:255], v[76:79]\n"
    "v_mul_f32_e64 v133, -v179, v173\n"
    "v_mul_f32_e64 v134, -v179, v174\n"
    "v_mul_f32_e64 v135, -v179, v175\n"
    "v_mfma_f32_16x16x32_bf16 v[80:83], v[164:167], v[248:251], v[80:83]\n"
    "ds_read_b128 v[152:155], v26 offset:28672\n"
    "ds_read_b128 v[156:159], v26 offset:29696\n"
    "v_mfma_f32_16x16x32_bf16 v[84:87], v[164:167], v[252:255], v[84:87]\n"
    "s_waitcnt lgkmcnt(4)\n"
    "buffer_load_dwordx4 v1, s[28:31], s88 offen offset:1024 sc1 lds\n"
    "v_mfma_f32_16x16x32_bf16 v[88:91], v[136:139], a[160:163], v[88:91]\n"
    "ds_read_b128 v[160:163], v26 offset:30720\n"
    "v_mfma_f32_16x16x32_bf16 v[92:95], v[136:139], a[164:167], v[92:95]\n"
    "buffer_load_dwordx4 v1, s[28:31], s88 offen offset:2048 sc1 lds\n"
    "v_mfma_f32_16x16x32_bf16 v[96:99], v[140:143], a[160:163], v[96:99]\n"
    "ds_read_b128 v[164:167], v26 offset:31744\n"
    "v_mfma_f32_16x16x32_bf16 v[100:103], v[140:143], a[164:167], v[100:103]\n"
    "s_waitcnt lgkmcnt(4)\n"
    "v_mfma_f32_16x16x32_bf16 v[88:91], v[144:147], a[168:171], v[88:91]\n"
    "ds_read_b128 v[136:139], v26 offset:32768\n"
    "v_mfma_f32_16x16x32_bf16 v[92:95], v[144:147], a[172:175], v[92:95]\n"
    "buffer_load_dwordx4 v1, s[28:31], s88 offen offset:3072 sc1 lds\n"
    "v_mfma_f32_16x16x32_bf16 v[96:99], v[148:151], a[168:171], v[96:99]\n"
    "ds_read_b128 v[140:143], v26 offset:33792\n"
    "v_mul_f32 v40, v177, v40\n"
    "v_mfma_f32_16x16x32_bf16 v[100:103], v[148:151], a[172:175], v[100:103]\n"
    "s_waitcnt lgkmcnt(4)\n"
    "v_mul_f32 v44, v180, v44\n"
    "v_mul_f32 v41, v177, v41\n"
    "v_mfma_f32_16x16x32_bf16 v[88:91], v[152:155], a[176:179], v[88:91]\n"
    "ds_read_b128 v[144:147], v26 offset:34816\n"
    "v_mul_f32 v45, v180, v45\n"
    "v_mfma_f32_16x16x32_bf16 v[92:95], v[152:155], a[180:183], v[92:95]\n"
    "s_add_u32 m0, s86, 49152\n"
    "v_exp_f32 v40, v40\n"
    "v_mfma_f32_16x16x32_bf16 v[96:99], v[156:159], a[176:179], v[96:99]\n"
    "s_add_u32 s88, s83, 196608\n"
    "ds_read_b128 v[148:151], v26 offset:35840\n"
    "v_mul_f32 v42, v177, v42\n"
    "v_mfma_f32_16x16x32_bf16 v[100:103], v[156:159], a[180:183], v[100:103]\n"
    "s_waitcnt lgkmcnt(4)\n"
    "buffer_load_dwordx4 v1, s[28:31], s88 offen sc1 lds\n"
    "v_mfma_f32_16x16x32_bf16 v[88:91], v[160:163], a[184:187], v[88:91]\n"
    "v_exp_f32 v44, v44\n"
    "v_mfma_f32_16x16x32_bf16 v[92:95], v[160:163], a[188:191], v[92:95]\n"
    "ds_read_b128 v[152:155], v26 offset:36864\n"
    "v_mul_f32 v46, v180, v46\n"
    "v_mfma_f32_16x16x32_bf16 v[96:99], v[164:167], a[184:187], v[96:99]\n"
    "buffer_load_dwordx4 v1, s[28:31], s88 offen offset:1024 sc1 lds\n"
    "v_mfma_f32_16x16x32_bf16 v[100:103], v[164:167], a[188:191], v[100:103]\n"
    "s_waitcnt lgkmcnt(3)\n"
    "v_exp_f32 v41, v41\n"
    "v_mfma_f32_16x16x32_bf16 v[88:91], v[136:139], a[192:195], v[88:91]\n"
    "ds_read_b128 v[156:159], v26 offset:37888\n"
    "ds_read_b128 v[160:163], v26 offset:38912\n"
    "v_mfma_f32_16x16x32_bf16 v[92:95], v[136:139], a[196:199], v[92:95]\n"
    "v_exp_f32 v45, v45\n"
    "v_mfma_f32_16x16x32_bf16 v[96:99], v[140:143], a[192:195], v[96:99]\n"
    "ds_read_b128 v[164:167], v26 offset:39936\n"
    "v_mul_f32 v43, v177, v43\n"
    "v_mfma_f32_16x16x32_bf16 v[100:103], v[140:143], a[196:199], v[100:103]\n"
    "s_waitcnt lgkmcnt(4)\n"
    "buffer_load_dwordx4 v1, s[28:31], s88 offen offset:2048 sc1 lds\n"
    "v_mfma_f32_16x16x32_bf16 v[88:91], v[144:147], a[200:203], v[88:91]\n"
    "ds_read_b128 v[136:139], v26 offset:40960\n"
    "v_mul_f32 v47, v180, v47\n"
    "v_mfma_f32_16x16x32_bf16 v[92:95], v[144:147], a[204:207], v[92:95]\n"
    "v_exp_f32 v42, v42\n"
    "v_mfma_f32_16x16x32_bf16 v[96:99], v[148:151], a[200:203], v[96:99]\n"
    "ds_read_b128 v[140:143], v26 offset:41984\n"
    "v_fma_f32 v40, v40, v178, v178\n"
    "v_mfma_f32_16x16x32_bf16 v[100:103], v[148:151], a[204:207], v[100:103]\n"
    "s_waitcnt lgkmcnt(4)\n"
    "buffer_load_dwordx4 v1, s[28:31], s88 offen offset:3072 sc1 lds\n"
    "v_mfma_f32_16x16x32_bf16 v[88:91], v[152:155], a[208:211], v[88:91]\n"
    "v_exp_f32 v46, v46\n"
    "v_mfma_f32_16x16x32_bf16 v[92:95], v[152:155], a[212:215], v[92:95]\n"
    "ds_read_b128 v[144:147], v26 offset:43008\n"
    "v_fma_f32 v44, v44, v181, v181\n"
    "v_mfma_f32_16x16x32_bf16 v[96:99], v[156:159], a[208:211], v[96:99]\n"
    "v_exp_f32 v43, v43\n"
    "v_mfma_f32_16x16x32_bf16 v[100:103], v[156:159], a[212:215], v[100:103]\n"
    "s_waitcnt lgkmcnt(3)\n"
    "ds_read_b128 v[148:151], v26 offset:44032\n"
    "s_add_u32 m0, s86, 65536\n"
    "v_mfma_f32_16x16x32_bf16 v[88:91], v[160:163], a[216:219], v[88:91]\n"
    "s_add_u32 s88, s83, 212992\n"
    "ds_read_b128 v[152:155], v26 offset:45056\n"
    "v_fma_f32 v41, v41, v178, v178\n"
    "v_mfma_f32_16x16x32_bf16 v[92:95], v[160:163], a[220:223], v[92:95]\n"
    "buffer_load_dwordx4 v1, s[28:31], s88 offen sc1 lds\n"
    "v_mfma_f32_16x16x32_bf16 v[96:99], v[164:167], a[216:219], v[96:99]\n"
    "v_exp_f32 v47, v47\n"
    "v_mfma_f32_16x16x32_bf16 v[100:103], v[164:167], a[220:223], v[100:103]\n"
    "s_waitcnt lgkmcnt(3)\n"
    "ds_read_b128 v[156:159], v26 offset:46080\n"
    "v_fma_f32 v45, v45, v181, v181\n"
    "v_mfma_f32_16x16x32_bf16 v[88:91], v[136:139], a[224:227], v[88:91]\n"
    "buffer_load_dwordx4 v1, s[28:31], s88 offen offset:1024 sc1 lds\n"
    "v_mfma_f32_16x16x32_bf16 v[92:95], v[136:139], a[228:231], v[92:95]\n"
    "v_rcp_f32 v40, v40\n"
    "v_mfma_f32_16x16x32_bf16 v[96:99], v[140:143], a[224:227], v[96:99]\n"
    "ds_read_b128 v[160:163], v26 offset:47104\n"
    "ds_read_b128 v[164:167], v26 offset:48128\n"
    "v_mfma_f32_16x16x32_bf16 v[100:103], v[140:143], a[228:231], v[100:103]\n"
    "s_waitcnt vmcnt(14) lgkmcnt(4)\n"
    "s_barrier\n"
    "v_fma_f32 v42, v42, v178, v178\n"
    "v_mfma_f32_16x16x32_bf16 v[88:91], v[144:147], a[232:235], v[88:91]\n"
    "v_rcp_f32 v44, v44\n"
    "v_mfma_f32_16x16x32_bf16 v[92:95], v[144:147], a[236:239], v[92:95]\n"
    "ds_read_b128 v[136:139], v1 offset:0\n"
    "v_fma_f32 v46, v46, v181, v181\n"
    "v_mfma_f32_16x16x32_bf16 v[96:99], v[148:151], a[232:235], v[96:99]\n"
    "buffer_load_dwordx4 v1, s[28:31], s88 offen offset:2048 sc1 lds\n"
    "v_mfma_f32_16x16x32_bf16 v[100:103], v[148:151], a[236:239], v[100:103]\n"
    "s_waitcnt lgkmcnt(3)\n"
    "v_rcp_f32 v41, v41\n"
    "v_mfma_f32_16x16x32_bf16 v[88:91], v[152:155], a[240:243], v[88:91]\n"
    "ds_read_b128 v[140:143], v1 offset:1024\n"
    "ds_read_b128 v[144:147], v1 offset:2048\n"
    "v_mfma_f32_16x16x32_bf16 v[92:95], v[152:155], a[244:247], v[92:95]\n"
    "buffer_load_dwordx4 v1, s[28:31], s88 offen offset:3072 sc1 lds\n"
    "v_mfma_f32_16x16x32_bf16 v[96:99], v[156:159], a[240:243], v[96:99]\n"
    "ds_read_b128 v[148:151], v1 offset:3072\n"
    "v_fma_f32 v43, v43, v178, v178\n"
    "v_mfma_f32_16x16x32_bf16 v[100:103], v[156:159], a[244:247], v[100:103]\n"
    "s_waitcnt lgkmcnt(4)\n"
    "v_rcp_f32 v45, v45\n"
    "v_mfma_f32_16x16x32_bf16 v[88:91], v[160:163], a[248:251], v[88:91]\n"
    "ds_read_b128 v[152:155], v1 offset:4096\n"
    "v_fma_f32 v47, v47, v181, v181\n"
    "v_mfma_f32_16x16x32_bf16 v[92:95], v[160:163], a[252:255], v[92:95]\n"
    "s_add_u32 m0, s86, 81920\n"
    "v_rcp_f32 v42, v42\n"
    "v_mfma_f32_16x16x32_bf16 v[96:99], v[164:167], a[248:251], v[96:99]\n"
    "s_add_u32 s88, s83, 229376\n"
    "ds_read_b128 v[156:159], v1 offset:5120\n"
    "v_mul_f32 v48, v48, v40\n"
    "v_mfma_f32_16x16x32_bf16 v[100:103], v[164:167], a[252:255], v[100:103]\n"
    "s_waitcnt lgkmcnt(4)\n"
    "buffer_load_dwordx4 v1, s[28:31], s88 offen sc1 lds\n"
    "v_mfma_f32_16x16x32_bf16 v[88:91], v[136:139], v[192:195], v[88:91]\n"
    "v_rcp_f32 v46, v46\n"
    "v_mfma_f32_16x16x32_bf16 v[92:95], v[136:139], v[196:199], v[92:95]\n"
    "ds_read_b128 v[160:163], v1 offset:6144\n"
    "v_mul_f32 v52, v52, v44\n"
    "v_mfma_f32_16x16x32_bf16 v[96:99], v[140:143], v[192:195], v[96:99]\n"
    "buffer_load_dwordx4 v1, s[28:31], s88 offen offset:1024 sc1 lds\n"
    "v_mfma_f32_16x16x32_bf16 v[100:103], v[140:143], v[196:199], v[100:103]\n"
    "s_waitcnt lgkmcnt(3)\n"
    "v_rcp_f32 v43, v43\n"
    "v_mfma_f32_16x16x32_bf16 v[88:91], v[144:147], v[200:203], v[88:91]\n"
    "ds_read_b128 v[164:167], v1 offset:7168\n"
    "ds_read_b128 v[136:139], v1 offset:8192\n"
    "v_mfma_f32_16x16x32_bf16 v[92:95], v[144:147], v[204:207], v[92:95]\n"
    "v_rcp_f32 v47, v47\n"
    "v_mfma_f32_16x16x32_bf16 v[96:99], v[148:151], v[200:203], v[96:99]\n"
    "ds_read_b128 v[140:143], v1 offset:9216\n"
    "v_mul_f32 v49, v49, v41\n"
    "v_mfma_f32_16x16x32_bf16 v[100:103], v[148:151], v[204:207], v[100:103]\n"
    "s_waitcnt lgkmcnt(4)\n"
    "buffer_load_dwordx4 v1, s[28:31], s88 offen offset:2048 sc1 lds\n"
    "v_mfma_f32_16x16x32_bf16 v[88:91], v[152:155], v[208:211], v[88:91]\n"
    "ds_read_b128 v[144:147], v1 offset:10240\n"
    "v_mul_f32 v53, v53, v45\n"
    "v_mfma_f32_16x16x32_bf16 v[92:95], v[152:155], v[212:215], v[92:95]\n"
    "v_mul_f32 v50, v50, v42\n"
    "v_mul_f32 v54, v54, v46\n"
    "v_mfma_f32_16x16x32_bf16 v[96:99], v[156:159], v[208:211], v[96:99]\n"
    "ds_read_b128 v[148:151], v1 offset:11264\n"
    "v_cvt_pk_bf16_f32 v182, v48, v49\n"
    "v_mfma_f32_16x16x32_bf16 v[100:103], v[156:159], v[212:215], v[100:103]\n"
    "s_waitcnt lgkmcnt(4)\n"
    "buffer_load_dwordx4 v1, s[28:31], s88 offen offset:3072 sc1 lds\n"
    "v_mfma_f32_16x16x32_bf16 v[88:91], v[160:163], v[216:219], v[88:91]\n"
    "ds_read_b128 v[152:155], v1 offset:12288\n"
    "v_cvt_pk_bf16_f32 v186, v52, v53\n"
    "v_mfma_f32_16x16x32_bf16 v[92:95], v[160:163], v[220:223], v[92:95]\n"
    "v_mul_f32 v51, v51, v43\n"
    "v_mul_f32 v55, v55, v47\n"
    "v_mfma_f32_16x16x32_bf16 v[96:99], v[164:167], v[216:219], v[96:99]\n"
    "ds_read_b128 v[156:159], v1 offset:13312\n"
    "v_cvt_pk_bf16_f32 v183, v50, v51\n"
    "v_mfma_f32_16x16x32_bf16 v[100:103], v[164:167], v[220:223], v[100:103]\n"
    "s_waitcnt lgkmcnt(4)\n"
    "v_mul_f32 v56, v177, v56\n"
    "v_mul_f32 v57, v177, v57\n"
    "v_mfma_f32_16x16x32_bf16 v[88:91], v[136:139], v[224:227], v[88:91]\n"
    "ds_read_b128 v[160:163], v1 offset:14336\n"
    "v_cvt_pk_bf16_f32 v187, v54, v55\n"
    "v_mfma_f32_16x16x32_bf16 v[92:95], v[136:139], v[228:231], v[92:95]\n"
    "buffer_load_dwordx4 a[104:107], v1, s[28:31], s90 offen offset:2048 sc1\n"
    "v_mfma_f32_16x16x32_bf16 v[96:99], v[140:143], v[224:227], v[96:99]\n"
    "ds_read_b128 v[164:167], v1 offset:15360\n"
    "s_waitcnt vmcnt(17)\n"
    "v_mfma_f32_16x16x32_bf16 v[100:103], v[140:143], v[228:231], v[100:103]\n"
    "s_waitcnt lgkmcnt(4)\n"
    "s_barrier\n"
    "v_mul_f32 v60, v180, v60\n"
    "v_mfma_f32_16x16x32_bf16 v[88:91], v[144:147], v[232:235], v[88:91]\n"
    "ds_read_b128 v[136:139], v1 offset:16384\n"
    "v_mul_f32 v61, v180, v61\n"
    "v_mfma_f32_16x16x32_bf16 v[92:95], v[144:147], v[236:239], v[92:95]\n"
    "buffer_load_dwordx4 a[108:111], v1, s[28:31], s90 offen offset:3072 sc1\n"
    "v_mfma_f32_16x16x32_bf16 v[96:99], v[148:151], v[232:235], v[96:99]\n"
    "v_exp_f32 v56, v56\n"
    "v_mfma_f32_16x16x32_bf16 v[100:103], v[148:151], v[236:239], v[100:103]\n"
    "s_waitcnt lgkmcnt(3)\n"
    "ds_read_b128 v[140:143], v1 offset:17408\n"
    "v_mul_f32 v58, v177, v58\n"
    "v_mfma_f32_16x16x32_bf16 v[88:91], v[152:155], v[240:243], v[88:91]\n"
    "s_add_u32 s90, s82, 28672\n"
    "v_exp_f32 v57, v57\n"
    "v_mfma_f32_16x16x32_bf16 v[92:95], v[152:155], v[244:247], v[92:95]\n"
    "ds_read_b128 v[144:147], v1 offset:18432\n"
    "v_mul_f32 v59, v177, v59\n"
    "v_mfma_f32_16x16x32_bf16 v[96:99], v[156:159], v[240:243], v[96:99]\n"
    "buffer_load_dwordx4 a[112:115], v1, s[28:31], s90 offen sc1\n"
    "v_mfma_f32_16x16x32_bf16 v[100:103], v[156:159], v[244:247], v[100:103]\n"
    "s_waitcnt lgkmcnt(3)\n"
    "v_exp_f32 v58, v58\n"
    "v_mfma_f32_16x16x32_bf16 v[88:91], v[160:163], v[248:251], v[88:91]\n"
    "ds_read_b128 v[148:151], v1 offset:19456\n"
    "ds_read_b128 v[152:155], v1 offset:20480\n"
    "v_mfma_f32_16x16x32_bf16 v[92:95], v[160:163], v[252:255], v[92:95]\n"
    "buffer_load_dwordx4 a[116:119], v1, s[28:31], s90 offen offset:1024 sc1\n"
    "v_mfma_f32_16x16x32_bf16 v[96:99], v[164:167], v[248:251], v[96:99]\n"
    "ds_read_b128 v[156:159], v1 offset:21504\n"
    "v_fma_f32 v56, v56, v178, v178\n"
    "v_mfma_f32_16x16x32_bf16 v[100:103], v[164:167], v[252:255], v[100:103]\n"
    "s_waitcnt lgkmcnt(4)\n"
    "v_exp_f32 v59, v59\n"
    "v_mfma_f32_16x16x32_bf16 v[104:107], v[136:139], a[160:163], v[104:107]\n"
    "v_fma_f32 v57, v57, v178, v178\n"
    "ds_read_b128 v[160:163], v1 offset:22528\n"
    "v_mfma_f32_16x16x32_bf16 v[108:111], v[136:139], a[164:167], v[108:111]\n"
    "v_rcp_f32 v56, v56\n"
    "v_exp_f32 v60, v60\n"
    "v_mul_f32 v62, v180, v62\n"
    "v_exp_f32 v61, v61\n"
    "v_mfma_f32_16x16x32_bf16 v[112:115], v[140:143], a[160:163], v[112:115]\n"
    "v_fma_f32 v58, v58, v178, v178\n"
    "v_rcp_f32 v57, v57\n"
    "v_fma_f32 v59, v59, v178, v178\n"
    "v_rcp_f32 v58, v58\n"
    "v_mul_f32 v64, v64, v56\n"
    "v_rcp_f32 v59, v59\n"
    "v_mul_f32 v63, v180, v63\n"
    "v_exp_f32 v62, v62\n"
    "v_fma_f32 v60, v60, v181, v181\n"
    "v_exp_f32 v63, v63\n"
    "v_fma_f32 v61, v61, v181, v181\n"
    "v_rcp_f32 v60, v60\n"
    "v_mfma_f32_16x16x32_bf16 v[116:119], v[140:143], a[164:167], v[116:119]\n"
    "s_waitcnt lgkmcnt(3)\n"
    "v_mul_f32 v65, v65, v57\n"
    "v_mul_f32 v66, v66, v58\n"
    "v_cvt_pk_bf16_f32 v184, v64, v65\n"
    "v_mul_f32 v67, v67, v59\n"
    "v_cvt_pk_bf16_f32 v185, v66, v67\n"
    "v_fma_f32 v62, v62, v181, v181\n"
    "v_rcp_f32 v61, v61\n"
    "v_fma_f32 v63, v63, v181, v181\n"
    "v_rcp_f32 v62, v62\n"
    "v_mul_f32 v68, v68, v60\n"
    "v_rcp_f32 v63, v63\n"
    "v_mfma_f32_16x16x32_bf16 v[104:107], v[144:147], a[168:171], v[104:107]\n"
    "buffer_store_dwordx4 v[182:185], v1, s[28:31], s79 offen sc1\n"
    "v_mul_f32 v69, v69, v61\n"
    "v_mul_f32 v70, v70, v62\n"
    "v_cvt_pk_bf16_f32 v188, v68, v69\n"
    "v_mul_f32 v71, v71, v63\n"
    "v_cvt_pk_bf16_f32 v189, v70, v71\n"
    "v_mfma_f32_16x16x32_bf16 v[108:111], v[144:147], a[172:175], v[108:111]\n"
    "ds_read_b128 v[164:167], v1 offset:23552\n"
    "buffer_load_dwordx4 a[120:123], v1, s[28:31], s90 offen offset:2048 sc1\n"
    "buffer_store_dwordx4 v[186:189], v1, s[28:31], s80 offen sc1\n"
    "v_mfma_f32_16x16x32_bf16 v[112:115], v[148:151], a[168:171], v[112:115]\n"
    "ds_read_b128 v[136:139], v1 offset:24576\n"
    "v_mul_f32 v72, v177, v72\n"
    "v_mfma_f32_16x16x32_bf16 v[116:119], v[148:151], a[172:175], v[116:119]\n"
    "s_waitcnt lgkmcnt(3)\n"
    "v_mul_f32 v76, v180, v76\n"
    "ds_read_b128 v[140:143], v1 offset:25600\n"
    "v_mfma_f32_16x16x32_bf16 v[104:107], v[152:155], a[176:179], v[104:107]\n"
    "buffer_load_dwordx4 a[124:127], v1, s[28:31], s90 offen offset:3072 sc1\n"
    "v_mfma_f32_16x16x32_bf16 v[108:111], v[152:155], a[180:183], v[108:111]\n"
    "v_mul_f32 v73, v177, v73\n"
    "v_mul_f32 v77, v180, v77\n"
    "v_mfma_f32_16x16x32_bf16 v[112:115], v[156:159], a[176:179], v[112:115]\n"
    "s_add_u32 s90, s82, 32768\n"
    "ds_read_b128 v[144:147], v1 offset:26624\n"
    "ds_read_b128 v[148:151], v1 offset:27648\n"
    "v_mfma_f32_16x16x32_bf16 v[116:119], v[156:159], a[180:183], v[116:119]\n"
    "s_waitcnt lgkmcnt(4)\n"
    "v_exp_f32 v72, v72\n"
    "v_mfma_f32_16x16x32_bf16 v[104:107], v[160:163], a[184:187], v[104:107]\n"
    "v_exp_f32 v76, v76\n"
    "v_mfma_f32_16x16x32_bf16 v[108:111], v[160:163], a[188:191], v[108:111]\n"
    "v_mul_f32 v74, v177, v74\n"
    "v_mul_f32 v78, v180, v78\n"
    "v_mfma_f32_16x16x32_bf16 v[112:115], v[164:167], a[184:187], v[112:115]\n"
    "buffer_load_dwordx4 a[128:131], v1, s[28:31], s90 offen sc1\n"
    "v_mfma_f32_16x16x32_bf16 v[116:119], v[164:167], a[188:191], v[116:119]\n"
    "s_waitcnt lgkmcnt(2)\n"
    "ds_read_b128 v[152:155], v1 offset:28672\n"
    "ds_read_b128 v[156:159], v1 offset:29696\n"
    "v_mfma_f32_16x16x32_bf16 v[104:107], v[136:139], a[192:195], v[104:107]\n"
    "v_exp_f32 v73, v73\n"
    "v_mfma_f32_16x16x32_bf16 v[108:111], v[136:139], a[196:199], v[108:111]\n"
    "v_exp_f32 v77, v77\n"
    "v_mfma_f32_16x16x32_bf16 v[112:115], v[140:143], a[192:195], v[112:115]\n"
    "buffer_load_dwordx4 a[132:135], v1, s[28:31], s90 offen offset:1024 sc1\n"
    "v_mul_f32 v75, v177, v75\n"
    "v_mfma_f32_16x16x32_bf16 v[116:119], v[140:143], a[196:199], v[116:119]\n"
    "s_waitcnt lgkmcnt(2)\n"
    "ds_read_b128 v[160:163], v1 offset:30720\n"
    "v_mul_f32 v79, v180, v79\n"
    "v_mfma_f32_16x16x32_bf16 v[104:107], v[144:147], a[200:203], v[104:107]\n"
    "v_exp_f32 v74, v74\n"
    "v_mfma_f32_16x16x32_bf16 v[108:111], v[144:147], a[204:207], v[108:111]\n"
    "ds_read_b128 v[164:167], v1 offset:31744\n"
    "s_waitcnt vmcnt(22)\n"
    "s_barrier\n"
    "v_exp_f32 v78, v78\n"
    "v_mfma_f32_16x16x32_bf16 v[112:115], v[148:151], a[200:203], v[112:115]\n"
    "v_fma_f32 v72, v72, v178, v178\n"
    "ds_read_b128 v[136:139], v1 offset:32768\n"
    "v_mfma_f32_16x16x32_bf16 v[116:119], v[148:151], a[204:207], v[116:119]\n"
    "s_waitcnt lgkmcnt(3)\n"
    "buffer_load_dwordx4 a[136:139], v1, s[28:31], s90 offen offset:2048 sc1\n"
    "v_fma_f32 v76, v76, v181, v181\n"
    "v_mfma_f32_16x16x32_bf16 v[104:107], v[152:155], a[208:211], v[104:107]\n"
    "ds_read_b128 v[140:143], v1 offset:33792\n"
    "ds_read_b128 v[144:147], v1 offset:34816\n"
    "v_mfma_f32_16x16x32_bf16 v[108:111], v[152:155], a[212:215], v[108:111]\n"
    "v_exp_f32 v75, v75\n"
    "v_mfma_f32_16x16x32_bf16 v[112:115], v[156:159], a[208:211], v[112:115]\n"
    "v_exp_f32 v79, v79\n"
    "v_mfma_f32_16x16x32_bf16 v[116:119], v[156:159], a[212:215], v[116:119]\n"
    "s_waitcnt lgkmcnt(3)\n"
    "buffer_load_dwordx4 a[140:143], v1, s[28:31], s90 offen offset:3072 sc1\n"
    "v_fma_f32 v73, v73, v178, v178\n"
    "v_mfma_f32_16x16x32_bf16 v[104:107], v[160:163], a[216:219], v[104:107]\n"
    "s_add_u32 s90, s82, 36864\n"
    "v_fma_f32 v77, v77, v181, v181\n"
    "ds_read_b128 v[148:151], v1 offset:35840\n"
    "v_mfma_f32_16x16x32_bf16 v[108:111], v[160:163], a[220:223], v[108:111]\n"
    "v_rcp_f32 v72, v72\n"
    "v_mfma_f32_16x16x32_bf16 v[112:115], v[164:167], a[216:219], v[112:115]\n"
    "v_rcp_f32 v76, v76\n"
    "v_mfma_f32_16x16x32_bf16 v[116:119], v[164:167], a[220:223], v[116:119]\n"
    "s_waitcnt lgkmcnt(2)\n"
    "ds_read_b128 v[152:155], v1 offset:36864\n"
    "v_fma_f32 v74, v74, v178, v178\n"
    "v_mfma_f32_16x16x32_bf16 v[104:107], v[136:139], a[224:227], v[104:107]\n"
    "buffer_load_dwordx4 a[144:147], v1, s[28:31], s90 offen sc1\n"
    "v_fma_f32 v78, v78, v181, v181\n"
    "v_mfma_f32_16x16x32_bf16 v[108:111], v[136:139], a[228:231], v[108:111]\n"
    "ds_read_b128 v[156:159], v1 offset:37888\n"
    "v_rcp_f32 v73, v73\n"
    "v_mfma_f32_16x16x32_bf16 v[112:115], v[140:143], a[224:227], v[112:115]\n"
    "v_rcp_f32 v77, v77\n"
    "v_mfma_f32_16x16x32_bf16 v[116:119], v[140:143], a[228:231], v[116:119]\n"
    "s_waitcnt lgkmcnt(2)\n"
    "ds_read_b128 v[160:163], v1 offset:38912\n"
    "v_fma_f32 v75, v75, v178, v178\n"
    "v_mfma_f32_16x16x32_bf16 v[104:107], v[144:147], a[232:235], v[104:107]\n"
    "buffer_load_dwordx4 a[148:151], v1, s[28:31], s90 offen offset:1024 sc1\n"
    "v_mfma_f32_16x16x32_bf16 v[108:111], v[144:147], a[236:239], v[108:111]\n"
    "ds_read_b128 v[164:167], v1 offset:39936\n"
    "v_fma_f32 v79, v79, v181, v181\n"
    "v_mfma_f32_16x16x32_bf16 v[112:115], v[148:151], a[232:235], v[112:115]\n"
    "v_rcp_f32 v74, v74\n"
    "v_mfma_f32_16x16x32_bf16 v[116:119], v[148:151], a[236:239], v[116:119]\n"
    "s_waitcnt lgkmcnt(2)\n"
    "ds_read_b128 v[136:139], v1 offset:40960\n"
    "v_rcp_f32 v78, v78\n"
    "v_mfma_f32_16x16x32_bf16 v[104:107], v[152:155], a[240:243], v[104:107]\n"
    "v_mul_f32 v80, v80, v72\n"
    "ds_read_b128 v[140:143], v1 offset:41984\n"
    "v_mfma_f32_16x16x32_bf16 v[108:111], v[152:155], a[244:247], v[108:111]\n"
    "buffer_load_dwordx4 a[152:155], v1, s[28:31], s90 offen offset:2048 sc1\n"
    "v_mul_f32 v84, v84, v76\n"
    "v_mfma_f32_16x16x32_bf16 v[112:115], v[156:159], a[240:243], v[112:115]\n"
    "v_rcp_f32 v75, v75\n"
    "v_mfma_f32_16x16x32_bf16 v[116:119], v[156:159], a[244:247], v[116:119]\n"
    "s_waitcnt lgkmcnt(2)\n"
    "ds_read_b128 v[144:147], v1 offset:43008\n"
    "v_rcp_f32 v79, v79\n"
    "v_mfma_f32_16x16x32_bf16 v[104:107], v[160:163], a[248:251], v[104:107]\n"
    "ds_read_b128 v[148:151], v1 offset:44032\n"
    "v_mul_f32 v81, v81, v73\n"
    "v_mfma_f32_16x16x32_bf16 v[108:111], v[160:163], a[252:255], v[108:111]\n"
    "s_add_u32 s91, s11, 0\n"
    "buffer_load_dwordx4 a[156:159], v1, s[28:31], s90 offen offset:3072 sc1\n"
    "v_mfma_f32_16x16x32_bf16 v[112:115], v[164:167], a[248:251], v[112:115]\n"
    "s_waitcnt vmcnt(9)\n"
    "v_mul_f32 v85, v85, v77\n"
    "ds_read_b128 v[152:155], v1 offset:45056\n"
    "v_mfma_f32_16x16x32_bf16 v[116:119], v[164:167], a[252:255], v[116:119]\n"
    "s_waitcnt lgkmcnt(3)\n"
    "s_barrier\n"
    "v_mul_f32 v82, v82, v74\n"
    "v_mfma_f32_16x16x32_bf16 v[104:107], v[136:139], v[192:195], v[104:107]\n"
    "s_mov_b64 exec, 0xf\n"
    "buffer_atomic_swap_x2 v[36:37], v29, s[52:55], s91 offen sc1\n"
    "s_mov_b64 exec, -1\n"
    "v_mfma_f32_16x16x32_bf16 v[108:111], v[136:139], v[196:199], v[108:111]\n"
    "s_add_u32 m0, s62, 0\n"
    "ds_read_b128 v[156:159], v1 offset:46080\n"
    "s_add_u32 s90, s82, 40960\n"
    "v_cvt_pk_bf16_f32 v182, v80, v81\n"
    "v_mul_f32 v86, v86, v78\n"
    "v_mfma_f32_16x16x32_bf16 v[112:115], v[140:143], v[192:195], v[112:115]\n"
    "buffer_load_dwordx4 v1, s[28:31], s90 offen sc1 lds\n"
    "v_cvt_pk_bf16_f32 v186, v84, v85\n"
    "v_mfma_f32_16x16x32_bf16 v[116:119], v[140:143], v[196:199], v[116:119]\n"
    "s_waitcnt lgkmcnt(2)\n"
    "ds_read_b128 v[160:163], v1 offset:47104\n"
    "v_mul_f32 v83, v83, v75\n"
    "v_mfma_f32_16x16x32_bf16 v[104:107], v[144:147], v[200:203], v[104:107]\n"
    "v_mul_f32 v87, v87, v79\n"
    "ds_read_b128 v[164:167], v1 offset:48128\n"
    "v_mfma_f32_16x16x32_bf16 v[108:111], v[144:147], v[204:207], v[108:111]\n"
    "s_barrier\n"
    "buffer_load_dwordx4 v1, s[28:31], s90 offen offset:1024 sc1 lds\n"
    "v_mfma_f32_16x16x32_bf16 v[112:115], v[148:151], v[200:203], v[112:115]\n"
    "v_cvt_pk_bf16_f32 v183, v82, v83\n"
    "ds_read_b128 v[136:139], v1 offset:49152\n"
    "v_mfma_f32_16x16x32_bf16 v[116:119], v[148:151], v[204:207], v[116:119]\n"
    "s_waitcnt lgkmcnt(3)\n"
    "v_cvt_pk_bf16_f32 v187, v86, v87\n"
    "ds_read_b128 v[140:143], v1 offset:50176\n"
    "v_mfma_f32_16x16x32_bf16 v[104:107], v[152:155], v[208:211], v[104:107]\n"
    "v_mul_f32 v88, v177, v88\n"
    "v_mul_f32 v92, v180, v92\n"
    "v_mfma_f32_16x16x32_bf16 v[108:111], v[152:155], v[212:215], v[108:111]\n"
    "ds_read_b128 v[144:147], v1 offset:51200\n"
    "v_mul_f32 v89, v177, v89\n"
    "v_mfma_f32_16x16x32_bf16 v[112:115], v[156:159], v[208:211], v[112:115]\n"
    "buffer_load_dwordx4 v1, s[28:31], s90 offen offset:2048 sc1 lds\n"
    "v_mfma_f32_16x16x32_bf16 v[116:119], v[156:159], v[212:215], v[116:119]\n"
    "s_waitcnt lgkmcnt(3)\n"
    "v_mul_f32 v93, v180, v93\n"
    "ds_read_b128 v[148:151], v1 offset:52224\n"
    "v_mfma_f32_16x16x32_bf16 v[104:107], v[160:163], v[216:219], v[104:107]\n"
    "v_exp_f32 v88, v88\n"
    "v_mfma_f32_16x16x32_bf16 v[108:111], v[160:163], v[220:223], v[108:111]\n"
    "ds_read_b128 v[152:155], v1 offset:53248\n"
    "v_mul_f32 v90, v177, v90\n"
    "v_mfma_f32_16x16x32_bf16 v[112:115], v[164:167], v[216:219], v[112:115]\n"
    "buffer_load_dwordx4 v1, s[28:31], s90 offen offset:3072 sc1 lds\n"
    "v_mfma_f32_16x16x32_bf16 v[116:119], v[164:167], v[220:223], v[116:119]\n"
    "s_waitcnt lgkmcnt(3)\n"
    "v_exp_f32 v92, v92\n"
    "v_mfma_f32_16x16x32_bf16 v[104:107], v[136:139], v[224:227], v[104:107]\n"
    "s_add_u32 m0, s62, 4096\n"
    "ds_read_b128 v[156:159], v1 offset:54272\n"
    "v_mul_f32 v94, v180, v94\n"
    "v_mfma_f32_16x16x32_bf16 v[108:111], v[136:139], v[228:231], v[108:111]\n"
    "s_add_u32 s90, s82, 45056\n"
    "v_exp_f32 v89, v89\n"
    "v_mfma_f32_16x16x32_bf16 v[112:115], v[140:143], v[224:227], v[112:115]\n"
    "ds_read_b128 v[160:163], v1 offset:55296\n"
    "ds_read_b128 v[164:167], v1 offset:56320\n"
    "v_mfma_f32_16x16x32_bf16 v[116:119], v[140:143], v[228:231], v[116:119]\n"
    "s_waitcnt lgkmcnt(4)\n"
    "v_exp_f32 v93, v93\n"
    "v_mfma_f32_16x16x32_bf16 v[104:107], v[144:147], v[232:235], v[104:107]\n"
    "buffer_load_dwordx4 v1, s[28:31], s90 offen sc1 lds\n"
    "v_mfma_f32_16x16x32_bf16 v[108:111], v[144:147], v[236:239], v[108:111]\n"
    "v_mul_f32 v91, v177, v91\n"
    "v_mul_f32 v95, v180, v95\n"
    "v_mfma_f32_16x16x32_bf16 v[112:115], v[148:151], v[232:235], v[112:115]\n"
    "ds_read_b128 v[136:139], v1 offset:57344\n"
    "ds_read_b128 v[140:143], v1 offset:58368\n"
    "v_mfma_f32_16x16x32_bf16 v[116:119], v[148:151], v[236:239], v[116:119]\n"
    "s_waitcnt lgkmcnt(4)\n"
    "v_exp_f32 v90, v90\n"
    "v_mfma_f32_16x16x32_bf16 v[104:107], v[152:155], v[240:243], v[104:107]\n"
    "buffer_load_dwordx4 v1, s[28:31], s90 offen offset:1024 sc1 lds\n"
    "v_exp_f32 v94, v94\n"
    "v_mfma_f32_16x16x32_bf16 v[108:111], v[152:155], v[244:247], v[108:111]\n"
    "v_fma_f32 v88, v88, v178, v178\n"
    "v_fma_f32 v92, v92, v181, v181\n"
    "v_mfma_f32_16x16x32_bf16 v[112:115], v[156:159], v[240:243], v[112:115]\n"
    "ds_read_b128 v[144:147], v1 offset:59392\n"
    "ds_read_b128 v[148:151], v1 offset:60416\n"
    "v_mfma_f32_16x16x32_bf16 v[116:119], v[156:159], v[244:247], v[116:119]\n"
    "s_waitcnt lgkmcnt(4)\n"
    "v_exp_f32 v91, v91\n"
    "v_mfma_f32_16x16x32_bf16 v[104:107], v[160:163], v[248:251], v[104:107]\n"
    "buffer_load_dwordx4 v1, s[28:31], s90 offen offset:2048 sc1 lds\n"
    "v_mfma_f32_16x16x32_bf16 v[108:111], v[160:163], v[252:255], v[108:111]\n"
    "v_exp_f32 v95, v95\n"
    "v_mfma_f32_16x16x32_bf16 v[112:115], v[164:167], v[248:251], v[112:115]\n"
    "v_fma_f32 v89, v89, v178, v178\n"
    "ds_read_b128 v[152:155], v1 offset:61440\n"
    "v_mfma_f32_16x16x32_bf16 v[116:119], v[164:167], v[252:255], v[116:119]\n"
    "s_waitcnt lgkmcnt(3)\n"
    "v_fma_f32 v93, v93, v181, v181\n"
    "ds_read_b128 v[156:159], v1 offset:62464\n"
    "v_mfma_f32_16x16x32_bf16 v[120:123], v[136:139], a[160:163], v[120:123]\n"
    "v_rcp_f32 v88, v88\n"
    "v_mfma_f32_16x16x32_bf16 v[124:127], v[136:139], a[164:167], v[124:127]\n"
    "buffer_load_dwordx4 v1, s[28:31], s90 offen offset:3072 sc1 lds\n"
    "v_rcp_f32 v92, v92\n"
    "v_mfma_f32_16x16x32_bf16 v[128:131], v[140:143], a[160:163], v[128:131]\n"
    "s_add_u32 m0, s62, 8192\n"
    "ds_read_b128 v[160:163], v1 offset:63488\n"
    "v_fma_f32 v90, v90, v178, v178\n"
    "v_mfma_f32_16x16x32_bf16 v[132:135], v[140:143], a[164:167], v[132:135]\n"
    "s_waitcnt lgkmcnt(3)\n"
    "v_fma_f32 v94, v94, v181, v181\n"
    "ds_read_b128 v[164:167], v1 offset:64512\n"
    "v_mfma_f32_16x16x32_bf16 v[120:123], v[144:147], a[168:171], v[120:123]\n"
    "s_barrier\n"
    "s_add_u32 s90, s82, 49152\n"
    "ds_read_b128 v[136:139], v27 offset:0\n"
    "v_mfma_f32_16x16x32_bf16 v[124:127], v[144:147], a[172:175], v[124:127]\n"
    "v_rcp_f32 v89, v89\n"
    "v_mfma_f32_16x16x32_bf16 v[128:131], v[148:151], a[168:171], v[128:131]\n"
    "buffer_load_dwordx4 v1, s[28:31], s90 offen sc1 lds\n"
    "v_rcp_f32 v93, v93\n"
    "v_mfma_f32_16x16x32_bf16 v[132:135], v[148:151], a[172:175], v[132:135]\n"
    "s_waitcnt lgkmcnt(3)\n"
    "v_fma_f32 v91, v91, v178, v178\n"
    "v_fma_f32 v95, v95, v181, v181\n"
    "v_mfma_f32_16x16x32_bf16 v[120:123], v[152:155], a[176:179], v[120:123]\n"
    "ds_read_b128 v[140:143], v27 offset:1024\n"
    "ds_read_b128 v[144:147], v27 offset:2048\n"
    "v_mfma_f32_16x16x32_bf16 v[124:127], v[152:155], a[180:183], v[124:127]\n"
    "buffer_load_dwordx4 v1, s[28:31], s90 offen offset:1024 sc1 lds\n"
    "v_mfma_f32_16x16x32_bf16 v[128:131], v[156:159], a[176:179], v[128:131]\n"
    "v_rcp_f32 v90, v90\n"
    "v_mfma_f32_16x16x32_bf16 v[132:135], v[156:159], a[180:183], v[132:135]\n"
    "s_waitcnt lgkmcnt(3)\n"
    "v_rcp_f32 v94, v94\n"
    "v_mfma_f32_16x16x32_bf16 v[120:123], v[160:163], a[184:187], v[120:123]\n"
    "v_mul_f32 v96, v96, v88\n"
    "ds_read_b128 v[148:151], v27 offset:3072\n"
    "v_mfma_f32_16x16x32_bf16 v[124:127], v[160:163], a[188:191], v[124:127]\n"
    "v_mul_f32 v100, v100, v92\n"
    "ds_read_b128 v[152:155], v27 offset:4096\n"
    "v_mfma_f32_16x16x32_bf16 v[128:131], v[164:167], a[184:187], v[128:131]\n"
    "v_rcp_f32 v91, v91\n"
    "v_mfma_f32_16x16x32_bf16 v[132:135], v[164:167], a[188:191], v[132:135]\n"
    "s_waitcnt lgkmcnt(3)\n"
    "v_mov_b64 v[34:35], 0\n"
    "buffer_load_dwordx4 v1, s[28:31], s90 offen offset:2048 sc1 lds\n"
    "v_rcp_f32 v95, v95\n"
    "v_mfma_f32_16x16x32_bf16 v[120:123], v[136:139], a[192:195], v[120:123]\n"
    "s_mov_b64 exec, 0xf\n"
    "buffer_atomic_or_x2 v[34:35], v28, s[52:55], s15 offen sc0 sc1\n"
    "s_mov_b64 exec, -1\n"
    "v_mfma_f32_16x16x32_bf16 v[124:127], v[136:139], a[196:199], v[124:127]\n"
    "ds_read_b128 v[156:159], v27 offset:5120\n"
    "v_mul_f32 v97, v97, v89\n"
    "v_mfma_f32_16x16x32_bf16 v[128:131], v[140:143], a[192:195], v[128:131]\n"
    "v_mul_f32 v101, v101, v93\n"
    "ds_read_b128 v[160:163], v27 offset:6144\n"
    "v_mfma_f32_16x16x32_bf16 v[132:135], v[140:143], a[196:199], v[132:135]\n"
    "s_waitcnt lgkmcnt(3)\n"
    "buffer_load_dwordx4 v1, s[28:31], s90 offen offset:3072 sc1 lds\n"
    "v_mul_f32 v98, v98, v90\n"
    "v_mfma_f32_16x16x32_bf16 v[120:123], v[144:147], a[200:203], v[120:123]\n"
    "s_add_u32 m0, s62, 12288\n"
    "v_mul_f32 v102, v102, v94\n"
    "ds_read_b128 v[164:167], v27 offset:7168\n"
    "v_mfma_f32_16x16x32_bf16 v[124:127], v[144:147], a[204:207], v[124:127]\n"
    "s_add_u32 s90, s82, 53248\n"
    "v_cvt_pk_bf16_f32 v184, v96, v97\n"
    "v_cvt_pk_bf16_f32 v188, v100, v101\n"
    "v_mfma_f32_16x16x32_bf16 v[128:131], v[148:151], a[200:203], v[128:131]\n"
    "ds_read_b128 v[136:139], v27 offset:8192\n"
    "v_mul_f32 v99, v99, v91\n"
    "v_mfma_f32_16x16x32_bf16 v[132:135], v[148:151], a[204:207], v[132:135]\n"
    "s_waitcnt lgkmcnt(3)\n"
    "buffer_load_dwordx4 v1, s[28:31], s90 offen sc1 lds\n"
    "v_mfma_f32_16x16x32_bf16 v[120:123], v[152:155], a[208:211], v[120:123]\n"
    "ds_read_b128 v[140:143], v27 offset:9216\n"
    "v_mul_f32 v103, v103, v95\n"
    "v_mfma_f32_16x16x32_bf16 v[124:127], v[152:155], a[212:215], v[124:127]\n"
    "v_cvt_pk_bf16_f32 v185, v98, v99\n"
    "ds_read_b128 v[144:147], v27 offset:10240\n"
    "v_mfma_f32_16x16x32_bf16 v[128:131], v[156:159], a[208:211], v[128:131]\n"
    "v_cvt_pk_bf16_f32 v189, v102, v103\n"
    "ds_read_b128 v[148:151], v27 offset:11264\n"
    "v_mfma_f32_16x16x32_bf16 v[132:135], v[156:159], a[212:215], v[132:135]\n"
    "s_waitcnt lgkmcnt(4)\n"
    "buffer_load_dwordx4 v1, s[28:31], s90 offen offset:1024 sc1 lds\n"
    "v_mfma_f32_16x16x32_bf16 v[120:123], v[160:163], a[216:219], v[120:123]\n"
    "buffer_store_dwordx4 v[182:185], v31, s[28:31], s79 offen sc1\n"
    "v_mfma_f32_16x16x32_bf16 v[124:127], v[160:163], a[220:223], v[124:127]\n"
    "buffer_store_dwordx4 v[186:189], v31, s[28:31], s80 offen sc1\n"
    "v_mfma_f32_16x16x32_bf16 v[128:131], v[164:167], a[216:219], v[128:131]\n"
    "ds_read_b128 v[152:155], v27 offset:12288\n"
    "v_mul_f32 v104, v177, v104\n"
    "v_mfma_f32_16x16x32_bf16 v[132:135], v[164:167], a[220:223], v[132:135]\n"
    "s_waitcnt lgkmcnt(3)\n"
    "ds_read_b128 v[156:159], v27 offset:13312\n"
    "v_mul_f32 v108, v180, v108\n"
    "v_mfma_f32_16x16x32_bf16 v[120:123], v[136:139], a[224:227], v[120:123]\n"
    "buffer_load_dwordx4 v1, s[28:31], s90 offen offset:2048 sc1 lds\n"
    "v_mfma_f32_16x16x32_bf16 v[124:127], v[136:139], a[228:231], v[124:127]\n"
    "s_add_u32 s90, s82, 53248\n"
    "v_mul_f32 v105, v177, v105\n"
    "ds_read_b128 v[160:163], v27 offset:14336\n"
    "v_mfma_f32_16x16x32_bf16 v[128:131], v[140:143], a[224:227], v[128:131]\n"
    "v_mul_f32 v109, v180, v109\n"
    "ds_read_b128 v[164:167], v27 offset:15360\n"
    "v_mfma_f32_16x16x32_bf16 v[132:135], v[140:143], a[228:231], v[132:135]\n"
    "s_waitcnt lgkmcnt(4)\n"
    "s_barrier\n"
    "v_mfma_f32_16x16x32_bf16 v[120:123], v[144:147], a[232:235], v[120:123]\n"
    "v_exp_f32 v104, v104\n"
    "v_mfma_f32_16x16x32_bf16 v[124:127], v[144:147], a[236:239], v[124:127]\n"
    "buffer_load_dwordx4 a[220:223], v1, s[28:31], s90 offen offset:3072 sc1\n"
    "v_exp_f32 v108, v108\n"
    "v_mfma_f32_16x16x32_bf16 v[128:131], v[148:151], a[232:235], v[128:131]\n"
    "s_add_u32 s90, s82, 57344\n"
    "v_mul_f32 v106, v177, v106\n"
    "ds_read_b128 v[136:139], v27 offset:16384\n"
    "v_mfma_f32_16x16x32_bf16 v[132:135], v[148:151], a[236:239], v[132:135]\n"
    "s_waitcnt lgkmcnt(3)\n"
    "v_mul_f32 v110, v180, v110\n"
    "ds_read_b128 v[140:143], v27 offset:17408\n"
    "v_mfma_f32_16x16x32_bf16 v[120:123], v[152:155], a[240:243], v[120:123]\n"
    "v_exp_f32 v105, v105\n"
    "v_mfma_f32_16x16x32_bf16 v[124:127], v[152:155], a[244:247], v[124:127]\n"
    "v_exp_f32 v109, v109\n"
    "v_mfma_f32_16x16x32_bf16 v[128:131], v[156:159], a[240:243], v[128:131]\n"
    "buffer_load_dwordx4 a[224:227], v1, s[28:31], s90 offen sc1\n"
    "v_mul_f32 v107, v177, v107\n"
    "v_mfma_f32_16x16x32_bf16 v[132:135], v[156:159], a[244:247], v[132:135]\n"
    "s_waitcnt lgkmcnt(2)\n"
    "ds_read_b128 v[144:147], v27 offset:18432\n"
    "v_mul_f32 v111, v180, v111\n"
    "v_mfma_f32_16x16x32_bf16 v[120:123], v[160:163], a[248:251], v[120:123]\n"
    "v_exp_f32 v106, v106\n"
    "v_mfma_f32_16x16x32_bf16 v[124:127], v[160:163], a[252:255], v[124:127]\n"
    "ds_read_b128 v[148:151], v27 offset:19456\n"
    "v_exp_f32 v110, v110\n"
    "v_mfma_f32_16x16x32_bf16 v[128:131], v[164:167], a[248:251], v[128:131]\n"
    "buffer_load_dwordx4 a[228:231], v1, s[28:31], s90 offen offset:1024 sc1\n"
    "v_mfma_f32_16x16x32_bf16 v[132:135], v[164:167], a[252:255], v[132:135]\n"
    "s_waitcnt lgkmcnt(2)\n"
    "ds_read_b128 v[152:155], v27 offset:20480\n"
    "v_fma_f32 v104, v104, v178, v178\n"
    "v_mfma_f32_16x16x32_bf16 v[120:123], v[136:139], v[192:195], v[120:123]\n"
    "v_fma_f32 v108, v108, v181, v181\n"
    "ds_read_b128 v[156:159], v27 offset:21504\n"
    "v_mfma_f32_16x16x32_bf16 v[124:127], v[136:139], v[196:199], v[124:127]\n"
    "v_exp_f32 v107, v107\n"
    "v_mfma_f32_16x16x32_bf16 v[128:131], v[140:143], v[192:195], v[128:131]\n"
    "s_waitcnt vmcnt(9)\n"
    "v_exp_f32 v111, v111\n"
    "v_mfma_f32_16x16x32_bf16 v[132:135], v[140:143], v[196:199], v[132:135]\n"
    "s_waitcnt lgkmcnt(2)\n"
    "v_cmp_ne_u64 vcc, s[66:67], v[34:35]\n"
    "ds_read_b128 v[160:163], v27 offset:22528\n"
    "v_fma_f32 v105, v105, v178, v178\n"
    "v_mfma_f32_16x16x32_bf16 v[120:123], v[144:147], v[200:203], v[120:123]\n"
    "s_and_b64 vcc, vcc, 15\n"
    "s_cbranch_scc1 L_miss_40\n"
    "L_ok_39:\n"
    "v_fma_f32 v109, v109, v181, v181\n"
    "v_mfma_f32_16x16x32_bf16 v[124:127], v[144:147], v[204:207], v[124:127]\n"
    "ds_read_b128 v[164:167], v27 offset:23552\n"
    "v_rcp_f32 v104, v104\n"
    "v_mfma_f32_16x16x32_bf16 v[128:131], v[148:151], v[200:203], v[128:131]\n"
    "v_rcp_f32 v108, v108\n"
    "v_mfma_f32_16x16x32_bf16 v[132:135], v[148:151], v[204:207], v[132:135]\n"
    "s_waitcnt lgkmcnt(2)\n"
    "ds_read_b128 v[136:139], v27 offset:24576\n"
    "v_fma_f32 v106, v106, v178, v178\n"
    "buffer_load_dwordx4 v[192:195], v1, s[28:31], s20 offen sc1\n"
    "v_mfma_f32_16x16x32_bf16 v[120:123], v[152:155], v[208:211], v[120:123]\n"
    "v_fma_f32 v110, v110, v181, v181\n"
    "ds_read_b128 v[140:143], v27 offset:25600\n"
    "v_mfma_f32_16x16x32_bf16 v[124:127], v[152:155], v[212:215], v[124:127]\n"
    "v_rcp_f32 v105, v105\n"
    "v_mfma_f32_16x16x32_bf16 v[128:131], v[156:159], v[208:211], v[128:131]\n"
    "v_rcp_f32 v109, v109\n"
    "v_mfma_f32_16x16x32_bf16 v[132:135], v[156:159], v[212:215], v[132:135]\n"
    "s_waitcnt lgkmcnt(2)\n"
    "ds_read_b128 v[144:147], v27 offset:26624\n"
    "v_fma_f32 v107, v107, v178, v178\n"
    "v_mfma_f32_16x16x32_bf16 v[120:123], v[160:163], v[216:219], v[120:123]\n"
    "v_fma_f32 v111, v111, v181, v181\n"
    "buffer_load_dwordx4 v[196:199], v1, s[28:31], s20 offen offset:1024 sc1\n"
    "v_mfma_f32_16x16x32_bf16 v[124:127], v[160:163], v[220:223], v[124:127]\n"
    "ds_read_b128 v[148:151], v27 offset:27648\n"
    "ds_read_b128 v[152:155], v27 offset:28672\n"
    "v_mfma_f32_16x16x32_bf16 v[128:131], v[164:167], v[216:219], v[128:131]\n"
    "v_rcp_f32 v106, v106\n"
    "v_mfma_f32_16x16x32_bf16 v[132:135], v[164:167], v[220:223], v[132:135]\n"
    "s_waitcnt lgkmcnt(3)\n"
    "v_rcp_f32 v110, v110\n"
    "v_mfma_f32_16x16x32_bf16 v[120:123], v[136:139], v[224:227], v[120:123]\n"
    "v_mul_f32 v112, v112, v104\n"
    "ds_read_b128 v[156:159], v27 offset:29696\n"
    "v_mfma_f32_16x16x32_bf16 v[124:127], v[136:139], v[228:231], v[124:127]\n"
    "v_mul_f32 v116, v116, v108\n"
    "buffer_load_dwordx4 v[200:203], v1, s[28:31], s20 offen offset:2048 sc1\n"
    "v_mfma_f32_16x16x32_bf16 v[128:131], v[140:143], v[224:227], v[128:131]\n"
    "v_rcp_f32 v107, v107\n"
    "v_mfma_f32_16x16x32_bf16 v[132:135], v[140:143], v[228:231], v[132:135]\n"
    "s_waitcnt lgkmcnt(2)\n"
    "ds_read_b128 v[160:163], v27 offset:30720\n"
    "v_rcp_f32 v111, v111\n"
    "v_mfma_f32_16x16x32_bf16 v[120:123], v[144:147], v[232:235], v[120:123]\n"
    "v_mul_f32 v113, v113, v105\n"
    "ds_read_b128 v[164:167], v27 offset:31744\n"
    "v_mfma_f32_16x16x32_bf16 v[124:127], v[144:147], v[236:239], v[124:127]\n"
    "v_mul_f32 v117, v117, v109\n"
    "v_mul_f32 v114, v114, v106\n"
    "v_mfma_f32_16x16x32_bf16 v[128:131], v[148:151], v[232:235], v[128:131]\n"
    "s_add_u32 s91, s11, 131072\n"
    "buffer_load_dwordx4 v[204:207], v1, s[28:31], s20 offen offset:3072 sc1\n"
    "v_mfma_f32_16x16x32_bf16 v[132:135], v[148:151], v[236:239], v[132:135]\n"
    "s_waitcnt vmcnt(8) lgkmcnt(2)\n"
    "v_mul_f32 v118, v118, v110\n"
    "v_cvt_pk_bf16_f32 v182, v112, v113\n"
    "v_mfma_f32_16x16x32_bf16 v[120:123], v[152:155], v[240:243], v[120:123]\n"
    "s_barrier\n"
    "v_cvt_pk_bf16_f32 v186, v116, v117\n"
    "v_mul_f32 v115, v115, v107\n"
    "v_mfma_f32_16x16x32_bf16 v[124:127], v[152:155], v[244:247], v[124:127]\n"
    "s_mov_b64 exec, 0xf\n"
    "buffer_atomic_swap_x2 v[36:37], v29, s[52:55], s91 offen sc1\n"
    "s_mov_b64 exec, -1\n"
    "v_mfma_f32_16x16x32_bf16 v[128:131], v[156:159], v[240:243], v[128:131]\n"
    "v_mul_f32 v119, v119, v111\n"
    "v_cvt_pk_bf16_f32 v183, v114, v115\n"
    "v_mfma_f32_16x16x32_bf16 v[132:135], v[156:159], v[244:247], v[132:135]\n"
    "s_waitcnt lgkmcnt(0)\n"
    "buffer_load_dwordx4 v[208:211], v1, s[28:31], s21 offen sc1\n"
    "v_mfma_f32_16x16x32_bf16 v[120:123], v[160:163], v[248:251], v[120:123]\n"
    "v_cvt_pk_bf16_f32 v187, v118, v119\n"
    "v_mfma_f32_16x16x32_bf16 v[124:127], v[160:163], v[252:255], v[124:127]\n"
    "v_mfma_f32_16x16x32_bf16 v[128:131], v[164:167], v[248:251], v[128:131]\n"
    "buffer_load_dwordx4 v[212:215], v1, s[28:31], s21 offen offset:1024 sc1\n"
    "v_mfma_f32_16x16x32_bf16 v[132:135], v[164:167], v[252:255], v[132:135]\n"
    "buffer_load_dwordx4 v[216:219], v1, s[28:31], s21 offen offset:2048 sc1\n"
    "s_nop 1\n"
    "v_mul_f32 v120, v177, v120\n"
    "s_nop 0\n"
    "v_mul_f32 v124, v180, v124\n"
    "v_mul_f32 v121, v177, v121\n"
    "v_mul_f32 v125, v180, v125\n"
    "v_exp_f32 v120, v120\n"
    "v_exp_f32 v124, v124\n"
    "v_mul_f32 v122, v177, v122\n"
    "v_mul_f32 v126, v180, v126\n"
    "buffer_load_dwordx4 v[220:223], v1, s[28:31], s21 offen offset:3072 sc1\n"
    "v_exp_f32 v121, v121\n"
    "v_exp_f32 v125, v125\n"
    "v_mul_f32 v123, v177, v123\n"
    "v_mul_f32 v127, v180, v127\n"
    "v_exp_f32 v122, v122\n"
    "v_exp_f32 v126, v126\n"
    "v_fma_f32 v120, v120, v178, v178\n"
    "v_fma_f32 v124, v124, v181, v181\n"
    "v_exp_f32 v123, v123\n"
    "v_exp_f32 v127, v127\n"
    "v_fma_f32 v121, v121, v178, v178\n"
    "v_fma_f32 v125, v125, v181, v181\n"
    "v_rcp_f32 v120, v120\n"
    "v_rcp_f32 v124, v124\n"
    "v_fma_f32 v122, v122, v178, v178\n"
    "v_fma_f32 v126, v126, v181, v181\n"
    "v_rcp_f32 v121, v121\n"
    "v_rcp_f32 v125, v125\n"
    "v_fma_f32 v123, v123, v178, v178\n"
    "v_fma_f32 v127, v127, v181, v181\n"
    "v_rcp_f32 v122, v122\n"
    "v_rcp_f32 v126, v126\n"
    "v_mul_f32 v128, v128, v120\n"
    "v_mul_f32 v132, v132, v124\n"
    "v_rcp_f32 v123, v123\n"
    "v_rcp_f32 v127, v127\n"
    "v_mul_f32 v129, v129, v121\n"
    "v_mul_f32 v133, v133, v125\n"
    "v_mul_f32 v130, v130, v122\n"
    "v_mul_f32 v134, v134, v126\n"
    "v_cvt_pk_bf16_f32 v184, v128, v129\n"
    "v_cvt_pk_bf16_f32 v188, v132, v133\n"
    "v_mul_f32 v131, v131, v123\n"
    "v_mul_f32 v135, v135, v127\n"
    "v_cvt_pk_bf16_f32 v185, v130, v131\n"
    "v_cvt_pk_bf16_f32 v189, v134, v135\n"
    "s_add_u32 s93, s79, 8192\n"
    "s_add_u32 s94, s80, 8192\n"
    "buffer_store_dwordx4 v[182:185], v1, s[28:31], s93 offen sc1\n"
    "buffer_store_dwordx4 v[186:189], v1, s[28:31], s94 offen sc1\n"
    "v_mfma_f32_16x16x32_bf16 v[100:103], v[8:11], v[4:7], 0\n"
    "s_add_u32 s92, s15, 131072\n"
    "v_mfma_f32_16x16x32_bf16 v[104:107], v[8:11], v[2:5], 0\n"
    "s_mul_i32 s91, s56, 15360\n"
    "buffer_load_dwordx4 v[224:227], v1, s[28:31], s22 offen sc1\n"
    "v_mfma_f32_16x16x32_bf16 v[108:111], v[12:15], v[4:7], 0\n"
    "s_add_u32 s91, s91, 98304\n"
    "v_mov_b64 v[34:35], 0\n"
    "s_mov_b64 exec, 0xf\n"
    "buffer_atomic_or_x2 v[34:35], v28, s[52:55], s92 offen sc0 sc1\n"
    "s_mov_b64 exec, -1\n"
    "v_mfma_f32_16x16x32_bf16 v[112:115], v[12:15], v[2:5], 0\n"
    "v_add_u32 v183, s91, v1\n"
    "v_mfma_f32_16x16x32_bf16 v[116:119], v[16:19], v[4:7], 0\n"
    "s_waitcnt vmcnt(12)\n"
    "v_mfma_f32_16x16x32_bf16 v[40:43], v[192:195], a[0:3], 0\n"
    "s_waitcnt vmcnt(11)\n"
    "ds_read_b128 a[160:163], v183 offset:0\n"
    "v_mfma_f32_16x16x32_bf16 v[40:43], v[196:199], a[20:23], v[40:43]\n"
    "ds_read_b128 a[164:167], v183 offset:1024\n"
    "v_mfma_f32_16x16x32_bf16 v[44:47], v[192:195], a[4:7], 0\n"
    "ds_read_b128 a[168:171], v183 offset:2048\n"
    "v_mfma_f32_16x16x32_bf16 v[44:47], v[196:199], a[24:27], v[44:47]\n"
    "buffer_load_dwordx4 v[228:231], v1, s[28:31], s22 offen offset:1024 sc1\n"
    "v_mfma_f32_16x16x32_bf16 v[48:51], v[192:195], a[8:11], 0\n"
    "ds_read_b128 a[172:175], v183 offset:3072\n"
    "ds_read_b128 a[176:179], v183 offset:4096\n"
    "v_mfma_f32_16x16x32_bf16 v[48:51], v[196:199], a[28:31], v[48:51]\n"
    "ds_read_b128 a[180:183], v183 offset:5120\n"
    "v_mfma_f32_16x16x32_bf16 v[52:55], v[192:195], a[12:15], 0\n"
    "ds_read_b128 a[184:187], v183 offset:6144\n"
    "v_mfma_f32_16x16x32_bf16 v[52:55], v[196:199], a[32:35], v[52:55]\n"
    "ds_read_b128 a[188:191], v183 offset:7168\n"
    "v_mfma_f32_16x16x32_bf16 v[56:59], v[192:195], a[16:19], 0\n"
    "ds_read_b128 a[192:195], v183 offset:8192\n"
    "v_mfma_f32_16x16x32_bf16 v[56:59], v[196:199], a[36:39], v[56:59]\n"
    "s_waitcnt vmcnt(11)\n"
    "buffer_load_dwordx4 v[232:235], v1, s[28:31], s22 offen offset:2048 sc1\n"
    "v_mfma_f32_16x16x32_bf16 v[40:43], v[200:203], a[40:43], v[40:43]\n"
    "s_waitcnt vmcnt(11)\n"
    "ds_read_b128 a[196:199], v183 offset:9216\n"
    "ds_read_b128 a[200:203], v183 offset:10240\n"
    "v_mfma_f32_16x16x32_bf16 v[40:43], v[204:207], a[60:63], v[40:43]\n"
    "ds_read_b128 a[204:207], v183 offset:11264\n"
    "v_mfma_f32_16x16x32_bf16 v[44:47], v[204:207], a[64:67], v[44:47]\n"
    "ds_read_b128 a[208:211], v183 offset:12288\n"
    "v_mfma_f32_16x16x32_bf16 v[44:47], v[200:203], a[44:47], v[44:47]\n"
    "ds_read_b128 a[212:215], v183 offset:13312\n"
    "v_mfma_f32_16x16x32_bf16 v[48:51], v[200:203], a[48:51], v[48:51]\n"
    "buffer_load_dwordx4 v[236:239], v1, s[28:31], s22 offen offset:3072 sc1\n"
    "v_mfma_f32_16x16x32_bf16 v[48:51], v[204:207], a[68:71], v[48:51]\n"
    "ds_read_b128 a[216:219], v183 offset:14336\n"
    "v_mfma_f32_16x16x32_bf16 v[52:55], v[204:207], a[72:75], v[52:55]\n"
    "v_mfma_f32_16x16x32_bf16 v[52:55], v[200:203], a[52:55], v[52:55]\n"
    "v_mfma_f32_16x16x32_bf16 v[56:59], v[200:203], a[56:59], v[56:59]\n"
    "buffer_load_dwordx4 v[240:243], v1, s[28:31], s23 offen sc1\n"
    "v_mfma_f32_16x16x32_bf16 v[56:59], v[204:207], a[76:79], v[56:59]\n"
    "s_waitcnt vmcnt(11)\n"
    "v_mfma_f32_16x16x32_bf16 v[60:63], v[208:211], a[0:3], 0\n"
    "s_waitcnt vmcnt(10)\n"
    "v_mfma_f32_16x16x32_bf16 v[60:63], v[212:215], a[20:23], v[60:63]\n"
    "v_mfma_f32_16x16x32_bf16 v[64:67], v[208:211], a[4:7], 0\n"
    "v_mfma_f32_16x16x32_bf16 v[64:67], v[212:215], a[24:27], v[64:67]\n"
    "buffer_load_dwordx4 v[244:247], v1, s[28:31], s23 offen offset:1024 sc1\n"
    "v_mfma_f32_16x16x32_bf16 v[68:71], v[208:211], a[8:11], 0\n"
    "v_mfma_f32_16x16x32_bf16 v[68:71], v[212:215], a[28:31], v[68:71]\n"
    "s_waitcnt lgkmcnt(0)\n"
    "s_add_u32 s91, s11, 262144\n"
    "v_mfma_f32_16x16x32_bf16 v[72:75], v[208:211], a[12:15], 0\n"
    "s_waitcnt vmcnt(7)\n"
    "v_mfma_f32_16x16x32_bf16 v[72:75], v[212:215], a[32:35], v[72:75]\n"
    "s_barrier\n"
    "v_mfma_f32_16x16x32_bf16 v[76:79], v[208:211], a[16:19], 0\n"
    "s_mov_b64 exec, 0xf\n"
    "buffer_atomic_swap_x2 v[36:37], v29, s[52:55], s91 offen sc1\n"
    "s_mov_b64 exec, -1\n"
    "v_mfma_f32_16x16x32_bf16 v[76:79], v[212:215], a[36:39], v[76:79]\n"
    "buffer_load_dwordx4 v[248:251], v1, s[28:31], s23 offen offset:2048 sc1\n"
    "v_mfma_f32_16x16x32_bf16 v[60:63], v[216:219], a[40:43], v[60:63]\n"
    "s_waitcnt vmcnt(7)\n"
    "v_mfma_f32_16x16x32_bf16 v[60:63], v[220:223], a[60:63], v[60:63]\n"
    "v_cmp_ne_u64 vcc, s[66:67], v[34:35]\n"
    "v_mfma_f32_16x16x32_bf16 v[64:67], v[220:223], a[64:67], v[64:67]\n"
    "s_add_u32 s91, s56, 1\n"
    "s_and_b64 vcc, vcc, 15\n"
    "s_cbranch_scc1 L_miss_43\n"
    "L_ok_42:\n"
    "v_mfma_f32_16x16x32_bf16 v[64:67], v[216:219], a[44:47], v[64:67]\n"
    "s_and_b32 s91, s91, 3\n"
    "s_mul_i32 s91, s91, 15360\n"
    "s_add_u32 s91, s91, 98304\n"
    "v_mfma_f32_16x16x32_bf16 v[68:71], v[216:219], a[48:51], v[68:71]\n"
    "s_add_u32 s89, s20, 4096\n"
    "v_add_u32 v180, s91, v1\n"
    "v_mfma_f32_16x16x32_bf16 v[68:71], v[220:223], a[68:71], v[68:71]\n"
    "buffer_load_dwordx4 v[192:195], v1, s[28:31], s89 offen sc1\n"
    "v_mfma_f32_16x16x32_bf16 v[72:75], v[220:223], a[72:75], v[72:75]\n"
    "buffer_load_dwordx4 v[252:255], v1, s[28:31], s23 offen offset:3072 sc1\n"
    "v_mfma_f32_16x16x32_bf16 v[72:75], v[216:219], a[52:55], v[72:75]\n"
    "s_add_u32 s91, s56, 2\n"
    "s_and_b32 s91, s91, 3\n"
    "s_mul_i32 s91, s91, 15360\n"
    "v_mfma_f32_16x16x32_bf16 v[76:79], v[216:219], a[56:59], v[76:79]\n"
    "s_add_u32 s91, s91, 103424\n"
    "v_add_u32 v181, s91, v1\n"
    "v_mfma_f32_16x16x32_bf16 v[76:79], v[220:223], a[76:79], v[76:79]\n"
    "buffer_load_dwordx4 v[196:199], v1, s[28:31], s89 offen offset:1024 sc1\n"
    "v_mfma_f32_16x16x32_bf16 v[80:83], v[224:227], a[0:3], 0\n"
    "s_waitcnt vmcnt(9)\n"
    "v_mfma_f32_16x16x32_bf16 v[80:83], v[228:231], a[20:23], v[80:83]\n"
    "v_mfma_f32_16x16x32_bf16 v[84:87], v[224:227], a[4:7], 0\n"
    "s_add_u32 s91, s56, 3\n"
    "s_and_b32 s91, s91, 3\n"
    "s_mul_i32 s91, s91, 15360\n"
    "v_mfma_f32_16x16x32_bf16 v[84:87], v[228:231], a[24:27], v[84:87]\n"
    "s_add_u32 s91, s91, 108544\n"
    "v_add_u32 v182, s91, v1\n"
    "v_mfma_f32_16x16x32_bf16 v[88:91], v[224:227], a[8:11], 0\n"
    "buffer_load_dwordx4 v[200:203], v1, s[28:31], s89 offen offset:2048 sc1\n"
    "v_mfma_f32_16x16x32_bf16 v[88:91], v[228:231], a[28:31], v[88:91]\n"
    "buffer_load_dwordx4 a[232:235], v1, s[28:31], s90 offen offset:2048 sc1\n"
    "v_mfma_f32_16x16x32_bf16 v[92:95], v[224:227], a[12:15], 0\n"
    "v_mfma_f32_16x16x32_bf16 v[92:95], v[228:231], a[32:35], v[92:95]\n"
    "v_mfma_f32_16x16x32_bf16 v[96:99], v[224:227], a[16:19], 0\n"
    "v_and_b32 v190, 15, v0\n"
    "v_mfma_f32_16x16x32_bf16 v[96:99], v[228:231], a[36:39], v[96:99]\n"
    "s_waitcnt vmcnt(10)\n"
    "v_mul_u32_u24 v190, 160, v190\n"
    "v_mfma_f32_16x16x32_bf16 v[80:83], v[232:235], a[40:43], v[80:83]\n"
    "s_waitcnt vmcnt(9)\n"
    "buffer_load_dwordx4 v[204:207], v1, s[28:31], s89 offen offset:3072 sc1\n"
    "v_mfma_f32_16x16x32_bf16 v[80:83], v[236:239], a[60:63], v[80:83]\n"
    "s_add_u32 s92, s15, 262144\n"
    "v_bfe_u32 v191, v0, 4, 2\n"
    "v_mfma_f32_16x16x32_bf16 v[84:87], v[236:239], a[64:67], v[84:87]\n"
    "v_lshl_add_u32 v190, v191, 3, v190\n"
    "v_mfma_f32_16x16x32_bf16 v[84:87], v[232:235], a[44:47], v[84:87]\n"
    "buffer_load_dwordx4 a[236:239], v1, s[28:31], s90 offen offset:3072 sc1\n"
    "v_mov_b64 v[34:35], 0\n"
    "s_mov_b64 exec, 0xf\n"
    "buffer_atomic_or_x2 v[34:35], v28, s[52:55], s92 offen sc0 sc1\n"
    "s_mov_b64 exec, -1\n"
    "v_mfma_f32_16x16x32_bf16 v[88:91], v[232:235], a[48:51], v[88:91]\n"
    "s_add_u32 s89, s21, 4096\n"
    "v_add_u32 v190, s62, v190\n"
    "v_mfma_f32_16x16x32_bf16 v[88:91], v[236:239], a[68:71], v[88:91]\n"
    "v_and_b32 v187, 63, v0\n"
    "v_mfma_f32_16x16x32_bf16 v[92:95], v[236:239], a[72:75], v[92:95]\n"
    "buffer_load_dwordx4 v[208:211], v1, s[28:31], s89 offen sc1\n"
    "v_mfma_f32_16x16x32_bf16 v[92:95], v[232:235], a[52:55], v[92:95]\n"
    "v_add_u32 v188, 0, v187\n"
    "v_mfma_f32_16x16x32_bf16 v[96:99], v[232:235], a[56:59], v[96:99]\n"
    "v_mul_u32_u24 v189, 6554, v188\n"
    "v_mfma_f32_16x16x32_bf16 v[96:99], v[236:239], a[76:79], v[96:99]\n"
    "s_waitcnt vmcnt(12)\n"
    "v_mfma_f32_16x16x32_bf16 v[100:103], v[240:243], a[0:3], v[100:103]\n"
    "s_waitcnt vmcnt(11)\n"
    "v_lshrrev_b32 v189, 16, v189\n"
    "v_mfma_f32_16x16x32_bf16 v[100:103], v[244:247], a[20:23], v[100:103]\n"
    "v_mul_u32_u24 v189, 1120, v189\n"
    "v_mfma_f32_16x16x32_bf16 v[104:107], v[240:243], a[4:7], v[104:107]\n"
    "buffer_load_dwordx4 v[212:215], v1, s[28:31], s89 offen offset:1024 sc1\n"
    "v_mfma_f32_16x16x32_bf16 v[104:107], v[244:247], a[24:27], v[104:107]\n"
    "v_lshl_add_u32 v184, v188, 4, v189\n"
    "v_mfma_f32_16x16x32_bf16 v[108:111], v[240:243], a[8:11], v[108:111]\n"
    "v_add_u32 v188, 64, v187\n"
    "v_mfma_f32_16x16x32_bf16 v[108:111], v[244:247], a[28:31], v[108:111]\n"
    "v_mfma_f32_16x16x32_bf16 v[112:115], v[240:243], a[12:15], v[112:115]\n"
    "v_mul_u32_u24 v189, 6554, v188\n"
    "v_mfma_f32_16x16x32_bf16 v[112:115], v[244:247], a[32:35], v[112:115]\n"
    "v_lshrrev_b32 v189, 16, v189\n"
    "v_mfma_f32_16x16x32_bf16 v[116:119], v[240:243], a[16:19], v[116:119]\n"
    "buffer_load_dwordx4 v[216:219], v1, s[28:31], s89 offen offset:2048 sc1\n"
    "v_mfma_f32_16x16x32_bf16 v[116:119], v[244:247], a[36:39], v[116:119]\n"
    "s_waitcnt vmcnt(11)\n"
    "v_mul_u32_u24 v189, 1120, v189\n"
    "v_mfma_f32_16x16x32_bf16 v[100:103], v[248:251], a[40:43], v[100:103]\n"
    "s_waitcnt vmcnt(9)\n"
    "v_lshl_add_u32 v185, v188, 4, v189\n"
    "v_mfma_f32_16x16x32_bf16 v[100:103], v[252:255], a[60:63], v[100:103]\n"
    "buffer_load_dwordx4 v[220:223], v1, s[28:31], s89 offen offset:3072 sc1\n"
    "v_mfma_f32_16x16x32_bf16 v[104:107], v[252:255], a[64:67], v[104:107]\n"
    "v_add_u32 v188, 128, v187\n"
    "v_mfma_f32_16x16x32_bf16 v[104:107], v[248:251], a[44:47], v[104:107]\n"
    "v_mul_u32_u24 v189, 6554, v188\n"
    "v_mfma_f32_16x16x32_bf16 v[108:111], v[248:251], a[48:51], v[108:111]\n"
    "v_mfma_f32_16x16x32_bf16 v[108:111], v[252:255], a[68:71], v[108:111]\n"
    "v_lshrrev_b32 v189, 16, v189\n"
    "v_mfma_f32_16x16x32_bf16 v[112:115], v[252:255], a[72:75], v[112:115]\n"
    "s_add_u32 s89, s22, 4096\n"
    "v_mul_u32_u24 v189, 1120, v189\n"
    "v_mfma_f32_16x16x32_bf16 v[112:115], v[248:251], a[52:55], v[112:115]\n"
    "buffer_load_dwordx4 v[224:227], v1, s[28:31], s89 offen sc1\n"
    "v_mfma_f32_16x16x32_bf16 v[116:119], v[248:251], a[56:59], v[116:119]\n"
    "v_lshl_add_u32 v186, v188, 4, v189\n"
    "s_mov_b32 s48, s24\n"
    "v_mfma_f32_16x16x32_bf16 v[116:119], v[252:255], a[76:79], v[116:119]\n"
    "s_and_b32 s49, s25, 0xffff\n"
    "s_mov_b32 s50, 2621440\n"
    "s_mov_b32 s51, 0x20000\n"
    "v_mfma_f32_16x16x32_bf16 v[40:43], v[192:195], a[80:83], v[40:43]\n"
    "s_waitcnt vmcnt(10)\n"
    "s_mul_i32 s93, s59, 20480\n"
    "s_mul_i32 s94, s57, 160\n"
    "v_mfma_f32_16x16x32_bf16 v[40:43], v[196:199], a[100:103], v[40:43]\n"
    "s_add_u32 s93, s93, s94\n"
    "v_mfma_f32_16x16x32_bf16 v[44:47], v[196:199], a[104:107], v[44:47]\n"
    "buffer_load_dwordx4 v[228:231], v1, s[28:31], s89 offen offset:1024 sc1\n"
    "v_mfma_f32_16x16x32_bf16 v[44:47], v[192:195], a[84:87], v[44:47]\n"
    "v_mfma_f32_16x16x32_bf16 v[48:51], v[192:195], a[88:91], v[48:51]\n"
    "v_mfma_f32_16x16x32_bf16 v[48:51], v[196:199], a[108:111], v[48:51]\n"
    "v_mfma_f32_16x16x32_bf16 v[52:55], v[196:199], a[112:115], v[52:55]\n"
    "v_mfma_f32_16x16x32_bf16 v[52:55], v[192:195], a[92:95], v[52:55]\n"
    "v_mfma_f32_16x16x32_bf16 v[56:59], v[192:195], a[96:99], v[56:59]\n"
    "buffer_load_dwordx4 v[232:235], v1, s[28:31], s89 offen offset:2048 sc1\n"
    "v_mfma_f32_16x16x32_bf16 v[56:59], v[196:199], a[116:119], v[56:59]\n"
    "s_waitcnt vmcnt(11)\n"
    "v_mfma_f32_16x16x32_bf16 v[40:43], v[200:203], a[120:123], v[40:43]\n"
    "s_waitcnt vmcnt(9)\n"
    "v_mfma_f32_16x16x32_bf16 v[40:43], v[204:207], a[140:143], v[40:43]\n"
    "v_mfma_f32_16x16x32_bf16 v[44:47], v[204:207], a[144:147], v[44:47]\n"
    "v_mfma_f32_16x16x32_bf16 v[44:47], v[200:203], a[124:127], v[44:47]\n"
    "buffer_load_dwordx4 v[236:239], v1, s[28:31], s89 offen offset:3072 sc1\n"
    "v_mfma_f32_16x16x32_bf16 v[48:51], v[200:203], a[128:131], v[48:51]\n"
    "s_waitcnt vmcnt(8)\n"
    "v_mfma_f32_16x16x32_bf16 v[48:51], v[204:207], a[148:151], v[48:51]\n"
    "v_cmp_ne_u64 vcc, s[66:67], v[34:35]\n"
    "v_mfma_f32_16x16x32_bf16 v[52:55], v[204:207], a[152:155], v[52:55]\n"
    "s_and_b64 vcc, vcc, 15\n"
    "s_cbranch_scc1 L_miss_46\n"
    "L_ok_45:\n"
    "s_add_u32 s89, s20, 8192\n"
    "v_mfma_f32_16x16x32_bf16 v[52:55], v[200:203], a[132:135], v[52:55]\n"
    "buffer_load_dwordx4 v[240:243], v1, s[28:31], s89 offen sc1\n"
    "v_mfma_f32_16x16x32_bf16 v[56:59], v[200:203], a[136:139], v[56:59]\n"
    "v_mfma_f32_16x16x32_bf16 v[56:59], v[204:207], a[156:159], v[56:59]\n"
    "s_waitcnt vmcnt(8)\n"
    "v_mfma_f32_16x16x32_bf16 v[60:63], v[208:211], a[80:83], v[60:63]\n"
    "s_waitcnt vmcnt(7)\n"
    "v_mfma_f32_16x16x32_bf16 v[60:63], v[212:215], a[100:103], v[60:63]\n"
    "v_mfma_f32_16x16x32_bf16 v[64:67], v[212:215], a[104:107], v[64:67]\n"
    "buffer_load_dwordx4 v[244:247], v1, s[28:31], s89 offen offset:1024 sc1\n"
    "v_mfma_f32_16x16x32_bf16 v[64:67], v[208:211], a[84:87], v[64:67]\n"
    "v_mfma_f32_16x16x32_bf16 v[68:71], v[208:211], a[88:91], v[68:71]\n"
    "v_mfma_f32_16x16x32_bf16 v[68:71], v[212:215], a[108:111], v[68:71]\n"
    "v_mfma_f32_16x16x32_bf16 v[72:75], v[212:215], a[112:115], v[72:75]\n"
    "v_mfma_f32_16x16x32_bf16 v[72:75], v[208:211], a[92:95], v[72:75]\n"
    "v_mfma_f32_16x16x32_bf16 v[76:79], v[208:211], a[96:99], v[76:79]\n"
    "buffer_load_dwordx4 v[248:251], v1, s[28:31], s89 offen offset:2048 sc1\n"
    "v_mfma_f32_16x16x32_bf16 v[76:79], v[212:215], a[116:119], v[76:79]\n"
    "s_waitcnt vmcnt(8)\n"
    "v_mfma_f32_16x16x32_bf16 v[60:63], v[216:219], a[120:123], v[60:63]\n"
    "s_waitcnt vmcnt(7)\n"
    "v_mfma_f32_16x16x32_bf16 v[60:63], v[220:223], a[140:143], v[60:63]\n"
    "v_mfma_f32_16x16x32_bf16 v[64:67], v[220:223], a[144:147], v[64:67]\n"
    "v_mfma_f32_16x16x32_bf16 v[64:67], v[216:219], a[124:127], v[64:67]\n"
    "buffer_load_dwordx4 v[252:255], v1, s[28:31], s89 offen offset:3072 sc1\n"
    "v_mfma_f32_16x16x32_bf16 v[68:71], v[216:219], a[128:131], v[68:71]\n"
    "v_mfma_f32_16x16x32_bf16 v[68:71], v[220:223], a[148:151], v[68:71]\n"
    "v_mfma_f32_16x16x32_bf16 v[72:75], v[220:223], a[152:155], v[72:75]\n"
    "v_mfma_f32_16x16x32_bf16 v[72:75], v[216:219], a[132:135], v[72:75]\n"
    "v_mfma_f32_16x16x32_bf16 v[76:79], v[216:219], a[136:139], v[76:79]\n"
    "s_add_u32 s89, s21, 8192\n"
    "v_mfma_f32_16x16x32_bf16 v[76:79], v[220:223], a[156:159], v[76:79]\n"
    "s_waitcnt vmcnt(7)\n"
    "buffer_load_dwordx4 v[192:195], v1, s[28:31], s89 offen sc1\n"
    "v_mfma_f32_16x16x32_bf16 v[80:83], v[224:227], a[80:83], v[80:83]\n"
    "s_waitcnt vmcnt(7)\n"
    "v_mfma_f32_16x16x32_bf16 v[80:83], v[228:231], a[100:103], v[80:83]\n"
    "v_mfma_f32_16x16x32_bf16 v[84:87], v[228:231], a[104:107], v[84:87]\n"
    "v_mfma_f32_16x16x32_bf16 v[84:87], v[224:227], a[84:87], v[84:87]\n"
    "v_mfma_f32_16x16x32_bf16 v[88:91], v[224:227], a[88:91], v[88:91]\n"
    "buffer_load_dwordx4 v[196:199], v1, s[28:31], s89 offen offset:1024 sc1\n"
    "v_mfma_f32_16x16x32_bf16 v[88:91], v[228:231], a[108:111], v[88:91]\n"
    "v_mfma_f32_16x16x32_bf16 v[92:95], v[228:231], a[112:115], v[92:95]\n"
    "v_mfma_f32_16x16x32_bf16 v[92:95], v[224:227], a[92:95], v[92:95]\n"
    "v_mfma_f32_16x16x32_bf16 v[96:99], v[224:227], a[96:99], v[96:99]\n"
    "v_mfma_f32_16x16x32_bf16 v[96:99], v[228:231], a[116:119], v[96:99]\n"
    "s_waitcnt vmcnt(7)\n"
    "v_mfma_f32_16x16x32_bf16 v[80:83], v[232:235], a[120:123], v[80:83]\n"
    "s_waitcnt vmcnt(6)\n"
    "buffer_load_dwordx4 v[200:203], v1, s[28:31], s89 offen offset:2048 sc1\n"
    "v_mfma_f32_16x16x32_bf16 v[80:83], v[236:239], a[140:143], v[80:83]\n"
    "v_mfma_f32_16x16x32_bf16 v[84:87], v[236:239], a[144:147], v[84:87]\n"
    "v_mfma_f32_16x16x32_bf16 v[84:87], v[232:235], a[124:127], v[84:87]\n"
    "v_mfma_f32_16x16x32_bf16 v[88:91], v[232:235], a[128:131], v[88:91]\n"
    "v_mfma_f32_16x16x32_bf16 v[88:91], v[236:239], a[148:151], v[88:91]\n"
    "buffer_load_dwordx4 v[204:207], v1, s[28:31], s89 offen offset:3072 sc1\n"
    "v_mfma_f32_16x16x32_bf16 v[92:95], v[236:239], a[152:155], v[92:95]\n"
    "v_mfma_f32_16x16x32_bf16 v[92:95], v[232:235], a[132:135], v[92:95]\n"
    "v_mfma_f32_16x16x32_bf16 v[96:99], v[232:235], a[136:139], v[96:99]\n"
    "v_mfma_f32_16x16x32_bf16 v[96:99], v[236:239], a[156:159], v[96:99]\n"
    "s_waitcnt vmcnt(7)\n"
    "v_mfma_f32_16x16x32_bf16 v[40:43], v[240:243], a[160:163], v[40:43]\n"
    "s_waitcnt vmcnt(6)\n"
    "s_add_u32 s89, s22, 8192\n"
    "v_mfma_f32_16x16x32_bf16 v[40:43], v[244:247], a[180:183], v[40:43]\n"
    "buffer_load_dwordx4 v[208:211], v1, s[28:31], s89 offen sc1\n"
    "v_mfma_f32_16x16x32_bf16 v[44:47], v[244:247], a[184:187], v[44:47]\n"
    "v_mfma_f32_16x16x32_bf16 v[44:47], v[240:243], a[164:167], v[44:47]\n"
    "v_mfma_f32_16x16x32_bf16 v[48:51], v[240:243], a[168:171], v[48:51]\n"
    "v_mfma_f32_16x16x32_bf16 v[48:51], v[244:247], a[188:191], v[48:51]\n"
    "v_mfma_f32_16x16x32_bf16 v[52:55], v[244:247], a[192:195], v[52:55]\n"
    "buffer_load_dwordx4 v[212:215], v1, s[28:31], s89 offen offset:1024 sc1\n"
    "v_mfma_f32_16x16x32_bf16 v[52:55], v[240:243], a[172:175], v[52:55]\n"
    "v_mfma_f32_16x16x32_bf16 v[56:59], v[240:243], a[176:179], v[56:59]\n"
    "v_mfma_f32_16x16x32_bf16 v[56:59], v[244:247], a[196:199], v[56:59]\n"
    "s_waitcnt vmcnt(7)\n"
    "v_mfma_f32_16x16x32_bf16 v[40:43], v[248:251], a[200:203], v[40:43]\n"
    "s_waitcnt vmcnt(6)\n"
    "v_mfma_f32_16x16x32_bf16 v[40:43], v[252:255], a[220:223], v[40:43]\n"
    "v_mfma_f32_16x16x32_bf16 v[44:47], v[252:255], a[224:227], v[44:47]\n"
    "buffer_load_dwordx4 v[216:219], v1, s[28:31], s89 offen offset:2048 sc1\n"
    "v_mfma_f32_16x16x32_bf16 v[44:47], v[248:251], a[204:207], v[44:47]\n"
    "v_mfma_f32_16x16x32_bf16 v[48:51], v[248:251], a[208:211], v[48:51]\n"
    "v_mfma_f32_16x16x32_bf16 v[48:51], v[252:255], a[228:231], v[48:51]\n"
    "v_mfma_f32_16x16x32_bf16 v[52:55], v[252:255], a[232:235], v[52:55]\n"
    "v_mfma_f32_16x16x32_bf16 v[52:55], v[248:251], a[212:215], v[52:55]\n"
    "buffer_load_dwordx4 v[220:223], v1, s[28:31], s89 offen offset:3072 sc1\n"
    "v_mfma_f32_16x16x32_bf16 v[56:59], v[248:251], a[216:219], v[56:59]\n"
    "v_mfma_f32_16x16x32_bf16 v[56:59], v[252:255], a[236:239], v[56:59]\n"
    "s_waitcnt vmcnt(7)\n"
    "v_mfma_f32_16x16x32_bf16 v[60:63], v[192:195], a[160:163], v[60:63]\n"
    "s_waitcnt vmcnt(6)\n"
    "v_mfma_f32_16x16x32_bf16 v[60:63], v[196:199], a[180:183], v[60:63]\n"
    "v_mfma_f32_16x16x32_bf16 v[64:67], v[196:199], a[184:187], v[64:67]\n"
    "s_add_u32 s89, s23, 4096\n"
    "v_mfma_f32_16x16x32_bf16 v[64:67], v[192:195], a[164:167], v[64:67]\n"
    "buffer_load_dwordx4 v[224:227], v1, s[28:31], s89 offen sc1\n"
    "v_mfma_f32_16x16x32_bf16 v[68:71], v[192:195], a[168:171], v[68:71]\n"
    "ds_write_b128 v180, v[40:43] offset:0\n"
    "v_mfma_f32_16x16x32_bf16 v[68:71], v[196:199], a[188:191], v[68:71]\n"
    "v_mfma_f32_16x16x32_bf16 v[72:75], v[196:199], a[192:195], v[72:75]\n"
    "v_mfma_f32_16x16x32_bf16 v[72:75], v[192:195], a[172:175], v[72:75]\n"
    "ds_write_b128 v180, v[44:47] offset:1024\n"
    "v_mfma_f32_16x16x32_bf16 v[76:79], v[192:195], a[176:179], v[76:79]\n"
    "buffer_load_dwordx4 v[228:231], v1, s[28:31], s89 offen offset:1024 sc1\n"
    "v_mfma_f32_16x16x32_bf16 v[76:79], v[196:199], a[196:199], v[76:79]\n"
    "s_waitcnt vmcnt(7)\n"
    "v_mfma_f32_16x16x32_bf16 v[60:63], v[200:203], a[200:203], v[60:63]\n"
    "s_waitcnt vmcnt(6)\n"
    "ds_write_b128 v180, v[48:51] offset:2048\n"
    "v_mfma_f32_16x16x32_bf16 v[60:63], v[204:207], a[220:223], v[60:63]\n"
    "v_mfma_f32_16x16x32_bf16 v[64:67], v[204:207], a[224:227], v[64:67]\n"
    "v_mfma_f32_16x16x32_bf16 v[64:67], v[200:203], a[204:207], v[64:67]\n"
    "ds_write_b128 v180, v[52:55] offset:3072\n"
    "v_mfma_f32_16x16x32_bf16 v[68:71], v[200:203], a[208:211], v[68:71]\n"
    "buffer_load_dwordx4 v[232:235], v1, s[28:31], s89 offen offset:2048 sc1\n"
    "v_mfma_f32_16x16x32_bf16 v[68:71], v[204:207], a[228:231], v[68:71]\n"
    "v_mfma_f32_16x16x32_bf16 v[72:75], v[204:207], a[232:235], v[72:75]\n"
    "ds_write_b128 v180, v[56:59] offset:4096\n"
    "v_mfma_f32_16x16x32_bf16 v[72:75], v[200:203], a[212:215], v[72:75]\n"
    "v_mfma_f32_16x16x32_bf16 v[76:79], v[200:203], a[216:219], v[76:79]\n"
    "v_mfma_f32_16x16x32_bf16 v[76:79], v[204:207], a[236:239], v[76:79]\n"
    "s_waitcnt vmcnt(6)\n"
    "buffer_load_dwordx4 v[236:239], v1, s[28:31], s89 offen offset:3072 sc1\n"
    "v_mfma_f32_16x16x32_bf16 v[80:83], v[208:211], a[160:163], v[80:83]\n"
    "s_waitcnt vmcnt(6)\n"
    "v_mfma_f32_16x16x32_bf16 v[80:83], v[212:215], a[180:183], v[80:83]\n"
    "v_mfma_f32_16x16x32_bf16 v[84:87], v[212:215], a[184:187], v[84:87]\n"
    "v_mfma_f32_16x16x32_bf16 v[84:87], v[208:211], a[164:167], v[84:87]\n"
    "v_mfma_f32_16x16x32_bf16 v[88:91], v[208:211], a[168:171], v[88:91]\n"
    "s_add_u32 s89, s23, 8192\n"
    "ds_write_b128 v181, v[60:63] offset:0\n"
    "v_mfma_f32_16x16x32_bf16 v[88:91], v[212:215], a[188:191], v[88:91]\n"
    "buffer_load_dwordx4 v[240:243], v1, s[28:31], s89 offen sc1\n"
    "v_mfma_f32_16x16x32_bf16 v[92:95], v[212:215], a[192:195], v[92:95]\n"
    "v_mfma_f32_16x16x32_bf16 v[92:95], v[208:211], a[172:175], v[92:95]\n"
    "ds_write_b128 v181, v[64:67] offset:1024\n"
    "v_mfma_f32_16x16x32_bf16 v[96:99], v[208:211], a[176:179], v[96:99]\n"
    "v_mfma_f32_16x16x32_bf16 v[96:99], v[212:215], a[196:199], v[96:99]\n"
    "s_waitcnt vmcnt(6)\n"
    "v_mfma_f32_16x16x32_bf16 v[80:83], v[216:219], a[200:203], v[80:83]\n"
    "s_waitcnt vmcnt(5)\n"
    "buffer_load_dwordx4 v[244:247], v1, s[28:31], s89 offen offset:1024 sc1\n"
    "v_mfma_f32_16x16x32_bf16 v[80:83], v[220:223], a[220:223], v[80:83]\n"
    "ds_write_b128 v181, v[68:71] offset:2048\n"
    "v_mfma_f32_16x16x32_bf16 v[84:87], v[220:223], a[224:227], v[84:87]\n"
    "v_mfma_f32_16x16x32_bf16 v[84:87], v[216:219], a[204:207], v[84:87]\n"
    "ds_write_b128 v181, v[72:75] offset:3072\n"
    "v_mfma_f32_16x16x32_bf16 v[88:91], v[216:219], a[208:211], v[88:91]\n"
    "v_mfma_f32_16x16x32_bf16 v[88:91], v[220:223], a[228:231], v[88:91]\n"
    "v_mfma_f32_16x16x32_bf16 v[92:95], v[220:223], a[232:235], v[92:95]\n"
    "buffer_load_dwordx4 v[248:251], v1, s[28:31], s89 offen offset:2048 sc1\n"
    "v_mfma_f32_16x16x32_bf16 v[92:95], v[216:219], a[212:215], v[92:95]\n"
    "ds_write_b128 v181, v[76:79] offset:4096\n"
    "v_mfma_f32_16x16x32_bf16 v[96:99], v[216:219], a[216:219], v[96:99]\n"
    "v_mfma_f32_16x16x32_bf16 v[96:99], v[220:223], a[236:239], v[96:99]\n"
    "s_waitcnt vmcnt(6)\n"
    "v_mfma_f32_16x16x32_bf16 v[100:103], v[224:227], a[80:83], v[100:103]\n"
    "s_waitcnt vmcnt(5)\n"
    "v_mfma_f32_16x16x32_bf16 v[100:103], v[228:231], a[100:103], v[100:103]\n"
    "buffer_load_dwordx4 v[252:255], v1, s[28:31], s89 offen offset:3072 sc1\n"
    "v_mfma_f32_16x16x32_bf16 v[104:107], v[228:231], a[104:107], v[104:107]\n"
    "v_mfma_f32_16x16x32_bf16 v[104:107], v[224:227], a[84:87], v[104:107]\n"
    "s_add_u32 s88, s87, 0\n"
    "s_and_b32 s88, s88, 0x3ff\n"
    "s_add_u32 m0, s84, 0\n"
    "v_mfma_f32_16x16x32_bf16 v[108:111], v[224:227], a[88:91], v[108:111]\n"
    "s_or_b32 s88, s88, 0\n"
    "buffer_load_dwordx4 v20, s[32:35], s88 offen lds\n"
    "v_mfma_f32_16x16x32_bf16 v[108:111], v[228:231], a[108:111], v[108:111]\n"
    "ds_write_b128 v182, v[80:83] offset:0\n"
    "v_mfma_f32_16x16x32_bf16 v[112:115], v[228:231], a[112:115], v[112:115]\n"
    "ds_write_b128 v182, v[84:87] offset:1024\n"
    "v_mfma_f32_16x16x32_bf16 v[112:115], v[224:227], a[92:95], v[112:115]\n"
    "buffer_load_dwordx4 v21, s[36:39], s88 offen offset:1024 lds\n"
    "v_mfma_f32_16x16x32_bf16 v[116:119], v[224:227], a[96:99], v[116:119]\n"
    "ds_write_b128 v182, v[88:91] offset:2048\n"
    "v_mfma_f32_16x16x32_bf16 v[116:119], v[228:231], a[116:119], v[116:119]\n"
    "s_waitcnt vmcnt(7)\n"
    "v_mfma_f32_16x16x32_bf16 v[100:103], v[232:235], a[120:123], v[100:103]\n"
    "s_waitcnt vmcnt(6)\n"
    "buffer_load_dwordx4 v20, s[40:43], s88 offen offset:2048 lds\n"
    "v_mfma_f32_16x16x32_bf16 v[100:103], v[236:239], a[140:143], v[100:103]\n"
    "ds_write_b128 v182, v[92:95] offset:3072\n"
    "v_mfma_f32_16x16x32_bf16 v[104:107], v[236:239], a[144:147], v[104:107]\n"
    "ds_write_b128 v182, v[96:99] offset:4096\n"
    "v_mfma_f32_16x16x32_bf16 v[104:107], v[232:235], a[124:127], v[104:107]\n"
    "s_waitcnt lgkmcnt(0)\n"
    "s_barrier\n"
    "v_mfma_f32_16x16x32_bf16 v[108:111], v[232:235], a[128:131], v[108:111]\n"
    "buffer_load_dwordx4 v21, s[44:47], s88 offen offset:3072 lds\n"
    "v_mfma_f32_16x16x32_bf16 v[108:111], v[236:239], a[148:151], v[108:111]\n"
    "s_add_u32 s88, s87, 0\n"
    "s_and_b32 s88, s88, 0x3ff\n"
    "s_add_u32 m0, s84, 4096\n"
    "v_mfma_f32_16x16x32_bf16 v[112:115], v[236:239], a[152:155], v[112:115]\n"
    "s_or_b32 s88, s88, 128\n"
    "buffer_load_dwordx4 v20, s[32:35], s88 offen lds\n"
    "v_mfma_f32_16x16x32_bf16 v[112:115], v[232:235], a[132:135], v[112:115]\n"
    "ds_read_b128 v[120:123], v183 offset:0\n"
    "ds_read_b128 v[124:127], v183 offset:1024\n"
    "v_mfma_f32_16x16x32_bf16 v[116:119], v[232:235], a[136:139], v[116:119]\n"
    "ds_read_b128 v[128:131], v183 offset:2048\n"
    "ds_read_b128 v[132:135], v183 offset:3072\n"
    "v_mfma_f32_16x16x32_bf16 v[116:119], v[236:239], a[156:159], v[116:119]\n"
    "s_waitcnt vmcnt(8)\n"
    "buffer_load_dwordx4 v21, s[36:39], s88 offen offset:1024 lds\n"
    "v_mfma_f32_16x16x32_bf16 v[100:103], v[240:243], a[160:163], v[100:103]\n"
    "s_waitcnt vmcnt(8)\n"
    "ds_read_b128 v[136:139], v183 offset:4096\n"
    "ds_read_b128 v[140:143], v183 offset:5120\n"
    "v_mfma_f32_16x16x32_bf16 v[100:103], v[244:247], a[180:183], v[100:103]\n"
    "ds_read_b128 v[144:147], v183 offset:6144\n"
    "ds_read_b128 v[148:151], v183 offset:7168\n"
    "v_mfma_f32_16x16x32_bf16 v[104:107], v[244:247], a[184:187], v[104:107]\n"
    "buffer_load_dwordx4 v20, s[40:43], s88 offen offset:2048 lds\n"
    "v_mfma_f32_16x16x32_bf16 v[104:107], v[240:243], a[164:167], v[104:107]\n"
    "ds_read_b128 v[152:155], v183 offset:8192\n"
    "ds_read_b128 v[156:159], v183 offset:9216\n"
    "v_mfma_f32_16x16x32_bf16 v[108:111], v[240:243], a[168:171], v[108:111]\n"
    "ds_read_b128 v[160:163], v183 offset:10240\n"
    "ds_read_b128 v[164:167], v183 offset:11264\n"
    "v_mfma_f32_16x16x32_bf16 v[108:111], v[244:247], a[188:191], v[108:111]\n"
    "buffer_load_dwordx4 v21, s[44:47], s88 offen offset:3072 lds\n"
    "v_mfma_f32_16x16x32_bf16 v[112:115], v[244:247], a[192:195], v[112:115]\n"
    "ds_read_b128 v[168:171], v183 offset:12288\n"
    "ds_read_b128 v[172:175], v183 offset:13312\n"
    "v_mfma_f32_16x16x32_bf16 v[112:115], v[240:243], a[172:175], v[112:115]\n"
    "ds_read_b128 v[176:179], v183 offset:14336\n"
    "v_mfma_f32_16x16x32_bf16 v[116:119], v[240:243], a[176:179], v[116:119]\n"
    "s_cmp_eq_u32 s64, 0\n"
    "s_cbranch_scc1 L_l1_48\n"
    "v_mov_b32 v60, 0\n"
    "v_mov_b32 v61, 1\n"
    "v_mov_b32 v62, 0\n"
    "v_mov_b32 v63, 2\n"
    "v_bfe_u32 v66, v0, 5, 1\n"
    "s_mov_b32 s91, 4194304\n"
    "v_mul_lo_u32 v66, s91, v66\n"
    "v_bfe_u32 v64, v0, 4, 1\n"
    "v_lshl_add_u32 v64, v64, 18, v66\n"
    "v_and_b32 v68, 0xf0, v1\n"
    "v_add_u32 v64, v64, v68\n"
    "s_add_u32 s91, s69, 7798784\n"
    "buffer_store_dwordx4 v[60:63], v64, s[28:31], s91 offen sc1\n"
    "v_bfe_u32 v67, v0, 2, 3\n"
    "v_lshl_add_u32 v65, v67, 17, v66\n"
    "v_and_b32 v66, 48, v1\n"
    "v_add_u32 v65, v65, v66\n"
    "s_sub_u32 s91, s13, 131072\n"
    "v_mov_b32 v66, s91\n"
    "s_sub_u32 s91, s14, 262144\n"
    "v_mov_b32 v68, s91\n"
    "s_mov_b32 vcc_lo, 0xf00\n"
    "s_mov_b32 vcc_hi, 0xf00\n"
    "v_cndmask_b32 v66, v66, v68, vcc\n"
    "v_mov_b32 v68, s12\n"
    "s_mov_b32 vcc_lo, 0xf000000f\n"
    "s_mov_b32 vcc_hi, 0xf000000f\n"
    "v_cndmask_b32 v66, v66, v68, vcc\n"
    "v_add_u32 v65, v65, v66\n"
    "s_mov_b32 s91, 7798784\n"
    "buffer_store_dwordx4 v[60:63], v65, s[28:31], s91 offen sc1\n"
    "s_nop 1\n"
    "L_l1_48:\n"
    "v_mfma_f32_16x16x32_bf16 v[116:119], v[244:247], a[196:199], v[116:119]\n"
    "s_waitcnt vmcnt(9) lgkmcnt(5)\n"
    "v_add_f32 v140, v120, v140\n"
    "v_add_f32 v141, v121, v141\n"
    "v_mfma_f32_16x16x32_bf16 v[100:103], v[248:251], a[200:203], v[100:103]\n"
    "s_waitcnt vmcnt(8)\n"
    "v_add_f32 v142, v122, v142\n"
    "v_add_f32 v143, v123, v143\n"
    "v_mfma_f32_16x16x32_bf16 v[100:103], v[252:255], a[220:223], v[100:103]\n"
    "v_add_f32 v144, v124, v144\n"
    "v_add_f32 v145, v125, v145\n"
    "v_mfma_f32_16x16x32_bf16 v[104:107], v[252:255], a[224:227], v[104:107]\n"
    "v_add_f32 v146, v126, v146\n"
    "v_add_f32 v147, v127, v147\n"
    "v_mfma_f32_16x16x32_bf16 v[104:107], v[248:251], a[204:207], v[104:107]\n"
    "v_add_f32 v148, v128, v148\n"
    "v_add_f32 v149, v129, v149\n"
    "v_mfma_f32_16x16x32_bf16 v[108:111], v[248:251], a[208:211], v[108:111]\n"
    "v_add_f32 v150, v130, v150\n"
    "v_add_f32 v151, v131, v151\n"
    "v_mfma_f32_16x16x32_bf16 v[108:111], v[252:255], a[228:231], v[108:111]\n"
    "v_add_f32 v152, v132, v152\n"
    "v_add_f32 v153, v133, v153\n"
    "v_mfma_f32_16x16x32_bf16 v[112:115], v[252:255], a[232:235], v[112:115]\n"
    "v_add_f32 v154, v134, v154\n"
    "v_add_f32 v155, v135, v155\n"
    "v_mfma_f32_16x16x32_bf16 v[112:115], v[248:251], a[212:215], v[112:115]\n"
    "v_add_f32 v156, v136, v156\n"
    "v_add_f32 v157, v137, v157\n"
    "v_mfma_f32_16x16x32_bf16 v[116:119], v[248:251], a[216:219], v[116:119]\n"
    "v_add_f32 v158, v138, v158\n"
    "v_add_f32 v159, v139, v159\n"
    "v_mfma_f32_16x16x32_bf16 v[116:119], v[252:255], a[236:239], v[116:119]\n"
    "s_cmp_lg_u32 s64, 0\n"
    "s_cbranch_scc1 L_l0_49\n"
    "s_add_u32 s88, s87, 256\n"
    "s_and_b32 s88, s88, 0x3ff\n"
    "s_add_u32 m0, s84, 32768\n"
    "s_or_b32 s88, s88, 0\n"
    "buffer_load_dwordx4 v20, s[32:35], s88 offen lds\n"
    "buffer_load_dwordx4 v21, s[36:39], s88 offen offset:1024 lds\n"
    "buffer_load_dwordx4 v20, s[40:43], s88 offen offset:2048 lds\n"
    "buffer_load_dwordx4 v21, s[44:47], s88 offen offset:3072 lds\n"
    "s_add_u32 s88, s87, 256\n"
    "s_and_b32 s88, s88, 0x3ff\n"
    "s_add_u32 m0, s84, 36864\n"
    "s_or_b32 s88, s88, 128\n"
    "buffer_load_dwordx4 v20, s[32:35], s88 offen lds\n"
    "buffer_load_dwordx4 v21, s[36:39], s88 offen offset:1024 lds\n"
    "buffer_load_dwordx4 v20, s[40:43], s88 offen offset:2048 lds\n"
    "buffer_load_dwordx4 v21, s[44:47], s88 offen offset:3072 lds\n"
    "L_l0_49:\n"
    "s_waitcnt lgkmcnt(4)\n"
    "v_pk_add_f32 v[160:161], v[160:161], v[140:141]\n"
    "v_pk_add_f32 v[162:163], v[162:163], v[142:143]\n"
    "s_waitcnt lgkmcnt(3)\n"
    "v_pk_add_f32 v[164:165], v[164:165], v[144:145]\n"
    "v_pk_add_f32 v[166:167], v[166:167], v[146:147]\n"
    "s_waitcnt lgkmcnt(2)\n"
    "v_pk_add_f32 v[168:169], v[168:169], v[148:149]\n"
    "v_pk_add_f32 v[170:171], v[170:171], v[150:151]\n"
    "s_waitcnt lgkmcnt(1)\n"
    "v_pk_add_f32 v[172:173], v[172:173], v[152:153]\n"
    "v_pk_add_f32 v[174:175], v[174:175], v[154:155]\n"
    "s_waitcnt lgkmcnt(0)\n"
    "v_pk_add_f32 v[176:177], v[176:177], v[156:157]\n"
    "v_pk_add_f32 v[178:179], v[178:179], v[158:159]\n"
    "v_pk_add_f32 v[100:101], v[100:101], v[160:161]\n"
    "v_cvt_pk_bf16_f32 v8, v100, v101\n"
    "v_pk_add_f32 v[102:103], v[102:103], v[162:163]\n"
    "v_cvt_pk_bf16_f32 v9, v102, v103\n"
    "v_pk_add_f32 v[104:105], v[104:105], v[164:165]\n"
    "v_cvt_pk_bf16_f32 v10, v104, v105\n"
    "v_pk_add_f32 v[106:107], v[106:107], v[166:167]\n"
    "v_cvt_pk_bf16_f32 v11, v106, v107\n"
    "v_pk_add_f32 v[108:109], v[108:109], v[168:169]\n"
    "v_cvt_pk_bf16_f32 v12, v108, v109\n"
    "v_pk_add_f32 v[110:111], v[110:111], v[170:171]\n"
    "v_cvt_pk_bf16_f32 v13, v110, v111\n"
    "v_pk_add_f32 v[112:113], v[112:113], v[172:173]\n"
    "v_cvt_pk_bf16_f32 v14, v112, v113\n"
    "v_pk_add_f32 v[114:115], v[114:115], v[174:175]\n"
    "v_cvt_pk_bf16_f32 v15, v114, v115\n"
    "v_pk_add_f32 v[116:117], v[116:117], v[176:177]\n"
    "v_cvt_pk_bf16_f32 v16, v116, v117\n"
    "v_pk_add_f32 v[118:119], v[118:119], v[178:179]\n"
    "v_cvt_pk_bf16_f32 v17, v118, v119\n"
    "s_cmp_lg_u32 s64, 0\n"
    "s_cbranch_scc1 L_final_10\n"
    "s_mov_b32 s64, 1\n"
    "s_add_u32 s68, s68, 8\n"
    "s_add_u32 s52, s52, 4194304\n"
    "s_addc_u32 s53, s53, 0\n"
    "s_add_u32 s66, s68, 1\n"
    "s_mov_b32 s67, s66\n"
    "v_mfma_f32_16x16x32_bf16 v[40:43], v[8:11], v[4:7], 0\n"
    "v_mfma_f32_16x16x32_bf16 v[44:47], v[8:11], v[2:5], 0\n"
    "v_mfma_f32_16x16x32_bf16 v[48:51], v[12:15], v[4:7], 0\n"
    "v_mfma_f32_16x16x32_bf16 v[52:55], v[12:15], v[2:5], 0\n"
    "v_mfma_f32_16x16x32_bf16 v[56:59], v[16:19], v[4:7], 0\n"
    "v_mov_b32 v65, s66\n"
    "v_mov_b32 v67, s66\n"
    "s_nop 1\n"
    "v_pk_add_f32 v[60:61], v[40:41], v[42:43]\n"
    "v_pk_add_f32 v[62:63], v[44:45], v[46:47]\n"
    "v_pk_add_f32 v[60:61], v[60:61], v[48:49]\n"
    "v_pk_add_f32 v[62:63], v[62:63], v[50:51]\n"
    "v_pk_add_f32 v[60:61], v[60:61], v[52:53]\n"
    "v_pk_add_f32 v[62:63], v[62:63], v[54:55]\n"
    "v_pk_add_f32 v[60:61], v[60:61], v[56:57]\n"
    "v_pk_add_f32 v[62:63], v[62:63], v[58:59]\n"
    "v_pk_add_f32 v[60:61], v[60:61], v[62:63]\n"
    "v_add_f32 v64, v60, v61\n"
    "v_mov_b32 v60, v64\n"
    "s_nop 1\n"
    "v_permlane16_swap_b32 v60, v64\n"
    "v_add_f32 v64, v60, v64\n"
    "v_mov_b32 v60, v64\n"
    "s_nop 1\n"
    "v_permlane32_swap_b32 v60, v64\n"
    "v_add_f32 v64, v60, v64\n"
    "v_mul_f32 v64, 0x3c4ccccd, v64\n"
    "v_pk_add_f32 v[40:41], v[40:41], v[64:65] op_sel_hi:[1,0] neg_lo:[0,1] neg_hi:[0,1]\n"
    "v_pk_add_f32 v[42:43], v[42:43], v[64:65] op_sel_hi:[1,0] neg_lo:[0,1] neg_hi:[0,1]\n"
    "v_pk_add_f32 v[44:45], v[44:45], v[64:65] op_sel_hi:[1,0] neg_lo:[0,1] neg_hi:[0,1]\n"
    "v_pk_add_f32 v[46:47], v[46:47], v[64:65] op_sel_hi:[1,0] neg_lo:[0,1] neg_hi:[0,1]\n"
    "v_pk_add_f32 v[48:49], v[48:49], v[64:65] op_sel_hi:[1,0] neg_lo:[0,1] neg_hi:[0,1]\n"
    "v_pk_add_f32 v[50:51], v[50:51], v[64:65] op_sel_hi:[1,0] neg_lo:[0,1] neg_hi:[0,1]\n"
    "v_pk_add_f32 v[52:53], v[52:53], v[64:65] op_sel_hi:[1,0] neg_lo:[0,1] neg_hi:[0,1]\n"
    "v_pk_add_f32 v[54:55], v[54:55], v[64:65] op_sel_hi:[1,0] neg_lo:[0,1] neg_hi:[0,1]\n"
    "v_pk_add_f32 v[56:57], v[56:57], v[64:65] op_sel_hi:[1,0] neg_lo:[0,1] neg_hi:[0,1]\n"
    "v_pk_add_f32 v[58:59], v[58:59], v[64:65] op_sel_hi:[1,0] neg_lo:[0,1] neg_hi:[0,1]\n"
    "v_pk_mul_f32 v[60:61], v[40:41], v[40:41]\n"
    "v_pk_mul_f32 v[62:63], v[42:43], v[42:43]\n"
    "v_pk_fma_f32 v[60:61], v[44:45], v[44:45], v[60:61]\n"
    "v_pk_fma_f32 v[62:63], v[46:47], v[46:47], v[62:63]\n"
    "v_pk_fma_f32 v[60:61], v[48:49], v[48:49], v[60:61]\n"
    "v_pk_fma_f32 v[62:63], v[50:51], v[50:51], v[62:63]\n"
    "v_pk_fma_f32 v[60:61], v[52:53], v[52:53], v[60:61]\n"
    "v_pk_fma_f32 v[62:63], v[54:55], v[54:55], v[62:63]\n"
    "v_pk_fma_f32 v[60:61], v[56:57], v[56:57], v[60:61]\n"
    "v_pk_fma_f32 v[62:63], v[58:59], v[58:59], v[62:63]\n"
    "v_pk_add_f32 v[60:61], v[60:61], v[62:63]\n"
    "v_add_f32 v66, v60, v61\n"
    "v_mov_b32 v60, v66\n"
    "s_nop 1\n"
    "v_permlane16_swap_b32 v60, v66\n"
    "v_add_f32 v66, v60, v66\n"
    "v_mov_b32 v60, v66\n"
    "s_nop 1\n"
    "v_permlane32_swap_b32 v60, v66\n"
    "v_add_f32 v66, v60, v66\n"
    "s_mov_b64 exec, 0xffff\n"
    "buffer_store_dwordx4 v[64:67], v1, s[52:55], s69 offen sc1\n"
    "s_mov_b64 exec, -1\n"
    "s_waitcnt vmcnt(8)\n"
    "s_add_u32 s88, s87, 512\n"
    "s_and_b32 s88, s88, 0x3ff\n"
    "s_add_u32 m0, s84, 65536\n"
    "s_or_b32 s88, s88, 0\n"
    "buffer_load_dwordx4 v20, s[32:35], s88 offen lds\n"
    "buffer_load_dwordx4 v21, s[36:39], s88 offen offset:1024 lds\n"
    "buffer_load_dwordx4 v20, s[40:43], s88 offen offset:2048 lds\n"
    "buffer_load_dwordx4 v21, s[44:47], s88 offen offset:3072 lds\n"
    "ds_read_b128 a[0:3], v22 offset:0\n"
    "ds_read_b128 a[4:7], v22 offset:2048\n"
    "ds_read_b128 a[8:11], v23 offset:0\n"
    "ds_read_b128 a[12:15], v23 offset:2048\n"
    "ds_read_b128 a[16:19], v22 offset:4096\n"
    "ds_read_b128 a[20:23], v22 offset:6144\n"
    "ds_read_b128 a[24:27], v23 offset:4096\n"
    "ds_read_b128 a[28:31], v23 offset:6144\n"
    "s_waitcnt vmcnt(8) lgkmcnt(0)\n"
    "s_add_u32 s88, s87, 512\n"
    "s_and_b32 s88, s88, 0x3ff\n"
    "s_add_u32 m0, s84, 0\n"
    "s_or_b32 s88, s88, 128\n"
    "buffer_load_dwordx4 v20, s[32:35], s88 offen lds\n"
    "buffer_load_dwordx4 v21, s[36:39], s88 offen offset:1024 lds\n"
    "buffer_load_dwordx4 v20, s[40:43], s88 offen offset:2048 lds\n"
    "buffer_load_dwordx4 v21, s[44:47], s88 offen offset:3072 lds\n"
    "ds_read_b128 a[32:35], v22 offset:32768\n"
    "ds_read_b128 a[36:39], v22 offset:34816\n"
    "ds_read_b128 a[40:43], v23 offset:32768\n"
    "ds_read_b128 a[44:47], v23 offset:34816\n"
    "s_waitcnt vmcnt(8) lgkmcnt(0)\n"
    "s_add_u32 s88, s87, 768\n"
    "s_and_b32 s88, s88, 0x3ff\n"
    "s_add_u32 m0, s84, 4096\n"
    "s_or_b32 s88, s88, 0\n"
    "buffer_load_dwordx4 v20, s[32:35], s88 offen lds\n"
    "buffer_load_dwordx4 v21, s[36:39], s88 offen offset:1024 lds\n"
    "buffer_load_dwordx4 v20, s[40:43], s88 offen offset:2048 lds\n"
    "buffer_load_dwordx4 v21, s[44:47], s88 offen offset:3072 lds\n"
    "ds_read_b128 a[48:51], v22 offset:36864\n"
    "ds_read_b128 a[52:55], v22 offset:38912\n"
    "ds_read_b128 a[56:59], v23 offset:36864\n"
    "ds_read_b128 a[60:63], v23 offset:38912\n"
    "s_waitcnt vmcnt(8) lgkmcnt(0)\n"
    "s_add_u32 s88, s87, 768\n"
    "s_and_b32 s88, s88, 0x3ff\n"
    "s_add_u32 m0, s84, 32768\n"
    "s_or_b32 s88, s88, 128\n"
    "buffer_load_dwordx4 v20, s[32:35], s88 offen lds\n"
    "buffer_load_dwordx4 v21, s[36:39], s88 offen offset:1024 lds\n"
    "buffer_load_dwordx4 v20, s[40:43], s88 offen offset:2048 lds\n"
    "buffer_load_dwordx4 v21, s[44:47], s88 offen offset:3072 lds\n"
    "ds_read_b128 a[64:67], v24 offset:0\n"
    "ds_read_b128 a[68:71], v24 offset:2048\n"
    "ds_read_b128 a[72:75], v25 offset:0\n"
    "ds_read_b128 a[76:79], v25 offset:2048\n"
    "s_waitcnt vmcnt(8) lgkmcnt(0)\n"
    "s_add_u32 s88, s87, 0\n"
    "s_and_b32 s88, s88, 0x3ff\n"
    "s_add_u32 m0, s84, 36864\n"
    "s_or_b32 s88, s88, 1024\n"
    "buffer_load_dwordx4 v20, s[32:35], s88 offen lds\n"
    "buffer_load_dwordx4 v21, s[36:39], s88 offen offset:1024 lds\n"
    "buffer_load_dwordx4 v20, s[40:43], s88 offen offset:2048 lds\n"
    "buffer_load_dwordx4 v21, s[44:47], s88 offen offset:3072 lds\n"
    "ds_read_b128 a[80:83], v22 offset:0\n"
    "ds_read_b128 a[84:87], v22 offset:2048\n"
    "ds_read_b128 a[88:91], v23 offset:0\n"
    "ds_read_b128 a[92:95], v23 offset:2048\n"
    "v_lshlrev_b32 v72, 16, v8\n"
    "v_and_b32 v73, 0xffff0000, v8\n"
    "v_lshlrev_b32 v74, 16, v9\n"
    "v_and_b32 v75, 0xffff0000, v9\n"
    "v_lshlrev_b32 v76, 16, v10\n"
    "v_and_b32 v77, 0xffff0000, v10\n"
    "v_lshlrev_b32 v78, 16, v11\n"
    "v_and_b32 v79, 0xffff0000, v11\n"
    "v_lshlrev_b32 v80, 16, v12\n"
    "v_and_b32 v81, 0xffff0000, v12\n"
    "v_lshlrev_b32 v82, 16, v13\n"
    "v_and_b32 v83, 0xffff0000, v13\n"
    "v_lshlrev_b32 v84, 16, v14\n"
    "v_and_b32 v85, 0xffff0000, v14\n"
    "v_lshlrev_b32 v86, 16, v15\n"
    "v_and_b32 v87, 0xffff0000, v15\n"
    "v_lshlrev_b32 v88, 16, v16\n"
    "v_and_b32 v89, 0xffff0000, v16\n"
    "v_lshlrev_b32 v90, 16, v17\n"
    "v_and_b32 v91, 0xffff0000, v17\n"
    "v_bfe_u32 v92, v0, 2, 2\n"
    "v_lshlrev_b32 v92, 9, v92\n"
    "v_bfe_u32 v60, v0, 4, 2\n"
    "v_lshl_add_u32 v92, v60, 6, v92\n"
    "v_and_b32 v60, 3, v0\n"
    "v_lshl_add_u32 v92, v60, 4, v92\n"
    "v_and_b32 v93, 0x1f0, v30\n"
    "v_and_b32 v60, 8, v30\n"
    "v_lshl_add_u32 v93, v60, 7, v93\n"
    "buffer_load_dwordx4 v[64:67], v92, s[52:55], s70 offen sc1\n"
    "buffer_load_dwordx4 v[68:71], v92, s[52:55], s70 offen offset:256 sc1\n"
    "s_waitcnt vmcnt(8) lgkmcnt(0)\n"
    "s_add_u32 s88, s87, 0\n"
    "s_and_b32 s88, s88, 0x3ff\n"
    "s_add_u32 m0, s84, 65536\n"
    "s_or_b32 s88, s88, 1152\n"
    "buffer_load_dwordx4 v20, s[32:35], s88 offen lds\n"
    "buffer_load_dwordx4 v21, s[36:39], s88 offen offset:1024 lds\n"
    "buffer_load_dwordx4 v20, s[40:43], s88 offen offset:2048 lds\n"
    "buffer_load_dwordx4 v21, s[44:47], s88 offen offset:3072 lds\n"
    "ds_read_b128 a[96:99], v22 offset:4096\n"
    "ds_read_b128 a[100:103], v22 offset:6144\n"
    "ds_read_b128 a[104:107], v23 offset:4096\n"
    "ds_read_b128 a[108:111], v23 offset:6144\n"
    "s_waitcnt vmcnt(8) lgkmcnt(0)\n"
    "s_add_u32 s88, s87, 256\n"
    "s_and_b32 s88, s88, 0x3ff\n"
    "s_add_u32 m0, s84, 0\n"
    "s_or_b32 s88, s88, 1024\n"
    "buffer_load_dwordx4 v20, s[32:35], s88 offen lds\n"
    "buffer_load_dwordx4 v21, s[36:39], s88 offen offset:1024 lds\n"
    "buffer_load_dwordx4 v20, s[40:43], s88 offen offset:2048 lds\n"
    "buffer_load_dwordx4 v21, s[44:47], s88 offen offset:3072 lds\n"
    "ds_read_b128 a[112:115], v22 offset:32768\n"
    "ds_read_b128 a[116:119], v22 offset:34816\n"
    "ds_read_b128 a[120:123], v23 offset:32768\n"
    "ds_read_b128 a[124:127], v23 offset:34816\n"
    "s_add_u32 s92, s72, 4096\n"
    "s_waitcnt vmcnt(9)\n"
    "v_xor_b32 v60, s66, v65\n"
    "s_waitcnt vmcnt(8)\n"
    "v_xor_b32 v61, s66, v69\n"
    "v_or_b32 v60, v60, v61\n"
    "v_xor_b32 v61, s66, v67\n"
    "v_or_b32 v60, v60, v61\n"
    "v_xor_b32 v61, s66, v71\n"
    "v_or_b32 v60, v60, v61\n"
    "v_cmp_ne_u32 vcc, 0, v60\n"
    "s_nop 0\n"
    "s_cbranch_vccnz L_gspin_51\n"
    "L_gok_50:\n"
    "v_add_f32 v60, v64, v68\n"
    "v_add_f32 v61, v66, v70\n"
    "s_nop 0\n"
    "v_add_f32_dpp v60, v60, v60 row_ror:4 row_mask:0xf bank_mask:0xf\n"
    "s_nop 1\n"
    "v_add_f32_dpp v60, v60, v60 row_ror:8 row_mask:0xf bank_mask:0xf\n"
    "v_mul_f32 v60, 0x3e000000, v60\n"
    "v_sub_f32 v64, v64, v60\n"
    "v_sub_f32 v68, v68, v60\n"
    "v_mul_f32 v64, v64, v64\n"
    "v_fmac_f32 v64, v68, v68\n"
    "v_fmac_f32 v61, 0x42a00000, v64\n"
    "s_nop 0\n"
    "v_mov_b32_dpp v64, v60 quad_perm:[0,0,0,0] row_mask:0xf bank_mask:0xf\n"
    "v_mov_b32_dpp v65, v60 quad_perm:[1,1,1,1] row_mask:0xf bank_mask:0xf\n"
    "v_add_f32_dpp v61, v61, v61 row_ror:4 row_mask:0xf bank_mask:0xf\n"
    "v_mov_b32_dpp v66, v60 quad_perm:[2,2,2,2] row_mask:0xf bank_mask:0xf\n"
    "v_mov_b32_dpp v67, v60 quad_perm:[3,3,3,3] row_mask:0xf bank_mask:0xf\n"
    "v_add_f32_dpp v61, v61, v61 row_ror:8 row_mask:0xf bank_mask:0xf\n"
    "v_mov_b32 v62, 0x3727c5ac\n"
    "v_fmac_f32 v62, 0x3acccccd, v61\n"
    "v_rsq_f32 v61, v62\n"
    "v_pk_add_f32 v[72:73], v[72:73], v[64:65] neg_lo:[0,1] neg_hi:[0,1]\n"
    "v_pk_add_f32 v[74:75], v[74:75], v[66:67] neg_lo:[0,1] neg_hi:[0,1]\n"
    "v_pk_add_f32 v[76:77], v[76:77], v[64:65] neg_lo:[0,1] neg_hi:[0,1]\n"
    "v_pk_add_f32 v[78:79], v[78:79], v[66:67] neg_lo:[0,1] neg_hi:[0,1]\n"
    "v_pk_add_f32 v[80:81], v[80:81], v[64:65] neg_lo:[0,1] neg_hi:[0,1]\n"
    "v_pk_add_f32 v[82:83], v[82:83], v[66:67] neg_lo:[0,1] neg_hi:[0,1]\n"
    "v_pk_add_f32 v[84:85], v[84:85], v[64:65] neg_lo:[0,1] neg_hi:[0,1]\n"
    "v_pk_add_f32 v[86:87], v[86:87], v[66:67] neg_lo:[0,1] neg_hi:[0,1]\n"
    "v_pk_add_f32 v[88:89], v[88:89], v[64:65] neg_lo:[0,1] neg_hi:[0,1]\n"
    "v_pk_add_f32 v[90:91], v[90:91], v[66:67] neg_lo:[0,1] neg_hi:[0,1]\n"
    "v_mov_b32_dpp v68, v61 quad_perm:[0,0,0,0] row_mask:0xf bank_mask:0xf\n"
    "v_mov_b32_dpp v69, v61 quad_perm:[1,1,1,1] row_mask:0xf bank_mask:0xf\n"
    "v_mov_b32_dpp v70, v61 quad_perm:[2,2,2,2] row_mask:0xf bank_mask:0xf\n"
    "v_mov_b32_dpp v71, v61 quad_perm:[3,3,3,3] row_mask:0xf bank_mask:0xf\n"
    "v_pk_mul_f32 v[72:73], v[72:73], v[68:69]\n"
    "v_pk_mul_f32 v[74:75], v[74:75], v[70:71]\n"
    "v_cvt_pk_bf16_f32 v40, v72, v73\n"
    "v_cvt_pk_bf16_f32 v41, v74, v75\n"
    "v_pk_mul_f32 v[76:77], v[76:77], v[68:69]\n"
    "v_pk_mul_f32 v[78:79], v[78:79], v[70:71]\n"
    "v_cvt_pk_bf16_f32 v42, v76, v77\n"
    "v_cvt_pk_bf16_f32 v43, v78, v79\n"
    "s_nop 0\n"
    "v_permlane16_swap_b32 v40, v42\n"
    "v_permlane16_swap_b32 v41, v43\n"
    "buffer_store_dwordx4 v[40:43], v93, s[52:55], s72 offen sc1\n"
    "v_pk_mul_f32 v[80:81], v[80:81], v[68:69]\n"
    "v_pk_mul_f32 v[82:83], v[82:83], v[70:71]\n"
    "v_cvt_pk_bf16_f32 v44, v80, v81\n"
    "v_cvt_pk_bf16_f32 v45, v82, v83\n"
    "v_pk_mul_f32 v[84:85], v[84:85], v[68:69]\n"
    "v_pk_mul_f32 v[86:87], v[86:87], v[70:71]\n"
    "v_cvt_pk_bf16_f32 v46, v84, v85\n"
    "v_cvt_pk_bf16_f32 v47, v86, v87\n"
    "s_nop 0\n"
    "v_permlane16_swap_b32 v44, v46\n"
    "v_permlane16_swap_b32 v45, v47\n"
    "buffer_store_dwordx4 v[44:47], v93, s[52:55], s72 offen offset:2048 sc1\n"
    "v_pk_mul_f32 v[88:89], v[88:89], v[68:69]\n"
    "v_pk_mul_f32 v[90:91], v[90:91], v[70:71]\n"
    "v_cvt_pk_bf16_f32 v48, v88, v89\n"
    "v_cvt_pk_bf16_f32 v49, v90, v91\n"
    "buffer_store_dwordx2 v[48:49], v30, s[52:55], s92 offen sc1\n"
    "s_waitcnt vmcnt(8) lgkmcnt(0)\n"
    "s_add_u32 s88, s87, 256\n"
    "s_and_b32 s88, s88, 0x3ff\n"
    "s_add_u32 m0, s84, 4096\n"
    "s_or_b32 s88, s88, 1152\n"
    "buffer_load_dwordx4 v20, s[32:35], s88 offen lds\n"
    "buffer_load_dwordx4 v21, s[36:39], s88 offen offset:1024 lds\n"
    "buffer_load_dwordx4 v20, s[40:43], s88 offen offset:2048 lds\n"
    "buffer_load_dwordx4 v21, s[44:47], s88 offen offset:3072 lds\n"
    "ds_read_b128 a[128:131], v22 offset:36864\n"
    "ds_read_b128 a[132:135], v22 offset:38912\n"
    "ds_read_b128 a[136:139], v23 offset:36864\n"
    "ds_read_b128 a[140:143], v23 offset:38912\n"
    "s_waitcnt vmcnt(8) lgkmcnt(0)\n"
    "s_add_u32 s88, s87, 512\n"
    "s_and_b32 s88, s88, 0x3ff\n"
    "s_add_u32 m0, s84, 32768\n"
    "s_or_b32 s88, s88, 1024\n"
    "buffer_load_dwordx4 v20, s[32:35], s88 offen lds\n"
    "buffer_load_dwordx4 v21, s[36:39], s88 offen offset:1024 lds\n"
    "buffer_load_dwordx4 v20, s[40:43], s88 offen offset:2048 lds\n"
    "buffer_load_dwordx4 v21, s[44:47], s88 offen offset:3072 lds\n"
    "ds_read_b128 a[144:147], v24 offset:0\n"
    "ds_read_b128 a[148:151], v24 offset:2048\n"
    "ds_read_b128 a[152:155], v25 offset:0\n"
    "ds_read_b128 a[156:159], v25 offset:2048\n"
    "s_add_u32 s66, s68, 2\n"
    "s_mov_b32 s67, s66\n"
    "v_mov_b32 v36, s66\n"
    "v_mov_b32 v37, s66\n"
    "v_lshlrev_b32 v39, 3, v29\n"
    "v_add_u32 v176, 8192, v1\n"
    "v_add_u32 v177, 12288, v1\n"
    "s_waitcnt vmcnt(8) lgkmcnt(0)\n"
    "s_barrier\n"
    "s_mov_b64 exec, 0xff\n"
    "buffer_atomic_swap_x2 v[36:37], v39, s[52:55], s8 offen sc1\n"
    "s_mov_b64 exec, -1\n"
    "s_waitcnt vmcnt(8)\n"
    "s_add_u32 s88, s87, 512\n"
    "s_and_b32 s88, s88, 0x3ff\n"
    "s_add_u32 m0, s84, 36864\n"
    "s_or_b32 s88, s88, 1152\n"
    "buffer_load_dwordx4 v20, s[32:35], s88 offen lds\n"
    "buffer_load_dwordx4 v21, s[36:39], s88 offen offset:1024 lds\n"
    "buffer_load_dwordx4 v20, s[40:43], s88 offen offset:2048 lds\n"
    "buffer_load_dwordx4 v21, s[44:47], s88 offen offset:3072 lds\n"
    "ds_read_b128 a[160:163], v22 offset:0\n"
    "ds_read_b128 a[164:167], v22 offset:2048\n"
    "ds_read_b128 a[168:171], v23 offset:0\n"
    "ds_read_b128 a[172:175], v23 offset:2048\n"
    "v_mov_b64 v[40:41], 0\n"
    "s_mov_b64 exec, 0xff\n"
    "buffer_atomic_or_x2 v[40:41], v28, s[52:55], s12 offen sc0 sc1\n"
    "s_mov_b64 exec, -1\n"
    "s_waitcnt vmcnt(8) lgkmcnt(0)\n"
    "s_add_u32 s88, s87, 768\n"
    "s_and_b32 s88, s88, 0x3ff\n"
    "s_add_u32 m0, s84, 65536\n"
    "s_or_b32 s88, s88, 1024\n"
    "buffer_load_dwordx4 v20, s[32:35], s88 offen lds\n"
    "buffer_load_dwordx4 v21, s[36:39], s88 offen offset:1024 lds\n"
    "buffer_load_dwordx4 v20, s[40:43], s88 offen offset:2048 lds\n"
    "buffer_load_dwordx4 v21, s[44:47], s88 offen offset:3072 lds\n"
    "ds_read_b128 a[176:179], v22 offset:4096\n"
    "ds_read_b128 a[180:183], v22 offset:6144\n"
    "ds_read_b128 a[184:187], v23 offset:4096\n"
    "ds_read_b128 a[188:191], v23 offset:6144\n"
    "v_mov_b64 v[42:43], 0\n"
    "s_mov_b64 exec, 0xff\n"
    "buffer_atomic_or_x2 v[42:43], v28, s[52:55], s12 offen sc0 sc1\n"
    "s_mov_b64 exec, -1\n"
    "s_waitcnt vmcnt(8) lgkmcnt(0)\n"
    "s_add_u32 s88, s87, 768\n"
    "s_and_b32 s88, s88, 0x3ff\n"
    "s_add_u32 m0, s84, 0\n"
    "s_or_b32 s88, s88, 1152\n"
    "buffer_load_dwordx4 v20, s[32:35], s88 offen lds\n"
    "buffer_load_dwordx4 v21, s[36:39], s88 offen offset:1024 lds\n"
    "buffer_load_dwordx4 v20, s[40:43], s88 offen offset:2048 lds\n"
    "buffer_load_dwordx4 v21, s[44:47], s88 offen offset:3072 lds\n"
    "ds_read_b128 a[192:195], v22 offset:32768\n"
    "ds_read_b128 a[196:199], v22 offset:34816\n"
    "ds_read_b128 a[200:203], v23 offset:32768\n"
    "ds_read_b128 a[204:207], v23 offset:34816\n"
    "s_bfe_u32 s88, 0x3020100, s85\n"
    "s_lshl_b32 s94, s88, 8\n"
    "s_add_u32 s62, s94, s87\n"
    "s_and_b32 s62, s62, 0x3ff\n"
    "s_and_b32 s94, s94, 0x400\n"
    "s_or_b32 s62, s62, s94\n"
    "s_mul_i32 s62, s62, 80\n"
    "s_lshr_b32 s88, s88, 3\n"
    "s_lshl_b32 s88, s88, 10\n"
    "s_add_u32 s88, s88, s62\n"
    "s_add_u32 m0, s86, 94208\n"
    "s_add_u32 s89, s88, s60\n"
    "s_waitcnt vmcnt(9)\n"
    "v_cmp_ne_u64 vcc, s[66:67], v[40:41]\n"
    "s_nop 0\n"
    "s_and_b64 vcc, vcc, 255\n"
    "s_cbranch_scc0 L_ok_52\n"
    "v_mov_b64 v[40:41], 0\n"
    "s_mov_b64 exec, 0xff\n"
    "buffer_atomic_or_x2 v[40:41], v28, s[52:55], s12 offen sc0 sc1\n"
    "s_mov_b64 exec, -1\n"
    "s_waitcnt vmcnt(5)\n"
    "v_cmp_ne_u64 vcc, s[66:67], v[42:43]\n"
    "s_nop 0\n"
    "s_and_b64 vcc, vcc, 255\n"
    "s_cbranch_scc0 L_ok_52\n"
    "v_mov_b64 v[42:43], 0\n"
    "s_mov_b64 exec, 0xff\n"
    "buffer_atomic_or_x2 v[42:43], v28, s[52:55], s12 offen sc0 sc1\n"
    "s_mov_b64 exec, -1\n"
    "s_branch L_spin_53\n"
    "L_ok_52:\n"
    "s_waitcnt lgkmcnt(0)\n"
    "s_barrier\n"
    "buffer_load_dwordx4 v1, s[52:55], s89 offen sc1 lds\n"
    "buffer_load_dwordx4 v31, s[52:55], s89 offen offset:1024 sc1 lds\n"
    "buffer_load_dwordx4 v176, s[52:55], s89 offen offset:2048 sc1 lds\n"
    "buffer_load_dwordx4 v177, s[52:55], s89 offen offset:3072 sc1 lds\n"
    "s_barrier\n"
    "s_bfe_u32 s88, 0x7060504, s85\n"
    "s_lshl_b32 s94, s88, 8\n"
    "s_add_u32 s62, s94, s87\n"
    "s_and_b32 s62, s62, 0x3ff\n"
    "s_and_b32 s94, s94, 0x400\n"
    "s_or_b32 s62, s62, s94\n"
    "s_mul_i32 s62, s62, 80\n"
    "s_lshr_b32 s88, s88, 3\n"
    "s_lshl_b32 s88, s88, 10\n"
    "s_add_u32 s88, s88, s62\n"
    "s_add_u32 m0, s86, 110592\n"
    "s_add_u32 s89, s88, s60\n"
    "buffer_load_dwordx4 v1, s[52:55], s89 offen sc1 lds\n"
    "s_nop 7\n"
    "buffer_load_dwordx4 v31, s[52:55], s89 offen offset:1024 sc1 lds\n"
    "s_nop 7\n"
    "buffer_load_dwordx4 v176, s[52:55], s89 offen offset:2048 sc1 lds\n"
    "s_nop 7\n"
    "buffer_load_dwordx4 v177, s[52:55], s89 offen offset:3072 sc1 lds\n"
    "s_bfe_u32 s88, 0xb0a0908, s85\n"
    "s_lshl_b32 s94, s88, 8\n"
    "s_add_u32 s62, s94, s87\n"
    "s_and_b32 s62, s62, 0x3ff\n"
    "s_and_b32 s94, s94, 0x400\n"
    "s_or_b32 s62, s62, s94\n"
    "s_mul_i32 s62, s62, 80\n"
    "s_lshr_b32 s88, s88, 3\n"
    "s_lshl_b32 s88, s88, 10\n"
    "s_add_u32 s88, s88, s62\n"
    "s_add_u32 m0, s86, 126976\n"
    "s_add_u32 s89, s88, s60\n"
    "buffer_load_dwordx4 v1, s[52:55], s89 offen sc1 lds\n"
    "s_nop 7\n"
    "buffer_load_dwordx4 v31, s[52:55], s89 offen offset:1024 sc1 lds\n"
    "s_nop 7\n"
    "buffer_load_dwordx4 v176, s[52:55], s89 offen offset:2048 sc1 lds\n"
    "s_nop 7\n"
    "buffer_load_dwordx4 v177, s[52:55], s89 offen offset:3072 sc1 lds\n"
    "s_waitcnt vmcnt(20)\n"
    "ds_read_b128 a[208:211], v22 offset:36864\n"
    "ds_read_b128 a[212:215], v22 offset:38912\n"
    "ds_read_b128 a[216:219], v23 offset:36864\n"
    "ds_read_b128 a[220:223], v23 offset:38912\n"
    "s_waitcnt vmcnt(16)\n"
    "ds_read_b128 a[224:227], v24 offset:0\n"
    "ds_read_b128 a[228:231], v24 offset:2048\n"
    "ds_read_b128 a[232:235], v25 offset:0\n"
    "ds_read_b128 a[236:239], v25 offset:2048\n"
    "s_waitcnt vmcnt(12)\n"
    "ds_read_b128 a[240:243], v22 offset:0\n"
    "ds_read_b128 a[244:247], v22 offset:2048\n"
    "ds_read_b128 a[248:251], v23 offset:0\n"
    "ds_read_b128 a[252:255], v23 offset:2048\n"
    "s_waitcnt lgkmcnt(0)\n"
    "s_branch L_layer_9\n"
    "L_final_10:\n"
    "v_mfma_f32_16x16x32_bf16 v[40:43], v[8:11], v[4:7], 0\n"
    "v_mfma_f32_16x16x32_bf16 v[44:47], v[8:11], v[2:5], 0\n"
    "v_mfma_f32_16x16x32_bf16 v[48:51], v[12:15], v[4:7], 0\n"
    "v_mfma_f32_16x16x32_bf16 v[52:55], v[12:15], v[2:5], 0\n"
    "v_mfma_f32_16x16x32_bf16 v[56:59], v[16:19], v[4:7], 0\n"
    "s_nop 3\n"
    "v_cvt_pk_bf16_f32 v40, v40, v41\n"
    "v_cvt_pk_bf16_f32 v41, v42, v43\n"
    "ds_write_b64 v190, v[40:41] offset:0\n"
    "v_cvt_pk_bf16_f32 v44, v44, v45\n"
    "v_cvt_pk_bf16_f32 v45, v46, v47\n"
    "ds_write_b64 v190, v[44:45] offset:32\n"
    "v_cvt_pk_bf16_f32 v48, v48, v49\n"
    "v_cvt_pk_bf16_f32 v49, v50, v51\n"
    "ds_write_b64 v190, v[48:49] offset:64\n"
    "v_cvt_pk_bf16_f32 v52, v52, v53\n"
    "v_cvt_pk_bf16_f32 v53, v54, v55\n"
    "ds_write_b64 v190, v[52:53] offset:96\n"
    "v_cvt_pk_bf16_f32 v56, v56, v57\n"
    "v_cvt_pk_bf16_f32 v57, v58, v59\n"
    "ds_write_b64 v190, v[56:57] offset:128\n"
    "ds_read_b128 v[80:83], v183 offset:0\n"
    "ds_read_b128 v[84:87], v183 offset:1024\n"
    "ds_read_b128 v[88:91], v183 offset:2048\n"
    "s_waitcnt lgkmcnt(2)\n"
    "buffer_store_dwordx4 v[80:83], v184, s[48:51], s93 offen\n"
    "s_waitcnt lgkmcnt(1)\n"
    "buffer_store_dwordx4 v[84:87], v185, s[48:51], s93 offen\n"
    "s_waitcnt lgkmcnt(0)\n"
    "s_mov_b32 exec_lo, 0xffffffff\n"
    "s_mov_b32 exec_hi, 0x0\n"
    "buffer_store_dwordx4 v[88:91], v186, s[48:51], s93 offen\n"
    "s_mov_b64 exec, -1\n"
    "s_or_b32 s91, s63, s56\n"
    "s_cmp_lg_u32 s91, 0\n"
    "s_cbranch_scc1 L_end_11\n"
    "s_add_u32 s91, s68, 24\n"
    "v_mov_b32 v34, s91\n"
    "v_mov_b32 v35, 0\n"
    "s_mov_b64 exec, 0x1\n"
    "buffer_store_dword v34, v35, s[28:31], 0 offen sc1\n"
    "s_mov_b64 exec, -1\n"
    "L_end_11:\n"
    "s_branch L_pc1\n"
    "L_gspin_4:\n"
    "buffer_load_dwordx4 v[64:67], v92, s[52:55], s70 offen sc1\n"
    "buffer_load_dwordx4 v[68:71], v92, s[52:55], s70 offen offset:256 sc1\n"
    "s_waitcnt vmcnt(0)\n"
    "v_xor_b32 v60, s66, v65\n"
    "s_waitcnt vmcnt(0)\n"
    "v_xor_b32 v61, s66, v69\n"
    "v_or_b32 v60, v60, v61\n"
    "v_xor_b32 v61, s66, v67\n"
    "v_or_b32 v60, v60, v61\n"
    "v_xor_b32 v61, s66, v71\n"
    "v_or_b32 v60, v60, v61\n"
    "v_cmp_ne_u32 vcc, 0, v60\n"
    "s_nop 0\n"
    "s_cbranch_vccz L_gok_3\n"
    "s_cmp_eq_u32 s65, 0\n"
    "s_cbranch_scc1 L_gok_3\n"
    "s_sub_u32 s65, s65, 1\n"
    "s_branch L_gspin_4\n"
    "L_spin_8:\n"
    "s_waitcnt vmcnt(2)\n"
    "v_cmp_ne_u64 vcc, s[66:67], v[40:41]\n"
    "s_nop 0\n"
    "s_and_b64 vcc, vcc, 255\n"
    "s_cbranch_scc0 L_ok_7\n"
    "s_sleep 1\n"
    "v_mov_b64 v[40:41], 0\n"
    "s_mov_b64 exec, 0xff\n"
    "buffer_atomic_or_x2 v[40:41], v28, s[52:55], s12 offen sc0 sc1\n"
    "s_mov_b64 exec, -1\n"
    "s_waitcnt vmcnt(2)\n"
    "v_cmp_ne_u64 vcc, s[66:67], v[42:43]\n"
    "s_nop 0\n"
    "s_and_b64 vcc, vcc, 255\n"
    "s_cbranch_scc0 L_ok_7\n"
    "s_sleep 1\n"
    "v_mov_b64 v[42:43], 0\n"
    "s_mov_b64 exec, 0xff\n"
    "buffer_atomic_or_x2 v[42:43], v28, s[52:55], s12 offen sc0 sc1\n"
    "s_mov_b64 exec, -1\n"
    "s_waitcnt vmcnt(2)\n"
    "v_cmp_ne_u64 vcc, s[66:67], v[44:45]\n"
    "s_nop 0\n"
    "s_and_b64 vcc, vcc, 255\n"
    "s_cbranch_scc0 L_ok_7\n"
    "s_sleep 1\n"
    "v_mov_b64 v[44:45], 0\n"
    "s_mov_b64 exec, 0xff\n"
    "buffer_atomic_or_x2 v[44:45], v28, s[52:55], s12 offen sc0 sc1\n"
    "s_mov_b64 exec, -1\n"
    "s_sub_u32 s65, s65, 1\n"
    "s_cbranch_scc0 L_spin_8\n"
    "s_mov_b32 s65, 0\n"
    "s_waitcnt vmcnt(0)\n"
    "s_branch L_ok_7\n"
    "L_miss_17:\n"
    "v_mov_b64 v[34:35], 0\n"
    "s_mov_b64 exec, 0x1\n"
    "buffer_atomic_or_x2 v[34:35], v28, s[52:55], s92 offen sc0 sc1\n"
    "s_mov_b64 exec, -1\n"
    "L_spin_18:\n"
    "s_waitcnt vmcnt(0)\n"
    "v_cmp_ne_u64 vcc, s[66:67], v[34:35]\n"
    "s_nop 0\n"
    "s_and_b64 vcc, vcc, 1\n"
    "s_cbranch_scc0 L_ok_16\n"
    "v_mov_b64 v[34:35], 0\n"
    "s_mov_b64 exec, 0x1\n"
    "buffer_atomic_or_x2 v[34:35], v28, s[52:55], s92 offen sc0 sc1\n"
    "s_mov_b64 exec, -1\n"
    "s_sub_u32 s65, s65, 1\n"
    "s_cbranch_scc0 L_spin_18\n"
    "s_mov_b32 s65, 0\n"
    "s_waitcnt vmcnt(0)\n"
    "s_branch L_ok_16\n"
    "L_miss_22:\n"
    "v_mov_b64 v[34:35], 0\n"
    "s_mov_b64 exec, 0xff\n"
    "buffer_atomic_or_x2 v[34:35], v28, s[52:55], s92 offen sc0 sc1\n"
    "s_mov_b64 exec, -1\n"
    "L_spin_23:\n"
    "s_waitcnt vmcnt(0)\n"
    "v_cmp_ne_u64 vcc, s[66:67], v[34:35]\n"
    "s_nop 0\n"
    "s_and_b64 vcc, vcc, 255\n"
    "s_cbranch_scc0 L_ok_21\n"
    "v_mov_b64 v[34:35], 0\n"
    "s_mov_b64 exec, 0xff\n"
    "buffer_atomic_or_x2 v[34:35], v28, s[52:55], s92 offen sc0 sc1\n"
    "s_mov_b64 exec, -1\n"
    "s_sub_u32 s65, s65, 1\n"
    "s_cbranch_scc0 L_spin_23\n"
    "s_mov_b32 s65, 0\n"
    "s_waitcnt vmcnt(0)\n"
    "s_branch L_ok_21\n"
    "L_miss_27:\n"
    "v_mov_b64 v[34:35], 0\n"
    "s_mov_b64 exec, 0x1\n"
    "buffer_atomic_or_x2 v[34:35], v28, s[52:55], s92 offen sc0 sc1\n"
    "s_mov_b64 exec, -1\n"
    "L_spin_28:\n"
    "s_waitcnt vmcnt(0)\n"
    "v_cmp_ne_u64 vcc, s[66:67], v[34:35]\n"
    "s_nop 0\n"
    "s_and_b64 vcc, vcc, 1\n"
    "s_cbranch_scc0 L_ok_26\n"
    "v_mov_b64 v[34:35], 0\n"
    "s_mov_b64 exec, 0x1\n"
    "buffer_atomic_or_x2 v[34:35], v28, s[52:55], s92 offen sc0 sc1\n"
    "s_mov_b64 exec, -1\n"
    "s_sub_u32 s65, s65, 1\n"
    "s_cbranch_scc0 L_spin_28\n"
    "s_mov_b32 s65, 0\n"
    "s_waitcnt vmcnt(0)\n"
    "s_branch L_ok_26\n"
    "L_spin_32:\n"
    "s_waitcnt vmcnt(0)\n"
    "v_cmp_ne_u64 vcc, s[66:67], v[40:41]\n"
    "s_nop 0\n"
    "s_and_b64 vcc, vcc, 3\n"
    "s_cbranch_scc0 L_ok_31\n"
    "v_mov_b64 v[40:41], 0\n"
    "s_mov_b64 exec, 0x3\n"
    "buffer_atomic_or_x2 v[40:41], v28, s[52:55], s13 offen sc0 sc1\n"
    "s_mov_b64 exec, -1\n"
    "s_sub_u32 s65, s65, 1\n"
    "s_cbranch_scc0 L_spin_32\n"
    "s_mov_b32 s65, 0\n"
    "s_waitcnt vmcnt(0)\n"
    "s_branch L_ok_31\n"
    "L_spin_36:\n"
    "s_waitcnt vmcnt(0)\n"
    "v_cmp_ne_u64 vcc, s[66:67], v[40:41]\n"
    "s_nop 0\n"
    "s_and_b64 vcc, vcc, 255\n"
    "s_cbranch_scc0 L_ok_35\n"
    "v_mov_b64 v[40:41], 0\n"
    "s_mov_b64 exec, 0xff\n"
    "buffer_atomic_or_x2 v[40:41], v28, s[52:55], s14 offen sc0 sc1\n"
    "s_mov_b64 exec, -1\n"
    "s_sub_u32 s65, s65, 1\n"
    "s_cbranch_scc0 L_spin_36\n"
    "s_mov_b32 s65, 0\n"
    "s_waitcnt vmcnt(0)\n"
    "s_branch L_ok_35\n"
    "L_gspin_38:\n"
    "buffer_load_dwordx4 v[88:91], v190, s[52:55], s71 offen sc1\n"
    "buffer_load_dwordx4 v[92:95], v190, s[52:55], s71 offen offset:256 sc1\n"
    "buffer_load_dwordx4 v[96:99], v190, s[52:55], s71 offen offset:2048 sc1\n"
    "buffer_load_dwordx4 v[100:103], v190, s[52:55], s71 offen offset:2304 sc1\n"
    "s_waitcnt vmcnt(0)\n"
    "v_xor_b32 v182, s66, v89\n"
    "s_waitcnt vmcnt(2)\n"
    "v_xor_b32 v183, s66, v93\n"
    "v_or_b32 v182, v182, v183\n"
    "s_waitcnt vmcnt(1)\n"
    "v_xor_b32 v183, s66, v97\n"
    "v_or_b32 v182, v182, v183\n"
    "s_waitcnt vmcnt(0)\n"
    "v_xor_b32 v183, s66, v101\n"
    "v_or_b32 v182, v182, v183\n"
    "v_xor_b32 v183, s66, v91\n"
    "v_or_b32 v182, v182, v183\n"
    "v_xor_b32 v183, s66, v95\n"
    "v_or_b32 v182, v182, v183\n"
    "v_xor_b32 v183, s66, v99\n"
    "v_or_b32 v182, v182, v183\n"
    "v_xor_b32 v183, s66, v103\n"
    "v_or_b32 v182, v182, v183\n"
    "v_cmp_ne_u32 vcc, 0, v182\n"
    "s_nop 0\n"
    "s_cbranch_vccz L_gok_37\n"
    "s_cmp_eq_u32 s65, 0\n"
    "s_cbranch_scc1 L_gok_37\n"
    "s_sub_u32 s65, s65, 1\n"
    "s_branch L_gspin_38\n"
    "L_miss_40:\n"
    "v_mov_b64 v[34:35], 0\n"
    "s_mov_b64 exec, 0xf\n"
    "buffer_atomic_or_x2 v[34:35], v28, s[52:55], s15 offen sc0 sc1\n"
    "s_mov_b64 exec, -1\n"
    "L_spin_41:\n"
    "s_waitcnt vmcnt(0)\n"
    "v_cmp_ne_u64 vcc, s[66:67], v[34:35]\n"
    "s_nop 0\n"
    "s_and_b64 vcc, vcc, 15\n"
    "s_cbranch_scc0 L_ok_39\n"
    "v_mov_b64 v[34:35], 0\n"
    "s_mov_b64 exec, 0xf\n"
    "buffer_atomic_or_x2 v[34:35], v28, s[52:55], s15 offen sc0 sc1\n"
    "s_mov_b64 exec, -1\n"
    "s_sub_u32 s65, s65, 1\n"
    "s_cbranch_scc0 L_spin_41\n"
    "s_mov_b32 s65, 0\n"
    "s_waitcnt vmcnt(0)\n"
    "s_branch L_ok_39\n"
    "L_miss_43:\n"
    "v_mov_b64 v[34:35], 0\n"
    "s_mov_b64 exec, 0xf\n"
    "buffer_atomic_or_x2 v[34:35], v28, s[52:55], s92 offen sc0 sc1\n"
    "s_mov_b64 exec, -1\n"
    "L_spin_44:\n"
    "s_waitcnt vmcnt(0)\n"
    "v_cmp_ne_u64 vcc, s[66:67], v[34:35]\n"
    "s_nop 0\n"
    "s_and_b64 vcc, vcc, 15\n"
    "s_cbranch_scc0 L_ok_42\n"
    "v_mov_b64 v[34:35], 0\n"
    "s_mov_b64 exec, 0xf\n"
    "buffer_atomic_or_x2 v[34:35], v28, s[52:55], s92 offen sc0 sc1\n"
    "s_mov_b64 exec, -1\n"
    "s_sub_u32 s65, s65, 1\n"
    "s_cbranch_scc0 L_spin_44\n"
    "s_mov_b32 s65, 0\n"
    "s_waitcnt vmcnt(0)\n"
    "s_branch L_ok_42\n"
    "L_miss_46:\n"
    "v_mov_b64 v[34:35], 0\n"
    "s_mov_b64 exec, 0xf\n"
    "buffer_atomic_or_x2 v[34:35], v28, s[52:55], s92 offen sc0 sc1\n"
    "s_mov_b64 exec, -1\n"
    "s_sleep 4\n"
    "v_mov_b64 v[38:39], 0\n"
    "s_mov_b64 exec, 0xf\n"
    "buffer_atomic_or_x2 v[38:39], v28, s[52:55], s92 offen sc0 sc1\n"
    "s_mov_b64 exec, -1\n"
    "L_spin_47:\n"
    "s_waitcnt vmcnt(1)\n"
    "v_cmp_ne_u64 vcc, s[66:67], v[34:35]\n"
    "s_nop 0\n"
    "s_and_b64 vcc, vcc, 15\n"
    "s_cbranch_scc0 L_ok_45\n"
    "s_sleep 1\n"
    "v_mov_b64 v[34:35], 0\n"
    "s_mov_b64 exec, 0xf\n"
    "buffer_atomic_or_x2 v[34:35], v28, s[52:55], s92 offen sc0 sc1\n"
    "s_mov_b64 exec, -1\n"
    "s_waitcnt vmcnt(1)\n"
    "v_cmp_ne_u64 vcc, s[66:67], v[38:39]\n"
    "s_nop 0\n"
    "s_and_b64 vcc, vcc, 15\n"
    "s_cbranch_scc0 L_ok_45\n"
    "s_sleep 1\n"
    "v_mov_b64 v[38:39], 0\n"
    "s_mov_b64 exec, 0xf\n"
    "buffer_atomic_or_x2 v[38:39], v28, s[52:55], s92 offen sc0 sc1\n"
    "s_mov_b64 exec, -1\n"
    "s_sub_u32 s65, s65, 1\n"
    "s_cbranch_scc0 L_spin_47\n"
    "s_mov_b32 s65, 0\n"
    "s_waitcnt vmcnt(0)\n"
    "s_branch L_ok_45\n"
    "L_gspin_51:\n"
    "buffer_load_dwordx4 v[64:67], v92, s[52:55], s70 offen sc1\n"
    "buffer_load_dwordx4 v[68:71], v92, s[52:55], s70 offen offset:256 sc1\n"
    "s_waitcnt vmcnt(0)\n"
    "v_xor_b32 v60, s66, v65\n"
    "s_waitcnt vmcnt(0)\n"
    "v_xor_b32 v61, s66, v69\n"
    "v_or_b32 v60, v60, v61\n"
    "v_xor_b32 v61, s66, v67\n"
    "v_or_b32 v60, v60, v61\n"
    "v_xor_b32 v61, s66, v71\n"
    "v_or_b32 v60, v60, v61\n"
    "v_cmp_ne_u32 vcc, 0, v60\n"
    "s_nop 0\n"
    "s_cbranch_vccz L_gok_50\n"
    "s_cmp_eq_u32 s65, 0\n"
    "s_cbranch_scc1 L_gok_50\n"
    "s_sub_u32 s65, s65, 1\n"
    "s_branch L_gspin_51\n"
    "L_spin_53:\n"
    "s_waitcnt vmcnt(1)\n"
    "v_cmp_ne_u64 vcc, s[66:67], v[40:41]\n"
    "s_nop 0\n"
    "s_and_b64 vcc, vcc, 255\n"
    "s_cbranch_scc0 L_ok_52\n"
    "s_sleep 1\n"
    "v_mov_b64 v[40:41], 0\n"
    "s_mov_b64 exec, 0xff\n"
    "buffer_atomic_or_x2 v[40:41], v28, s[52:55], s12 offen sc0 sc1\n"
    "s_mov_b64 exec, -1\n"
    "s_waitcnt vmcnt(1)\n"
    "v_cmp_ne_u64 vcc, s[66:67], v[42:43]\n"
    "s_nop 0\n"
    "s_and_b64 vcc, vcc, 255\n"
    "s_cbranch_scc0 L_ok_52\n"
    "s_sleep 1\n"
    "v_mov_b64 v[42:43], 0\n"
    "s_mov_b64 exec, 0xff\n"
    "buffer_atomic_or_x2 v[42:43], v28, s[52:55], s12 offen sc0 sc1\n"
    "s_mov_b64 exec, -1\n"
    "s_sub_u32 s65, s65, 1\n"
    "s_cbranch_scc0 L_spin_53\n"
    "s_mov_b32 s65, 0\n"
    "s_waitcnt vmcnt(0)\n"
    "s_branch L_ok_52\n"
    "L_pc1:\n"
    : "+v"(tid)
    : "s"(__builtin_amdgcn_kernarg_segment_ptr()), "s"(blockIdx.x)
    : "v1", "v2", "v3", "v4", "v5", "v6", "v7", "v8", "v9", "v10", "v11", "v12", "v13", "v14", "v15", "v16", "v17", "v18", "v19", "v20", "v21", "v22", "v23", "v24", "v25", "v26", "v27", "v28", "v29", "v30", "v31", "v32", "v33", "v34", "v35", "v36", "v37", "v38", "v39", "v40", "v41", "v42", "v43", "v44", "v45", "v46", "v47", "v48", "v49", "v50", "v51", "v52", "v53", "v54", "v55", "v56", "v57", "v58", "v59", "v60", "v61", "v62", "v63", "v64", "v65", "v66", "v67", "v68", "v69", "v70", "v71", "v72", "v73", "v74", "v75", "v76", "v77", "v78", "v79", "v80", "v81", "v82", "v83", "v84", "v85", "v86", "v87", "v88", "v89", "v90", "v91", "v92", "v93", "v94", "v95", "v96", "v97", "v98", "v99", "v100", "v101", "v102", "v103", "v104", "v105", "v106", "v107", "v108", "v109", "v110", "v111", "v112", "v113", "v114", "v115", "v116", "v117", "v118", "v119", "v120", "v121", "v122", "v123", "v124", "v125", "v126", "v127", "v128", "v129", "v130", "v131", "v132", "v133", "v134", "v135", "v136", "v137", "v138", "v139", "v140", "v141", "v142", "v143", "v144", "v145", "v146", "v147", "v148", "v149", "v150", "v151", "v152", "v153", "v154", "v155", "v156", "v157", "v158", "v159", "v160", "v161", "v162", "v163", "v164", "v165", "v166", "v167", "v168", "v169", "v170", "v171", "v172", "v173", "v174", "v175", "v176", "v177", "v178", "v179", "v180", "v181", "v182", "v183", "v184", "v185", "v186", "v187", "v188", "v189", "v190", "v191", "v192", "v193", "v194", "v195", "v196", "v197", "v198", "v199", "v200", "v201", "v202", "v203", "v204", "v205", "v206", "v207", "v208", "v209", "v210", "v211", "v212", "v213", "v214", "v215", "v216", "v217", "v218", "v219", "v220", "v221", "v222", "v223", "v224", "v225", "v226", "v227", "v228", "v229", "v230", "v231", "v232", "v233", "v234", "v235", "v236", "v237", "v238", "v239", "v240", "v241", "v242", "v243", "v244", "v245", "v246", "v247", "v248", "v249", "v250", "v251", "v252", "v253", "v254", "v255", "a0", "a1", "a2", "a3", "a4", "a5", "a6", "a7", "a8", "a9", "a10", "a11", "a12", "a13", "a14", "a15", "a16", "a17", "a18", "a19", "a20", "a21", "a22", "a23", "a24", "a25", "a26", "a27", "a28", "a29", "a30", "a31", "a32", "a33", "a34", "a35", "a36", "a37", "a38", "a39", "a40", "a41", "a42", "a43", "a44", "a45", "a46", "a47", "a48", "a49", "a50", "a51", "a52", "a53", "a54", "a55", "a56", "a57", "a58", "a59", "a60", "a61", "a62", "a63", "a64", "a65", "a66", "a67", "a68", "a69", "a70", "a71", "a72", "a73", "a74", "a75", "a76", "a77", "a78", "a79", "a80", "a81", "a82", "a83", "a84", "a85", "a86", "a87", "a88", "a89", "a90", "a91", "a92", "a93", "a94", "a95", "a96", "a97", "a98", "a99", "a100", "a101", "a102", "a103", "a104", "a105", "a106", "a107", "a108", "a109", "a110", "a111", "a112", "a113", "a114", "a115", "a116", "a117", "a118", "a119", "a120", "a121", "a122", "a123", "a124", "a125", "a126", "a127", "a128", "a129", "a130", "a131", "a132", "a133", "a134", "a135", "a136", "a137", "a138", "a139", "a140", "a141", "a142", "a143", "a144", "a145", "a146", "a147", "a148", "a149", "a150", "a151", "a152", "a153", "a154", "a155", "a156", "a157", "a158", "a159", "a160", "a161", "a162", "a163", "a164", "a165", "a166", "a167", "a168", "a169", "a170", "a171", "a172", "a173", "a174", "a175", "a176", "a177", "a178", "a179", "a180", "a181", "a182", "a183", "a184", "a185", "a186", "a187", "a188", "a189", "a190", "a191", "a192", "a193", "a194", "a195", "a196", "a197", "a198", "a199", "a200", "a201", "a202", "a203", "a204", "a205", "a206", "a207", "a208", "a209", "a210", "a211", "a212", "a213", "a214", "a215", "a216", "a217", "a218", "a219", "a220", "a221", "a222", "a223", "a224", "a225", "a226", "a227", "a228", "a229", "a230", "a231", "a232", "a233", "a234", "a235", "a236", "a237", "a238", "a239", "a240", "a241", "a242", "a243", "a244", "a245", "a246", "a247", "a248", "a249", "a250", "a251", "a252", "a253", "a254", "a255", "s4", "s5", "s6", "s7", "s8", "s9", "s10", "s11", "s12", "s13", "s14", "s15", "s16", "s17", "s18", "s19", "s20", "s21", "s22", "s23", "s24", "s25", "s26", "s27", "s28", "s29", "s30", "s31", "s33", "s34", "s35", "s36", "s37", "s38", "s39", "s40", "s41", "s42", "s43", "s44", "s45", "s46", "s47", "s48", "s49", "s50", "s51", "s52", "s53", "s54", "s55", "s56", "s57", "s58", "s59", "s60", "s61", "s62", "s63", "s64", "s65", "s66", "s67", "s68", "s69", "s70", "s71", "s72", "s73", "s74", "s75", "s76", "s77", "s78", "s79", "s80", "s81", "s82", "s83", "s84", "s85", "s86", "s87", "s88", "s89", "s90", "s91", "s92", "s93", "s94", "s95", "s96", "s97", "s98", "s99", "vcc", "scc", "memory");
}

extern "C" void kernel_launch(void* const* d_in, const int* in_sizes, int n_in, void* d_out, int out_size, void* d_ws, size_t ws_size,
                              hipStream_t stream) {
  (void)hipFuncSetAttribute((const void*)fnet_glu_kernel, hipFuncAttributeMaxDynamicSharedMemorySize, 163840);
  fnet_glu_kernel<<<256, 256, 163840, stream>>>(d_in[0], d_in[1], d_in[2], d_in[3], d_in[4], d_in[6], d_in[7], d_in[8], d_out, d_ws);
}
```
